```python
import torch

def _softmax(x, axis=-1):
    return torch.softmax(x, dim=axis)
def _gelu(x):
    return torch.nn.functional.gelu(x, approximate="tanh")
def _silu(x):
    return torch.nn.functional.silu(x)
def _sigmoid(x):
    return torch.sigmoid(x)
def _layernorm(x, axis=-1, eps=1e-5):
    m = x.mean(dim=axis, keepdim=True)
    v = x.var(dim=axis, keepdim=True, unbiased=False)
    return (x - m) / torch.sqrt(v + eps)
def _scatter_add(tgt, idx, src, axis=0):
    return tgt.clone().index_add_(axis, idx, src)
def _segment_sum(src, seg, n):
    out = torch.zeros((n,) + src.shape[1:], dtype=src.dtype)
    return out.index_add_(0, seg, src)
def _pad(x, pw):
    flat = tuple(v for pair in reversed(pw) for v in pair)
    return torch.nn.functional.pad(x, flat)

kernel_name = 'gated_mix__linear_kernel_hedgehog__dense_activation_relu__prompt'

def setup_inputs():

    _w = (lambda a: (lambda t: t.to(torch.bfloat16) if t.is_floating_point() else t)(torch.as_tensor(a)))
    def _A_setup():
        B, S, D, H = 1, 2048, 512, 32
        Dh = D // H
        return {
            "x": _w((lambda *s: torch.randn(*s, dtype=torch.bfloat16) * 0.05)(B, S, D)),
            "wqkv": _w((lambda *s: torch.randn(*s, dtype=torch.bfloat16) * 0.8)(D, 3 * D)),
            "w_phi": _w((lambda *s: torch.randn(*s, dtype=torch.bfloat16) * 0.8)(H, Dh, Dh) * (Dh ** -0.5)),
            "wo": _w((lambda *s: torch.randn(*s, dtype=torch.bfloat16) * 0.2)(D, D)),
        }
    _A_in = _A_setup()

    def _B_setup():
        B, S, D, Dff = 1, 2048, 512, 1792
        return {
            "x": _w((lambda *s: torch.randn(*s, dtype=torch.bfloat16) * 0.05)(B, S, D)),
            "w1": _w((lambda *s: torch.randn(*s, dtype=torch.bfloat16) * 0.05)(D, Dff)),
            "w2": _w((lambda *s: torch.randn(*s, dtype=torch.bfloat16) * 0.05)(Dff, D)),
        }
    _B_in = _B_setup()

    return {
        "A_x": _A_in["x"],
        "A_wqkv": _A_in["wqkv"],
        "A_w_phi": _A_in["w_phi"],
        "A_wo": _A_in["wo"],
        "B_x": _B_in["x"],
        "B_w1": _B_in["w1"],
        "B_w2": _B_in["w2"],
        "gate_w": _w((lambda *s: torch.randn(*s, dtype=torch.bfloat16) * 0.05)(512) * 16.0),
    }

def reference(A_x, A_wqkv, A_w_phi, A_wo, B_x, B_w1, B_w2, gate_w):
    def _A_ref(x, wqkv, w_phi, wo):
        B, S, D = x.shape
        H = 32; Dh = D // H
        qkv = (lambda x, *d: x.permute(*d))((x @ wqkv).reshape(B, S, 3, H, Dh), 2, 0, 3, 1, 4)
        q, k, v = qkv[0], qkv[1], qkv[2]
        pq = torch.einsum("bhsd,hdf->bhsf", q, w_phi)
        pk = torch.einsum("bhsd,hdf->bhsf", k, w_phi)
        phi_q = torch.concatenate([_softmax(pq, axis=-1), _softmax(-pq, axis=-1)], axis=-1)
        phi_k = torch.concatenate([_softmax(pk, axis=-1), _softmax(-pk, axis=-1)], axis=-1)
        kv = torch.einsum("bhsf,bhsd->bhfd", phi_k, v)
        z  = torch.einsum("bhsf->bhf", phi_k)
        num = torch.einsum("bhqf,bhfd->bhqd", phi_q, kv)
        den = torch.einsum("bhqf,bhf->bhq", phi_q, z)[..., None] + 1e-6
        o = num / den
        return (lambda x, *d: x.permute(*d))(o, 0, 2, 1, 3).reshape(B, S, D) @ wo

    def _B_ref(x, w1, w2):
        h = torch.einsum("bsd,df->bsf", x, w1)
        zero = torch.zeros_like(h)
        h = torch.maximum(h, zero)
        return torch.einsum("bsf,fd->bsd", h, w2)

    x = A_x
    g = _sigmoid((x * gate_w).sum(-1, keepdims=True))
    a = _A_ref(x, A_wqkv, A_w_phi, A_wo)
    b = _B_ref(x, B_w1, B_w2)
    return g * a + (1 - g) * b
```

```cpp
#include <hip/hip_runtime.h>
#include <hip/hip_bf16.h>
typedef unsigned short u16;
__device__ __forceinline__ float bf2f(u16 b){ return __uint_as_float(((unsigned)b)<<16); }
__device__ __forceinline__ u16 f2bf(float f){ unsigned u=__float_as_uint(f); u += 0x7fffu + ((u>>16)&1u); return (u16)(u>>16); }
__global__ void gemm_naive(const u16* A, const u16* B, float* C, int M, int N, int K){
  int n = blockIdx.x*blockDim.x+threadIdx.x; int m = blockIdx.y;
  if(n>=N) return; float s=0;
  for(int k=0;k<K;k++) s += bf2f(A[(size_t)m*K+k])*bf2f(B[(size_t)k*N+n]);
  C[(size_t)m*N+n]=s;
}
__global__ void phi_k(const float* qkv, const u16* wphi, float* phiq, float* phik){
  int idx = blockIdx.x*blockDim.x+threadIdx.x;
  int s = idx>>6, which=(idx>>5)&1, h=idx&31;
  float q[16]; for(int d=0;d<16;d++) q[d]=bf2f(f2bf(qkv[(size_t)s*1536+which*512+h*16+d]));
  float p[16]; float mx=-1e30f, mn=1e30f;
  for(int f=0;f<16;f++){ float a=0; for(int d=0;d<16;d++) a+=q[d]*bf2f(wphi[h*256+d*16+f]); p[f]=a; mx=fmaxf(mx,a); mn=fminf(mn,a);}
  float sp=0,sn=0; for(int f=0;f<16;f++){ sp+=__expf(p[f]-mx); sn+=__expf(mn-p[f]); }
  float* o = (which?phik:phiq) + ((size_t)s*32+h)*32;
  for(int f=0;f<16;f++){ o[f]=bf2f(f2bf(__expf(p[f]-mx)/sp)); o[16+f]=bf2f(f2bf(__expf(mn-p[f])/sn)); }
}
__global__ void kv_k(const float* qkv, const float* phik, float* kv){
  int idx = blockIdx.x*blockDim.x+threadIdx.x; if(idx>=32*32*17) return;
  int d=idx%17, f=(idx/17)%32, h=idx/(17*32);
  float a=0; for(int s=0;s<2048;s++){ float v = d<16? bf2f(f2bf(qkv[(size_t)s*1536+1024+h*16+d])):1.f; a+=phik[((size_t)s*32+h)*32+f]*v; }
  kv[idx]=bf2f(f2bf(a));
}
__global__ void o_k(const float* phiq, const float* kv, u16* o){
  int idx = blockIdx.x*blockDim.x+threadIdx.x; int s=idx>>9, c=idx&511, h=c>>4, d=c&15;
  float num=0,den=0; for(int f=0;f<32;f++){ float p=phiq[((size_t)s*32+h)*32+f]; num+=p*kv[(h*32+f)*17+d]; den+=p*kv[(h*32+f)*17+16]; }
  o[idx]=f2bf(num/(den+1e-6f));
}
__global__ void relu_k(const float* h, u16* hb, int n){ int i=blockIdx.x*blockDim.x+threadIdx.x; if(i<n) hb[i]=f2bf(fmaxf(h[i],0.f)); }
__global__ void fin_k(const u16* x, const u16* gw, const float* a, const float* b, u16* out){
  int s=blockIdx.x; __shared__ float g;
  if(threadIdx.x==0){ float t=0; for(int k=0;k<512;k++) t+=bf2f(x[s*512+k])*bf2f(gw[k]); g=1.f/(1.f+__expf(-t)); }
  __syncthreads();
  for(int c=threadIdx.x;c<512;c+=blockDim.x) out[s*512+c]=f2bf(g*a[s*512+c]+(1.f-g)*b[s*512+c]);
}
extern "C" void kernel_launch(void* const* d_in, const int* in_sizes, int n_in, void* d_out, int out_size, void* d_ws, size_t ws_size, hipStream_t stream){
  const u16* x=(const u16*)d_in[0]; const u16* wqkv=(const u16*)d_in[1]; const u16* wphi=(const u16*)d_in[2]; const u16* wo=(const u16*)d_in[3];
  const u16* w1=(const u16*)d_in[5]; const u16* w2=(const u16*)d_in[6]; const u16* gw=(const u16*)d_in[7];
  char* ws=(char*)d_ws;
  float* qkv=(float*)ws; ws+=2048*1536*4;
  float* phiq=(float*)ws; ws+=2048*1024*4;
  float* phik=(float*)ws; ws+=2048*1024*4;
  float* kv=(float*)ws; ws+=32*32*32*4;
  u16* o=(u16*)ws; ws+=2048*512*2;
  float* h=(float*)ws; ws+=2048*1792*4;
  u16* hb=(u16*)ws; ws+=2048*1792*2;
  float* a=(float*)ws; ws+=2048*512*4;
  float* b=(float*)ws; ws+=2048*512*4;
  gemm_naive<<<dim3(6,2048),256,0,stream>>>(x,wqkv,qkv,2048,1536,512);
  phi_k<<<2048*64/256,256,0,stream>>>(qkv,wphi,phiq,phik);
  kv_k<<<(32*32*17+255)/256,256,0,stream>>>(qkv,phik,kv);
  o_k<<<2048*512/256,256,0,stream>>>(phiq,kv,o);
  gemm_naive<<<dim3(2,2048),256,0,stream>>>(o,wo,a,2048,512,512);
  gemm_naive<<<dim3(7,2048),256,0,stream>>>(x,w1,h,2048,1792,512);
  relu_k<<<2048*1792/256,256,0,stream>>>(h,hb,2048*1792);
  gemm_naive<<<dim3(2,2048),256,0,stream>>>(hb,w2,b,2048,512,1792);
  fin_k<<<2048,256,0,stream>>>(x,gw,a,b,(u16*)d_out);
}
```

```cpp
#include <hip/hip_runtime.h>
#include <hip/hip_bf16.h>
typedef unsigned short u16;
__device__ __forceinline__ float bf2f(u16 b){ return __uint_as_float(((unsigned)b)<<16); }
__device__ __forceinline__ u16 f2bf(float f){ unsigned u=__float_as_uint(f); u += 0x7fffu + ((u>>16)&1u); return (u16)(u>>16); }
__global__ void gemm_naive(const u16* A, const u16* B, float* C, int M, int N, int K){
  int n = blockIdx.x*blockDim.x+threadIdx.x; int m = blockIdx.y;
  if(n>=N) return; float s=0;
  for(int k=0;k<K;k++) s += bf2f(A[(size_t)m*K+k])*bf2f(B[(size_t)k*N+n]);
  C[(size_t)m*N+n]=s;
}
__global__ void phi_k(const float* qkv, const u16* wphi, float* phiq, float* phik){
  int idx = blockIdx.x*blockDim.x+threadIdx.x;
  int s = idx>>6, which=(idx>>5)&1, h=idx&31;
  float q[16]; for(int d=0;d<16;d++) q[d]=bf2f(f2bf(qkv[(size_t)s*1536+which*512+h*16+d]));
  float p[16]; float mx=-1e30f, mn=1e30f;
  for(int f=0;f<16;f++){ float a=0; for(int d=0;d<16;d++) a+=q[d]*bf2f(wphi[h*256+d*16+f]); p[f]=a; mx=fmaxf(mx,a); mn=fminf(mn,a);}
  float sp=0,sn=0; for(int f=0;f<16;f++){ sp+=__expf(p[f]-mx); sn+=__expf(mn-p[f]); }
  float* o = (which?phik:phiq) + ((size_t)s*32+h)*32;
  for(int f=0;f<16;f++){ o[f]=bf2f(f2bf(__expf(p[f]-mx)/sp)); o[16+f]=bf2f(f2bf(__expf(mn-p[f])/sn)); }
}
__global__ void kv_k(const float* qkv, const float* phik, float* kv){
  int idx = blockIdx.x*blockDim.x+threadIdx.x; if(idx>=32*32*17) return;
  int d=idx%17, f=(idx/17)%32, h=idx/(17*32);
  float a=0; for(int s=0;s<2048;s++){ float v = d<16? bf2f(f2bf(qkv[(size_t)s*1536+1024+h*16+d])):1.f; a+=phik[((size_t)s*32+h)*32+f]*v; }
  kv[idx]=bf2f(f2bf(a));
}
__global__ void o_k(const float* phiq, const float* kv, u16* o){
  int idx = blockIdx.x*blockDim.x+threadIdx.x; int s=idx>>9, c=idx&511, h=c>>4, d=c&15;
  float num=0,den=0; for(int f=0;f<32;f++){ float p=phiq[((size_t)s*32+h)*32+f]; num+=p*kv[(h*32+f)*17+d]; den+=p*kv[(h*32+f)*17+16]; }
  o[idx]=f2bf(num/(den+1e-6f));
}
__global__ void relu_k(const float* h, u16* hb, int n){ int i=blockIdx.x*blockDim.x+threadIdx.x; if(i<n) hb[i]=f2bf(fmaxf(h[i],0.f)); }
__global__ void fin_k(const u16* x, const u16* gw, const float* a, const float* b, u16* out){
  int s=blockIdx.x; __shared__ float g;
  if(threadIdx.x==0){ float t=0; for(int k=0;k<512;k++) t+=bf2f(x[s*512+k])*bf2f(gw[k]); g=1.f/(1.f+__expf(-t)); }
  __syncthreads();
  for(int c=threadIdx.x;c<512;c+=blockDim.x) out[s*512+c]=f2bf(g*a[s*512+c]+(1.f-g)*b[s*512+c]);
}

typedef unsigned v4u __attribute__((ext_vector_type(4)));
__global__ __launch_bounds__(256) void probe(const char* x, const char* wqkv, const char* w1, const char* w2, unsigned* sink){
  int b=blockIdx.x, t=threadIdx.x;
  unsigned long long t0=__builtin_amdgcn_s_memrealtime();
  v4u v[7];
  v[0]=*(const v4u*)(x+(size_t)b*8192+t*16); v[1]=*(const v4u*)(x+(size_t)b*8192+4096+t*16);
  v[2]=*(const v4u*)(wqkv+(size_t)b*6144+t*16); v[3]=*(const v4u*)(wqkv+(size_t)b*6144+2048+t*16);
  v[4]=*(const v4u*)(w1+(size_t)b*7168+t*16); v[5]=*(const v4u*)(w1+(size_t)b*7168+3072+t*16);
  v[6]=*(const v4u*)(w2+(size_t)b*7168+t*16); v4u v7=*(const v4u*)(w2+(size_t)b*7168+3072+t*16);
  v4u a=v7; for(int i=0;i<7;i++) a^=v[i];
  if(a.x+a.y+a.z+a.w==0x12345u) sink[t]=1;
  asm volatile("s_waitcnt vmcnt(0)":::"memory");
  __syncthreads();
  unsigned long long t1=__builtin_amdgcn_s_memrealtime();
  unsigned long long until=t1+(t1-t0)*0;
  while(__builtin_amdgcn_s_memrealtime()<until) __builtin_amdgcn_s_sleep(8);
}

extern "C" void kernel_launch(void* const* d_in, const int* in_sizes, int n_in, void* d_out, int out_size, void* d_ws, size_t ws_size, hipStream_t stream){
  const u16* x=(const u16*)d_in[0]; const u16* wqkv=(const u16*)d_in[1]; const u16* wphi=(const u16*)d_in[2]; const u16* wo=(const u16*)d_in[3];
  const u16* w1=(const u16*)d_in[5]; const u16* w2=(const u16*)d_in[6]; const u16* gw=(const u16*)d_in[7];
  char* ws=(char*)d_ws;
  float* qkv=(float*)ws; ws+=2048*1536*4;
  float* phiq=(float*)ws; ws+=2048*1024*4;
  float* phik=(float*)ws; ws+=2048*1024*4;
  float* kv=(float*)ws; ws+=32*32*32*4;
  u16* o=(u16*)ws; ws+=2048*512*2;
  float* h=(float*)ws; ws+=2048*1792*4;
  u16* hb=(u16*)ws; ws+=2048*1792*2;
  float* a=(float*)ws; ws+=2048*512*4;
  float* b=(float*)ws; ws+=2048*512*4;
  probe<<<256,256,0,stream>>>((const char*)x,(const char*)wqkv,(const char*)w1,(const char*)w2,(unsigned*)((char*)d_ws+(200<<20)));
  gemm_naive<<<dim3(6,2048),256,0,stream>>>(x,wqkv,qkv,2048,1536,512);
  phi_k<<<2048*64/256,256,0,stream>>>(qkv,wphi,phiq,phik);
  kv_k<<<(32*32*17+255)/256,256,0,stream>>>(qkv,phik,kv);
  o_k<<<2048*512/256,256,0,stream>>>(phiq,kv,o);
  gemm_naive<<<dim3(2,2048),256,0,stream>>>(o,wo,a,2048,512,512);
  gemm_naive<<<dim3(7,2048),256,0,stream>>>(x,w1,h,2048,1792,512);
  relu_k<<<2048*1792/256,256,0,stream>>>(h,hb,2048*1792);
  gemm_naive<<<dim3(2,2048),256,0,stream>>>(hb,w2,b,2048,512,1792);
  fin_k<<<2048,256,0,stream>>>(x,gw,a,b,(u16*)d_out);
}
```

```cpp
#include <hip/hip_runtime.h>

__global__ __launch_bounds__(256, 1) void gated_mix_fused(const void* x, const void* wqkv, const void* w1, const void* w2, const void* wo,
                                                          void* ws, void* out, const void* wphi, const void* gate) {
  __shared__ __attribute__((aligned(16))) char lds[163840];
  asm volatile(
      ".Lk_start_%=:\n\t"
      "s_load_dwordx2 s[4:5], s[0:1], 0\n\t"
      "s_load_dwordx2 s[8:9], s[0:1], 8\n\t"
      "s_load_dwordx2 s[12:13], s[0:1], 16\n\t"
      "s_load_dwordx2 s[16:17], s[0:1], 24\n\t"
      "s_load_dwordx2 s[20:21], s[0:1], 32\n\t"
      "s_load_dwordx2 s[24:25], s[0:1], 40\n\t"
      "s_load_dwordx2 s[28:29], s[0:1], 48\n\t"
      "s_load_dwordx2 s[36:37], s[0:1], 56\n\t"
      "s_load_dwordx2 s[38:39], s[0:1], 64\n\t"
      "s_getpc_b64 s[64:65]\n\t"
      ".Lk_pc_%=:\n\t"
      "s_lshr_b32 s66, s2, 3\n\t"
      "s_mul_i32 s66, s66, 1536\n\t"
      "v_lshrrev_b32 v1, 6, v0\n\t"
      "v_mul_u32_u24 v1, 3, v1\n\t"
      "v_and_b32 v2, 63, v0\n\t"
      "v_add_u32 v1, v1, v2\n\t"
      "v_lshlrev_b32 v1, 7, v1\n\t"
      "v_add_u32 v1, s66, v1\n\t"
      "v_min_u32 v1, .Lk_end_%=-.Lk_pc_%=-4, v1\n\t"
      "s_mov_b64 exec, 7\n\t"
      "global_load_dword v39, v1, s[64:65]\n\t"
      "s_mov_b64 exec, -1\n\t"
      "v_lshrrev_b32 v2, 6, v0\n\t"
      "v_and_b32 v0, 63, v0\n\t"
      "v_readfirstlane_b32 s40, v2\n\t"
      "s_and_b32 s67, s2, 7\n\t"
      "s_lshr_b32 s68, s2, 3\n\t"
      "s_lshr_b32 s44, s68, 3\n\t"
      "s_lshr_b32 s41, s67, 2\n\t"
      "s_lshl_b32 s41, s41, 3\n\t"
      "s_and_b32 s3, s68, 7\n\t"
      "s_add_u32 s41, s41, s3\n\t"
      "s_and_b32 s43, s67, 3\n\t"
      "s_and_b32 s69, s44, 1\n\t"
      "s_lshl_b32 s75, s43, 1\n\t"
      "s_add_u32 s42, s75, s69\n\t"
      "s_mov_b32 s46, 0\n\t"
      "s_mov_b32 s6, 2097152\n\t"
      "s_mov_b32 s7, 0x20000\n\t"
      "s_mov_b32 s10, 1572864\n\t"
      "s_mov_b32 s11, 0x20000\n\t"
      "s_mov_b32 s14, 1835008\n\t"
      "s_mov_b32 s15, 0x20000\n\t"
      "s_mov_b32 s18, 1835008\n\t"
      "s_mov_b32 s19, 0x20000\n\t"
      "s_mov_b32 s22, 524288\n\t"
      "s_mov_b32 s23, 0x20000\n\t"
      "s_mov_b32 s26, 33554432\n\t"
      "s_mov_b32 s27, 0x20000\n\t"
      "s_mov_b32 s30, 2097152\n\t"
      "s_mov_b32 s31, 0x20000\n\t"
      "s_mov_b32 s34, 1024\n\t"
      "s_mov_b32 s35, 0x20000\n\t"
      "v_lshlrev_b32 v1, 4, v0\n\t"
      "v_lshrrev_b32 v48, 3, v0\n\t"
      "v_and_b32 v49, 7, v0\n\t"
      "v_lshrrev_b32 v50, 1, v48\n\t"
      "v_xor_b32 v51, v49, v50\n\t"
      "v_lshlrev_b32 v51, 4, v51\n\t"
      "v_lshl_add_u32 v2, v48, 10, v51\n\t"
      "v_xor_b32 v51, 64, v51\n\t"
      "v_lshl_add_u32 v3, v48, 10, v51\n\t"
      "v_add_u32 v4, 14336, v2\n\t"
      "v_add_u32 v5, 21504, v3\n\t"
      "v_add_u32 v3, 7168, v3\n\t"
      "v_and_b32 v50, 1, v50\n\t"
      "v_lshlrev_b32 v50, 2, v50\n\t"
      "v_xor_b32 v50, v49, v50\n\t"
      "v_lshlrev_b32 v50, 4, v50\n\t"
      "s_lshl_b32 s69, s40, 4\n\t"
      "v_add_u32 v51, s69, v48\n\t"
      "v_mul_u32_u24 v6, 3072, v51\n\t"
      "v_add_u32 v6, v6, v50\n\t"
      "v_add_u32 v7, 23552, v6\n\t"
      "v_mul_u32_u24 v8, 3584, v51\n\t"
      "v_add_u32 v8, v8, v50\n\t"
      "v_add_u32 v9, 27648, v8\n\t"
      "s_lshl_b32 s52, s41, 17\n\t"
      "s_lshl_b32 s69, s40, 15\n\t"
      "s_add_u32 s52, s52, s69\n\t"
      "s_lshl_b32 s57, s40, 11\n\t"
      "s_lshl_b32 s58, s40, 12\n\t"
      "s_add_u32 s58, s58, 16384\n\t"
      "s_lshl_b32 s53, s42, 7\n\t"
      "s_mul_i32 s74, s43, 5\n\t"
      "s_add_u32 s74, s74, 8\n\t"
      "s_cmp_ge_u32 s2, 128\n\t"
      "s_cbranch_scc1 .Lk_B_%=\n\t"
      ".Lk_A_%=:\n\t"
      "s_add_u32 s54, s53, 1024\n\t"
      "s_add_u32 s55, s53, 2048\n\t"
      "s_add_u32 s60, s46, 0\n\t"
      "s_and_b32 s60, s60, 7\n\t"
      "s_lshl_b32 s61, s60, 7\n\t"
      "s_add_u32 m0, s58, 0\n\t"
      "s_add_u32 s61, s61, s52\n\t"
      "s_waitcnt lgkmcnt(0)\n\t"
      "buffer_load_dwordx4 v2, s[4:7], s61 offen lds\n\t"
      "buffer_load_dwordx4 v3, s[4:7], s61 offen offset:1024 lds\n\t"
      "buffer_load_dwordx4 v4, s[4:7], s61 offen offset:2048 lds\n\t"
      "buffer_load_dwordx4 v5, s[4:7], s61 offen offset:3072 lds\n\t"
      "s_add_u32 s60, s46, 0\n\t"
      "s_and_b32 s60, s60, 7\n\t"
      "s_mul_i32 s61, s60, 196608\n\t"
      "s_add_u32 m0, s57, 0\n\t"
      "s_add_u32 s61, s61, s54\n\t"
      "buffer_load_dwordx4 v6, s[8:11], s61 offen lds\n\t"
      "buffer_load_dwordx4 v7, s[8:11], s61 offen offset:1024 lds\n\t"
      "s_add_u32 s60, s46, 0\n\t"
      "s_and_b32 s60, s60, 7\n\t"
      "s_mul_i32 s61, s60, 196608\n\t"
      "s_add_u32 m0, s57, 8192\n\t"
      "s_add_u32 s61, s61, s55\n\t"
      "buffer_load_dwordx4 v6, s[8:11], s61 offen lds\n\t"
      "buffer_load_dwordx4 v7, s[8:11], s61 offen offset:1024 lds\n\t"
      "s_add_u32 s64, s42, 0\n\t"
      "s_lshl_b32 s64, s64, 13\n\t"
      "s_lshl_b32 s65, s41, 17\n\t"
      "s_add_u32 s64, s64, s65\n\t"
      "s_lshl_b32 s65, s40, 11\n\t"
      "s_add_u32 s64, s64, s65\n\t"
      "s_add_u32 s64, s64, 0\n\t"
      "v_and_b32 v48, 1, v0\n\t"
      "v_lshlrev_b32 v48, 3, v48\n\t"
      "v_lshrrev_b32 v49, 1, v0\n\t"
      "v_add_u32 v48, v48, v49\n\t"
      "v_lshlrev_b32 v48, 7, v48\n\t"
      "s_mov_b64 exec, 255\n\t"
      "buffer_load_dword v39, v48, s[4:7], s64 offen\n\t"
      "s_mov_b64 exec, 0xffff\n\t"
      "s_lshl_b32 s64, s41, 4\n\t"
      "s_lshr_b32 s65, s40, 1\n\t"
      "s_lshl_b32 s65, s65, 8\n\t"
      "s_add_u32 s64, s64, s65\n\t"
      "v_add_u32 v48, s64, v0\n\t"
      "s_and_b32 s65, s40, 1\n\t"
      "s_cmp_lg_u32 s65, 0\n\t"
      "s_cbranch_scc1 .Lskip_1_%=\n\t"
      "v_mul_u32_u24 v49, 3072, v48\n\t"
      "buffer_load_dword v39, v49, s[8:11], s54 offen\n\t"
      "s_nop 0\n\t"
      ".Lskip_1_%=:\n\t"
      "s_and_b32 s65, s40, 1\n\t"
      "s_cmp_lg_u32 s65, 1\n\t"
      "s_cbranch_scc1 .Lskip_2_%=\n\t"
      "v_mul_u32_u24 v49, 3072, v48\n\t"
      "buffer_load_dword v39, v49, s[8:11], s55 offen\n\t"
      "s_nop 0\n\t"
      ".Lskip_2_%=:\n\t"
      "s_mov_b64 exec, -1\n\t"
      "s_lshl_b32 s67, s42, 11\n\t"
      "s_lshl_b32 s69, s40, 10\n\t"
      "s_add_u32 s67, s67, s69\n\t"
      "s_add_u32 s36, s36, s67\n\t"
      "s_addc_u32 s37, s37, 0\n\t"
      "s_cmp_lt_u32 s40, 2\n\t"
      "s_cselect_b32 s32, s36, s38\n\t"
      "s_cselect_b32 s33, s37, s39\n\t"
      "s_add_u32 m0, s69, 131072\n\t"
      "s_mov_b32 s67, 0\n\t"
      "buffer_load_dwordx4 v1, s[32:35], s67 offen lds\n\t"
      "buffer_load_dwordx2 v[58:59], off, s[24:27], 0 sc1\n\t"
      "s_and_b32 s45, s40, 1\n\t"
      "s_lshl_b32 s59, s40, 13\n\t"
      "v_and_b32 v48, 31, v0\n\t"
      "v_lshrrev_b32 v49, 5, v0\n\t"
      "v_bfe_u32 v50, v0, 1, 3\n\t"
      "v_xor_b32 v50, v50, v49\n\t"
      "v_lshlrev_b32 v50, 4, v50\n\t"
      "v_lshlrev_b32 v51, 7, v48\n\t"
      "v_add_u32 v51, s58, v51\n\t"
      "v_xor_b32 v52, 0, v50\n\t"
      "v_add_u32 v240, v51, v52\n\t"
      "v_xor_b32 v244, 4096, v240\n\t"
      "v_add_u32 v248, 65536, v240\n\t"
      "v_add_u32 v252, 65536, v244\n\t"
      "v_xor_b32 v52, 32, v50\n\t"
      "v_add_u32 v241, v51, v52\n\t"
      "v_xor_b32 v245, 4096, v241\n\t"
      "v_add_u32 v249, 65536, v241\n\t"
      "v_add_u32 v253, 65536, v245\n\t"
      "v_xor_b32 v52, 64, v50\n\t"
      "v_add_u32 v242, v51, v52\n\t"
      "v_xor_b32 v246, 4096, v242\n\t"
      "v_add_u32 v250, 65536, v242\n\t"
      "v_add_u32 v254, 65536, v246\n\t"
      "v_xor_b32 v52, 96, v50\n\t"
      "v_add_u32 v243, v51, v52\n\t"
      "v_xor_b32 v247, 4096, v243\n\t"
      "v_add_u32 v251, 65536, v243\n\t"
      "v_add_u32 v255, 65536, v247\n\t"
      "v_lshlrev_b32 v35, 10, v48\n\t"
      "v_lshl_add_u32 v35, v49, 4, v35\n\t"
      "v_lshlrev_b32 v200, 5, v48\n\t"
      "v_lshl_add_u32 v200, v49, 4, v200\n\t"
      "v_bfe_u32 v50, v0, 4, 1\n\t"
      "v_bfe_u32 v51, v0, 2, 2\n\t"
      "v_bfe_u32 v52, v0, 1, 1\n\t"
      "v_lshl_add_u32 v52, v50, 1, v52\n\t"
      "v_lshrrev_b32 v53, 1, v51\n\t"
      "v_lshlrev_b32 v53, 2, v53\n\t"
      "v_xor_b32 v53, v52, v53\n\t"
      "v_and_b32 v54, 1, v0\n\t"
      "v_lshlrev_b32 v54, 3, v54\n\t"
      "v_lshl_add_u32 v53, v53, 4, v54\n\t"
      "v_lshl_add_u32 v53, v51, 7, v53\n\t"
      "v_lshl_add_u32 v53, v49, 10, v53\n\t"
      "s_lshl_b32 s69, s45, 6\n\t"
      "v_xor_b32 v26, s69, v53\n\t"
      "v_add_u32 v27, 65536, v26\n\t"
      "v_lshlrev_b32 v55, 2, v51\n\t"
      "v_xor_b32 v55, v55, v52\n\t"
      "v_lshl_add_u32 v55, v55, 4, v54\n\t"
      "v_lshl_add_u32 v55, v51, 8, v55\n\t"
      "v_lshl_add_u32 v55, v49, 10, v55\n\t"
      "s_add_u32 s69, s43, 0\n\t"
      "s_and_b32 s69, s69, 3\n\t"
      "s_lshl_b32 s69, s69, 6\n\t"
      "v_xor_b32 v14, s69, v55\n\t"
      "v_add_u32 v18, 65536, v14\n\t"
      "v_add_u32 v22, 131072, v14\n\t"
      "s_add_u32 s69, s43, 1\n\t"
      "s_and_b32 s69, s69, 3\n\t"
      "s_lshl_b32 s69, s69, 6\n\t"
      "v_xor_b32 v15, s69, v55\n\t"
      "v_add_u32 v19, 65536, v15\n\t"
      "v_add_u32 v23, 131072, v15\n\t"
      "s_add_u32 s69, s43, 2\n\t"
      "s_and_b32 s69, s69, 3\n\t"
      "s_lshl_b32 s69, s69, 6\n\t"
      "v_xor_b32 v16, s69, v55\n\t"
      "v_add_u32 v20, 65536, v16\n\t"
      "v_add_u32 v24, 131072, v16\n\t"
      "s_add_u32 s69, s43, 3\n\t"
      "s_and_b32 s69, s69, 3\n\t"
      "s_lshl_b32 s69, s69, 6\n\t"
      "v_xor_b32 v17, s69, v55\n\t"
      "v_add_u32 v21, 65536, v17\n\t"
      "v_add_u32 v25, 131072, v17\n\t"
      "v_lshrrev_b32 v55, 4, v0\n\t"
      "v_lshlrev_b32 v29, 2, v55\n\t"
      "v_xor_b32 v29, v29, v0\n\t"
      "v_and_b32 v29, 15, v29\n\t"
      "v_lshlrev_b32 v29, 4, v29\n\t"
      "v_lshl_add_u32 v29, v55, 10, v29\n\t"
      "v_and_b32 v54, 3, v0\n\t"
      "v_lshlrev_b32 v54, 3, v54\n\t"
      "v_lshl_add_u32 v54, v50, 5, v54\n\t"
      "v_lshl_add_u32 v54, v51, 6, v54\n\t"
      "v_lshl_add_u32 v53, v49, 9, v54\n\t"
      "v_lshl_add_u32 v57, v49, 2, v51\n\t"
      "v_lshlrev_b32 v57, 5, v57\n\t"
      "v_lshl_add_u32 v57, v52, 3, v57\n\t"
      "s_lshl_b32 s69, s45, 10\n\t"
      "s_add_u32 s69, s69, 131072\n\t"
      "v_add_u32 v57, s69, v57\n\t"
      "v_bfe_u32 v56, v0, 2, 1\n\t"
      "v_mul_u32_u24 v56, 0x8000, v56\n\t"
      "v_lshl_or_b32 v56, v56, 16, v56\n\t"
      "v_lshrrev_b32 v36, 1, v0\n\t"
      "v_and_b32 v38, 1, v36\n\t"
      "v_add_u32 v38, s75, v38\n\t"
      "v_cmp_gt_u32 vcc, 2, v36\n\t"
      "s_nop 1\n\t"
      "v_cndmask_b32_e64 v37, 0, 8, vcc\n\t"
      "v_add_u32 v38, v38, v37\n\t"
      "v_add_u32 v37, 4, v36\n\t"
      "v_cmp_gt_u32 vcc, 4, v36\n\t"
      "s_nop 1\n\t"
      "v_cndmask_b32 v38, v37, v38, vcc\n\t"
      "s_and_b32 s69, s40, 2\n\t"
      "v_and_b32 v36, 1, v0\n\t"
      "v_add_u32 v36, s69, v36\n\t"
      "v_lshl_add_u32 v38, v38, 2, v36\n\t"
      "s_lshl_b32 s68, s41, 13\n\t"
      "s_add_u32 s68, s68, 3145728\n\t"
      "v_lshlrev_b32 v38, 7, v38\n\t"
      "v_add_u32 v38, s68, v38\n\t"
      "s_lshr_b32 s69, s44, 1\n\t"
      "s_lshl_b32 s69, s69, 3\n\t"
      "s_and_b32 s67, s44, 1\n\t"
      "s_add_u32 s69, s69, s67\n\t"
      "s_lshl_b32 s69, s69, 2\n\t"
      "s_add_u32 s69, s69, s40\n\t"
      "s_lshl_b32 s69, s69, 7\n\t"
      "s_add_u32 s69, s69, s68\n\t"
      "s_lshl_b32 s47, s43, 10\n\t"
      "s_add_u32 s47, s47, s69\n\t"
      "s_add_u32 s67, s43, 3\n\t"
      "v_sub_u32 v34, s67, v0\n\t"
      "v_and_b32 v34, 3, v34\n\t"
      "v_lshlrev_b32 v34, 10, v34\n\t"
      "v_add_u32 v34, s69, v34\n\t"
      "s_lshl_b32 s67, s41, 2\n\t"
      "s_add_u32 s66, s67, s40\n\t"
      "s_add_u32 s67, s67, s44\n\t"
      "s_mul_i32 s67, s67, 98304\n\t"
      "s_lshl_b32 s68, s40, 11\n\t"
      "s_add_u32 s67, s67, s68\n\t"
      "s_add_u32 s67, s67, 16777216\n\t"
      "s_mul_i32 s81, s43, 24576\n\t"
      "s_add_u32 s81, s81, s67\n\t"
      "s_add_u32 s69, s43, 1\n\t"
      "s_and_b32 s69, s69, 3\n\t"
      "s_mul_i32 s69, s69, 24576\n\t"
      "s_add_u32 s69, s69, s67\n\t"
      "s_add_u32 s32, s69, 0\n\t"
      "s_add_u32 s69, s43, 2\n\t"
      "s_and_b32 s69, s69, 3\n\t"
      "s_mul_i32 s69, s69, 24576\n\t"
      "s_add_u32 s69, s69, s67\n\t"
      "s_add_u32 s33, s69, 8192\n\t"
      "s_add_u32 s69, s43, 3\n\t"
      "s_and_b32 s69, s69, 3\n\t"
      "s_mul_i32 s69, s69, 24576\n\t"
      "s_add_u32 s69, s69, s67\n\t"
      "s_add_u32 s34, s69, 16384\n\t"
      "s_lshl_b32 s69, s44, 8\n\t"
      "s_add_u32 s80, s52, s69\n\t"
      "s_lshl_b32 s68, s40, 14\n\t"
      "s_add_u32 s73, s69, s68\n\t"
      "s_lshl_b32 s69, s43, 6\n\t"
      "s_add_u32 s80, s80, s69\n\t"
      "s_lshl_b32 s35, s66, 10\n\t"
      "s_add_u32 s35, s35, 196608\n\t"
      "s_lshl_b32 s68, s45, 11\n\t"
      "s_add_u32 s68, s68, 4194304\n\t"
      "s_mul_i32 s70, s66, 147456\n\t"
      "s_add_u32 s72, s70, 4194304\n\t"
      "s_add_u32 s70, s70, s68\n\t"
      "s_xor_b32 s66, s66, 1\n\t"
      "s_mul_i32 s71, s66, 147456\n\t"
      "s_add_u32 s71, s71, s68\n\t"
      "s_mov_b32 s39, 0x3fb8aa3b\n\t"
      "s_mov_b32 s38, 0xbfb8aa3b\n\t"
      "s_mov_b32 s50, 0x1ffff\n\t"
      "s_mov_b32 s51, 0x1ffff\n\t"
      "s_lshl_b32 s64, s40, 11\n\t"
      "s_lshl_b32 s65, s45, 14\n\t"
      "s_add_u32 s65, s64, s65\n\t"
      "s_add_u32 s65, s65, 131072\n\t"
      "v_and_b32 v48, 31, v0\n\t"
      "v_lshlrev_b32 v48, 6, v48\n\t"
      "v_add_u32 v48, s65, v48\n\t"
      "v_bfe_u32 v50, v0, 1, 2\n\t"
      "v_lshlrev_b32 v55, 1, v49\n\t"
      "v_xor_b32 v50, v50, v55\n\t"
      "v_lshl_add_u32 v10, v50, 4, v48\n\t"
      "v_xor_b32 v11, 16, v10\n\t"
      "v_xor_b32 v12, 2048, v10\n\t"
      "v_xor_b32 v13, 2048, v11\n\t"
      "s_lshl_b32 s65, s45, 13\n\t"
      "s_add_u32 s65, s65, s64\n\t"
      "s_add_u32 s65, s65, 65536\n\t"
      "v_add_u32 v192, s65, v1\n\t"
      "v_xor_b32 v193, 2048, v192\n\t"
      "v_lshl_add_u32 v50, v49, 2, v51\n\t"
      "v_lshrrev_b32 v55, 1, v50\n\t"
      "v_xor_b32 v55, v52, v55\n\t"
      "v_lshlrev_b32 v50, 6, v50\n\t"
      "v_lshl_add_u32 v50, v55, 4, v50\n\t"
      "v_and_b32 v55, 1, v0\n\t"
      "v_lshl_add_u32 v50, v55, 3, v50\n\t"
      "s_add_u32 s65, s59, 131072\n\t"
      "v_add_u32 v194, s65, v50\n\t"
      "v_and_b32 v48, 15, v0\n\t"
      "v_lshl_add_u32 v50, v49, 5, v48\n\t"
      "s_lshl_b32 s64, s45, 8\n\t"
      "s_lshr_b32 s65, s40, 1\n\t"
      "s_lshl_b32 s65, s65, 13\n\t"
      "s_add_u32 s64, s64, s65\n\t"
      "s_add_u32 s64, s64, 65536\n\t"
      "v_lshlrev_b32 v50, 4, v50\n\t"
      "v_add_u32 v195, s64, v50\n\t"
      "v_mov_b32 v196, 0x3f803f80\n\t"
      "s_mov_b32 s82, 0xffff\n\t"
      "s_mov_b32 s83, 0xffff\n\t"
      "s_lshl_b32 s64, s42, 2\n\t"
      "s_add_u32 s64, s64, s40\n\t"
      "s_lshl_b32 s64, s64, 6\n\t"
      "s_add_u32 s64, s64, s41\n\t"
      "s_lshl_b32 s64, s64, 10\n\t"
      "s_add_u32 s84, s64, 1048576\n\t"
      "s_lshl_b32 s64, s42, 4\n\t"
      "s_add_u32 s64, s64, s41\n\t"
      "s_lshl_b32 s37, s64, 10\n\t"
      "s_add_u32 s37, s37, 786432\n\t"
      "s_lshl_b32 s36, s64, 14\n\t"
      "s_add_u32 s36, s36, 1048576\n\t"
      "s_cmp_lt_u32 s40, 2\n\t"
      "s_cselect_b32 s68, 15, 0\n\t"
      "s_cselect_b32 s69, 0, 16\n\t"
      "v_and_b32 v48, s68, v48\n\t"
      "v_or_b32 v48, s69, v48\n\t"
      "s_lshl_b32 s68, s45, 5\n\t"
      "v_add_u32 v48, s68, v48\n\t"
      "v_lshlrev_b32 v197, 4, v48\n\t"
      "v_lshrrev_b32 v48, 4, v0\n\t"
      "v_lshl_add_u32 v198, v48, 10, v197\n\t"
      "v_add_u32 v199, 8192, v198\n\t"
      "s_mov_b32 s85, 0x3f80\n\t"
      "s_mov_b32 s88, 0x3f800000\n\t"
      "s_lshl_b32 s89, s42, 12\n\t"
      "s_sleep 4\n\t"
      "s_add_u32 s60, s46, 1\n\t"
      "s_and_b32 s60, s60, 7\n\t"
      "s_lshl_b32 s61, s60, 7\n\t"
      "s_add_u32 m0, s58, 32768\n\t"
      "s_add_u32 s61, s61, s52\n\t"
      "buffer_load_dwordx4 v2, s[4:7], s61 offen lds\n\t"
      "buffer_load_dwordx4 v3, s[4:7], s61 offen offset:1024 lds\n\t"
      "buffer_load_dwordx4 v4, s[4:7], s61 offen offset:2048 lds\n\t"
      "buffer_load_dwordx4 v5, s[4:7], s61 offen offset:3072 lds\n\t"
      "s_add_u32 s60, s46, 1\n\t"
      "s_and_b32 s60, s60, 7\n\t"
      "s_mul_i32 s61, s60, 196608\n\t"
      "s_add_u32 m0, s57, 32768\n\t"
      "s_add_u32 s61, s61, s54\n\t"
      "buffer_load_dwordx4 v6, s[8:11], s61 offen lds\n\t"
      "buffer_load_dwordx4 v7, s[8:11], s61 offen offset:1024 lds\n\t"
      "s_add_u32 s60, s46, 1\n\t"
      "s_and_b32 s60, s60, 7\n\t"
      "s_mul_i32 s61, s60, 196608\n\t"
      "s_add_u32 m0, s57, 40960\n\t"
      "s_add_u32 s61, s61, s55\n\t"
      "buffer_load_dwordx4 v6, s[8:11], s61 offen lds\n\t"
      "buffer_load_dwordx4 v7, s[8:11], s61 offen offset:1024 lds\n\t"
      "s_waitcnt vmcnt(11)\n\t"
      "s_barrier\n\t"
      "ds_read_b128 a[0:3], v240 offset:0\n\t"
      "ds_read_b128 a[128:131], v244 offset:0\n\t"
      "ds_read_b64_tr_b16 v[64:65], v26 offset:0\n\t"
      "ds_read_b64_tr_b16 v[66:67], v26 offset:512\n\t"
      "ds_read_b64_tr_b16 v[68:69], v26 offset:8192\n\t"
      "ds_read_b64_tr_b16 v[70:71], v26 offset:8704\n\t"
      "ds_read_b128 a[4:7], v241 offset:0\n\t"
      "ds_read_b128 a[132:135], v245 offset:0\n\t"
      "ds_read_b64_tr_b16 v[72:73], v26 offset:2048\n\t"
      "ds_read_b64_tr_b16 v[74:75], v26 offset:2560\n\t"
      "ds_read_b64_tr_b16 v[76:77], v26 offset:10240\n\t"
      "ds_read_b64_tr_b16 v[78:79], v26 offset:10752\n\t"
      "ds_read_b128 a[8:11], v242 offset:0\n\t"
      "ds_read_b128 a[136:139], v246 offset:0\n\t"
      "ds_read_b64_tr_b16 v[80:81], v26 offset:4096\n\t"
      "ds_read_b64_tr_b16 v[82:83], v26 offset:4608\n\t"
      "s_waitcnt lgkmcnt(12)\n\t"
      "v_mfma_f32_32x32x16_bf16 v[128:143], v[64:67], a[0:3], 0\n\t"
      "s_add_u32 s60, s46, 2\n\t"
      "s_and_b32 s60, s60, 7\n\t"
      "s_lshl_b32 s61, s60, 7\n\t"
      "s_add_u32 m0, s58, 65536\n\t"
      "s_add_u32 s61, s61, s52\n\t"
      "buffer_load_dwordx4 v2, s[4:7], s61 offen lds\n\t"
      "v_mfma_f32_32x32x16_bf16 v[144:159], v[64:67], a[128:131], 0\n\t"
      "buffer_load_dwordx4 v3, s[4:7], s61 offen offset:1024 lds\n\t"
      "ds_read_b64_tr_b16 v[84:85], v26 offset:12288\n\t"
      "ds_read_b64_tr_b16 v[86:87], v26 offset:12800\n\t"
      "s_waitcnt lgkmcnt(12)\n\t"
      "v_mfma_f32_32x32x16_bf16 v[160:175], a[0:3], v[68:71], 0\n\t"
      "buffer_load_dwordx4 v4, s[4:7], s61 offen offset:2048 lds\n\t"
      "v_mfma_f32_32x32x16_bf16 v[176:191], a[128:131], v[68:71], 0\n\t"
      "buffer_load_dwordx4 v5, s[4:7], s61 offen offset:3072 lds\n\t"
      "ds_read_b128 a[12:15], v243 offset:0\n\t"
      "ds_read_b128 a[140:143], v247 offset:0\n\t"
      "ds_read_b64_tr_b16 v[88:89], v26 offset:6144\n\t"
      "ds_read_b64_tr_b16 v[90:91], v26 offset:6656\n\t"
      "s_waitcnt lgkmcnt(12)\n\t"
      "v_mfma_f32_32x32x16_bf16 v[128:143], v[72:75], a[4:7], v[128:143]\n\t"
      "s_add_u32 s60, s46, 2\n\t"
      "s_and_b32 s60, s60, 7\n\t"
      "s_mul_i32 s61, s60, 196608\n\t"
      "s_add_u32 m0, s57, 65536\n\t"
      "s_add_u32 s61, s61, s54\n\t"
      "buffer_load_dwordx4 v6, s[8:11], s61 offen lds\n\t"
      "v_mfma_f32_32x32x16_bf16 v[144:159], v[72:75], a[132:135], v[144:159]\n\t"
      "buffer_load_dwordx4 v7, s[8:11], s61 offen offset:1024 lds\n\t"
      "ds_read_b64_tr_b16 v[92:93], v26 offset:14336\n\t"
      "ds_read_b64_tr_b16 v[94:95], v26 offset:14848\n\t"
      "s_waitcnt lgkmcnt(12)\n\t"
      "v_mfma_f32_32x32x16_bf16 v[160:175], a[4:7], v[76:79], v[160:175]\n\t"
      "s_add_u32 s60, s46, 2\n\t"
      "s_and_b32 s60, s60, 7\n\t"
      "s_mul_i32 s61, s60, 196608\n\t"
      "s_add_u32 m0, s57, 73728\n\t"
      "s_add_u32 s61, s61, s55\n\t"
      "buffer_load_dwordx4 v6, s[8:11], s61 offen lds\n\t"
      "v_mfma_f32_32x32x16_bf16 v[176:191], a[132:135], v[76:79], v[176:191]\n\t"
      "buffer_load_dwordx4 v7, s[8:11], s61 offen offset:1024 lds\n\t"
      "s_waitcnt lgkmcnt(8)\n\t"
      "v_mfma_f32_32x32x16_bf16 v[128:143], v[80:83], a[8:11], v[128:143]\n\t"
      "s_add_u32 s60, s46, 3\n\t"
      "s_and_b32 s60, s60, 7\n\t"
      "s_lshl_b32 s61, s60, 7\n\t"
      "s_add_u32 m0, s58, 98304\n\t"
      "s_add_u32 s61, s61, s52\n\t"
      "buffer_load_dwordx4 v2, s[4:7], s61 offen lds\n\t"
      "v_mfma_f32_32x32x16_bf16 v[144:159], v[80:83], a[136:139], v[144:159]\n\t"
      "buffer_load_dwordx4 v3, s[4:7], s61 offen offset:1024 lds\n\t"
      "s_waitcnt vmcnt(10)\n\t"
      "s_waitcnt lgkmcnt(0)\n\t"
      "s_barrier\n\t"
      "ds_read_b128 a[16:19], v240 offset:32768\n\t"
      "ds_read_b128 a[144:147], v244 offset:32768\n\t"
      "ds_read_b64_tr_b16 v[96:97], v26 offset:32768\n\t"
      "ds_read_b64_tr_b16 v[98:99], v26 offset:33280\n\t"
      "ds_read_b64_tr_b16 v[100:101], v26 offset:40960\n\t"
      "ds_read_b64_tr_b16 v[102:103], v26 offset:41472\n\t"
      "v_mfma_f32_32x32x16_bf16 v[160:175], a[8:11], v[84:87], v[160:175]\n\t"
      "buffer_load_dwordx4 v4, s[4:7], s61 offen offset:2048 lds\n\t"
      "v_readfirstlane_b32 s48, v58\n\t"
      "v_readfirstlane_b32 s49, v59\n\t"
      "ds_read_b64_tr_b16 v[40:41], v57 offset:0\n\t"
      "ds_read_b64_tr_b16 v[42:43], v57 offset:256\n\t"
      "v_mfma_f32_32x32x16_bf16 v[176:191], a[136:139], v[84:87], v[176:191]\n\t"
      "buffer_load_dwordx4 v5, s[4:7], s61 offen offset:3072 lds\n\t"
      "ds_read_b64_tr_b16 v[44:45], v57 offset:512\n\t"
      "ds_read_b64_tr_b16 v[46:47], v57 offset:768\n\t"
      "s_add_u32 s48, s48, 1\n\t"
      "s_addc_u32 s49, s49, 0\n\t"
      "v_mov_b32 v62, s48\n\t"
      "ds_read_b128 a[20:23], v241 offset:32768\n\t"
      "ds_read_b128 a[148:151], v245 offset:32768\n\t"
      "ds_read_b64_tr_b16 v[104:105], v26 offset:34816\n\t"
      "ds_read_b64_tr_b16 v[106:107], v26 offset:35328\n\t"
      "v_mfma_f32_32x32x16_bf16 v[128:143], v[88:91], a[12:15], v[128:143]\n\t"
      "s_add_u32 s60, s46, 3\n\t"
      "s_and_b32 s60, s60, 7\n\t"
      "s_mul_i32 s61, s60, 196608\n\t"
      "s_add_u32 m0, s57, 98304\n\t"
      "s_add_u32 s61, s61, s54\n\t"
      "buffer_load_dwordx4 v6, s[8:11], s61 offen lds\n\t"
      "v_mov_b32 v63, s49\n\t"
      "s_waitcnt lgkmcnt(7)\n\t"
      "v_xor_b32 v40, v56, v40\n\t"
      "v_xor_b32 v41, v56, v41\n\t"
      "s_waitcnt lgkmcnt(6)\n\t"
      "v_xor_b32 v42, v56, v42\n\t"
      "v_mfma_f32_32x32x16_bf16 v[144:159], v[88:91], a[140:143], v[144:159]\n\t"
      "buffer_load_dwordx4 v7, s[8:11], s61 offen offset:1024 lds\n\t"
      "v_xor_b32 v43, v56, v43\n\t"
      "s_waitcnt lgkmcnt(5)\n\t"
      "v_xor_b32 v44, v56, v44\n\t"
      "v_xor_b32 v45, v56, v45\n\t"
      "s_waitcnt lgkmcnt(4)\n\t"
      "v_xor_b32 v46, v56, v46\n\t"
      "ds_read_b64_tr_b16 v[108:109], v26 offset:43008\n\t"
      "ds_read_b64_tr_b16 v[110:111], v26 offset:43520\n\t"
      "v_mfma_f32_32x32x16_bf16 v[160:175], a[12:15], v[92:95], v[160:175]\n\t"
      "s_add_u32 s60, s46, 3\n\t"
      "s_and_b32 s60, s60, 7\n\t"
      "s_mul_i32 s61, s60, 196608\n\t"
      "s_add_u32 m0, s57, 106496\n\t"
      "s_add_u32 s61, s61, s55\n\t"
      "buffer_load_dwordx4 v6, s[8:11], s61 offen lds\n\t"
      "v_xor_b32 v47, v56, v47\n\t"
      "v_mfma_f32_32x32x16_bf16 v[176:191], a[140:143], v[92:95], v[176:191]\n\t"
      "buffer_load_dwordx4 v7, s[8:11], s61 offen offset:1024 lds\n\t"
      "ds_read_b128 a[24:27], v242 offset:32768\n\t"
      "ds_read_b128 a[152:155], v246 offset:32768\n\t"
      "ds_read_b64_tr_b16 v[112:113], v26 offset:36864\n\t"
      "ds_read_b64_tr_b16 v[114:115], v26 offset:37376\n\t"
      "v_mfma_f32_32x32x16_bf16 v[128:143], v[96:99], a[16:19], v[128:143]\n\t"
      "v_mfma_f32_32x32x16_bf16 v[144:159], v[96:99], a[144:147], v[144:159]\n\t"
      "s_add_u32 s60, s42, 0\n\t"
      "s_lshl_b32 s60, s60, 13\n\t"
      "s_lshl_b32 s61, s41, 17\n\t"
      "s_add_u32 s60, s60, s61\n\t"
      "s_lshl_b32 s61, s40, 11\n\t"
      "s_add_u32 s60, s60, s61\n\t"
      "s_add_u32 s60, s60, 512\n\t"
      "v_and_b32 v36, 1, v0\n\t"
      "v_lshlrev_b32 v36, 3, v36\n\t"
      "v_lshrrev_b32 v37, 1, v0\n\t"
      "v_add_u32 v36, v36, v37\n\t"
      "v_lshlrev_b32 v36, 7, v36\n\t"
      "s_mov_b64 exec, 255\n\t"
      "buffer_load_dword v39, v36, s[4:7], s60 offen\n\t"
      "s_mov_b64 exec, 0xffff\n\t"
      "s_lshl_b32 s60, s41, 4\n\t"
      "s_lshr_b32 s61, s40, 1\n\t"
      "s_lshl_b32 s61, s61, 8\n\t"
      "s_add_u32 s60, s60, s61\n\t"
      "v_add_u32 v36, s60, v0\n\t"
      "s_and_b32 s61, s40, 1\n\t"
      "s_cmp_lg_u32 s61, 0\n\t"
      "s_cbranch_scc1 .Lskip_3_%=\n\t"
      "v_mul_u32_u24 v37, 3584, v36\n\t"
      "buffer_load_dword v39, v37, s[12:15], s53 offen\n\t"
      "s_nop 0\n\t"
      ".Lskip_3_%=:\n\t"
      "s_mov_b64 exec, -1\n\t"
      "ds_read_b64_tr_b16 v[116:117], v26 offset:45056\n\t"
      "ds_read_b64_tr_b16 v[118:119], v26 offset:45568\n\t"
      "v_mfma_f32_32x32x16_bf16 v[160:175], a[16:19], v[100:103], v[160:175]\n\t"
      "v_mfma_f32_32x32x16_bf16 v[176:191], a[144:147], v[100:103], v[176:191]\n\t"
      "s_add_u32 s60, s46, 4\n\t"
      "s_and_b32 s60, s60, 7\n\t"
      "s_lshl_b32 s61, s60, 7\n\t"
      "s_add_u32 m0, s58, 0\n\t"
      "s_add_u32 s61, s61, s52\n\t"
      "buffer_load_dwordx4 v2, s[4:7], s61 offen lds\n\t"
      "ds_read_b128 a[28:31], v243 offset:32768\n\t"
      "ds_read_b128 a[156:159], v247 offset:32768\n\t"
      "ds_read_b64_tr_b16 v[120:121], v26 offset:38912\n\t"
      "ds_read_b64_tr_b16 v[122:123], v26 offset:39424\n\t"
      "s_waitcnt lgkmcnt(12)\n\t"
      "v_mfma_f32_32x32x16_bf16 v[128:143], v[104:107], a[20:23], v[128:143]\n\t"
      "v_mfma_f32_32x32x16_bf16 v[144:159], v[104:107], a[148:151], v[144:159]\n\t"
      "buffer_load_dwordx4 v3, s[4:7], s61 offen offset:1024 lds\n\t"
      "ds_read_b64_tr_b16 v[124:125], v26 offset:47104\n\t"
      "ds_read_b64_tr_b16 v[126:127], v26 offset:47616\n\t"
      "s_waitcnt lgkmcnt(12)\n\t"
      "v_mfma_f32_32x32x16_bf16 v[160:175], a[20:23], v[108:111], v[160:175]\n\t"
      "v_mfma_f32_32x32x16_bf16 v[176:191], a[148:151], v[108:111], v[176:191]\n\t"
      "buffer_load_dwordx4 v4, s[4:7], s61 offen offset:2048 lds\n\t"
      "s_waitcnt lgkmcnt(8)\n\t"
      "v_mfma_f32_32x32x16_bf16 v[128:143], v[112:115], a[24:27], v[128:143]\n\t"
      "v_mfma_f32_32x32x16_bf16 v[144:159], v[112:115], a[152:155], v[144:159]\n\t"
      "buffer_load_dwordx4 v5, s[4:7], s61 offen offset:3072 lds\n\t"
      "s_waitcnt vmcnt(13)\n\t"
      "s_waitcnt lgkmcnt(0)\n\t"
      "s_barrier\n\t"
      "ds_read_b128 a[32:35], v248 offset:0\n\t"
      "ds_read_b128 a[160:163], v252 offset:0\n\t"
      "ds_read_b64_tr_b16 v[64:65], v27 offset:0\n\t"
      "ds_read_b64_tr_b16 v[66:67], v27 offset:512\n\t"
      "ds_read_b64_tr_b16 v[68:69], v27 offset:8192\n\t"
      "ds_read_b64_tr_b16 v[70:71], v27 offset:8704\n\t"
      "v_mfma_f32_32x32x16_bf16 v[160:175], a[24:27], v[116:119], v[160:175]\n\t"
      "v_mfma_f32_32x32x16_bf16 v[176:191], a[152:155], v[116:119], v[176:191]\n\t"
      "s_add_u32 s60, s46, 4\n\t"
      "s_and_b32 s60, s60, 7\n\t"
      "s_mul_i32 s61, s60, 196608\n\t"
      "s_add_u32 m0, s57, 0\n\t"
      "s_add_u32 s61, s61, s54\n\t"
      "buffer_load_dwordx4 v6, s[8:11], s61 offen lds\n\t"
      "ds_read_b128 a[36:39], v249 offset:0\n\t"
      "ds_read_b128 a[164:167], v253 offset:0\n\t"
      "ds_read_b64_tr_b16 v[72:73], v27 offset:2048\n\t"
      "ds_read_b64_tr_b16 v[74:75], v27 offset:2560\n\t"
      "v_mfma_f32_32x32x16_bf16 v[128:143], v[120:123], a[28:31], v[128:143]\n\t"
      "v_mfma_f32_32x32x16_bf16 v[144:159], v[120:123], a[156:159], v[144:159]\n\t"
      "buffer_load_dwordx4 v7, s[8:11], s61 offen offset:1024 lds\n\t"
      "ds_read_b64_tr_b16 v[76:77], v27 offset:10240\n\t"
      "ds_read_b64_tr_b16 v[78:79], v27 offset:10752\n\t"
      "v_mfma_f32_32x32x16_bf16 v[160:175], a[28:31], v[124:127], v[160:175]\n\t"
      "v_mfma_f32_32x32x16_bf16 v[176:191], a[156:159], v[124:127], v[176:191]\n\t"
      "s_add_u32 s60, s46, 4\n\t"
      "s_and_b32 s60, s60, 7\n\t"
      "s_mul_i32 s61, s60, 196608\n\t"
      "s_add_u32 m0, s57, 8192\n\t"
      "s_add_u32 s61, s61, s55\n\t"
      "buffer_load_dwordx4 v6, s[8:11], s61 offen lds\n\t"
      "ds_read_b128 a[40:43], v250 offset:0\n\t"
      "ds_read_b128 a[168:171], v254 offset:0\n\t"
      "ds_read_b64_tr_b16 v[80:81], v27 offset:4096\n\t"
      "ds_read_b64_tr_b16 v[82:83], v27 offset:4608\n\t"
      "s_waitcnt lgkmcnt(12)\n\t"
      "v_mfma_f32_32x32x16_bf16 v[128:143], v[64:67], a[32:35], v[128:143]\n\t"
      "v_mfma_f32_32x32x16_bf16 v[144:159], v[64:67], a[160:163], v[144:159]\n\t"
      "buffer_load_dwordx4 v7, s[8:11], s61 offen offset:1024 lds\n\t"
      "ds_read_b64_tr_b16 v[84:85], v27 offset:12288\n\t"
      "ds_read_b64_tr_b16 v[86:87], v27 offset:12800\n\t"
      "s_waitcnt lgkmcnt(12)\n\t"
      "v_mfma_f32_32x32x16_bf16 v[160:175], a[32:35], v[68:71], v[160:175]\n\t"
      "v_mfma_f32_32x32x16_bf16 v[176:191], a[160:163], v[68:71], v[176:191]\n\t"
      "s_add_u32 s60, s46, 5\n\t"
      "s_and_b32 s60, s60, 7\n\t"
      "s_lshl_b32 s61, s60, 7\n\t"
      "s_add_u32 m0, s58, 32768\n\t"
      "s_add_u32 s61, s61, s52\n\t"
      "buffer_load_dwordx4 v2, s[4:7], s61 offen lds\n\t"
      "ds_read_b128 a[44:47], v251 offset:0\n\t"
      "ds_read_b128 a[172:175], v255 offset:0\n\t"
      "ds_read_b64_tr_b16 v[88:89], v27 offset:6144\n\t"
      "ds_read_b64_tr_b16 v[90:91], v27 offset:6656\n\t"
      "s_waitcnt lgkmcnt(12)\n\t"
      "v_mfma_f32_32x32x16_bf16 v[128:143], v[72:75], a[36:39], v[128:143]\n\t"
      "v_mfma_f32_32x32x16_bf16 v[144:159], v[72:75], a[164:167], v[144:159]\n\t"
      "buffer_load_dwordx4 v3, s[4:7], s61 offen offset:1024 lds\n\t"
      "ds_read_b64_tr_b16 v[92:93], v27 offset:14336\n\t"
      "ds_read_b64_tr_b16 v[94:95], v27 offset:14848\n\t"
      "s_waitcnt lgkmcnt(12)\n\t"
      "v_mfma_f32_32x32x16_bf16 v[160:175], a[36:39], v[76:79], v[160:175]\n\t"
      "v_mfma_f32_32x32x16_bf16 v[176:191], a[164:167], v[76:79], v[176:191]\n\t"
      "buffer_load_dwordx4 v4, s[4:7], s61 offen offset:2048 lds\n\t"
      "s_waitcnt lgkmcnt(8)\n\t"
      "v_mfma_f32_32x32x16_bf16 v[128:143], v[80:83], a[40:43], v[128:143]\n\t"
      "v_mfma_f32_32x32x16_bf16 v[144:159], v[80:83], a[168:171], v[144:159]\n\t"
      "buffer_load_dwordx4 v5, s[4:7], s61 offen offset:3072 lds\n\t"
      "s_waitcnt vmcnt(13)\n\t"
      "s_waitcnt lgkmcnt(0)\n\t"
      "s_barrier\n\t"
      "ds_read_b128 a[48:51], v248 offset:32768\n\t"
      "ds_read_b128 a[176:179], v252 offset:32768\n\t"
      "ds_read_b64_tr_b16 v[96:97], v27 offset:32768\n\t"
      "ds_read_b64_tr_b16 v[98:99], v27 offset:33280\n\t"
      "ds_read_b64_tr_b16 v[100:101], v27 offset:40960\n\t"
      "ds_read_b64_tr_b16 v[102:103], v27 offset:41472\n\t"
      "v_mfma_f32_32x32x16_bf16 v[160:175], a[40:43], v[84:87], v[160:175]\n\t"
      "v_mfma_f32_32x32x16_bf16 v[176:191], a[168:171], v[84:87], v[176:191]\n\t"
      "s_add_u32 s60, s46, 5\n\t"
      "s_and_b32 s60, s60, 7\n\t"
      "s_mul_i32 s61, s60, 196608\n\t"
      "s_add_u32 m0, s57, 32768\n\t"
      "s_add_u32 s61, s61, s54\n\t"
      "buffer_load_dwordx4 v6, s[8:11], s61 offen lds\n\t"
      "ds_read_b128 a[52:55], v249 offset:32768\n\t"
      "ds_read_b128 a[180:183], v253 offset:32768\n\t"
      "ds_read_b64_tr_b16 v[104:105], v27 offset:34816\n\t"
      "ds_read_b64_tr_b16 v[106:107], v27 offset:35328\n\t"
      "v_mfma_f32_32x32x16_bf16 v[128:143], v[88:91], a[44:47], v[128:143]\n\t"
      "v_mfma_f32_32x32x16_bf16 v[144:159], v[88:91], a[172:175], v[144:159]\n\t"
      "buffer_load_dwordx4 v7, s[8:11], s61 offen offset:1024 lds\n\t"
      "ds_read_b64_tr_b16 v[108:109], v27 offset:43008\n\t"
      "ds_read_b64_tr_b16 v[110:111], v27 offset:43520\n\t"
      "v_mfma_f32_32x32x16_bf16 v[160:175], a[44:47], v[92:95], v[160:175]\n\t"
      "v_mfma_f32_32x32x16_bf16 v[176:191], a[172:175], v[92:95], v[176:191]\n\t"
      "s_add_u32 s60, s46, 5\n\t"
      "s_and_b32 s60, s60, 7\n\t"
      "s_mul_i32 s61, s60, 196608\n\t"
      "s_add_u32 m0, s57, 40960\n\t"
      "s_add_u32 s61, s61, s55\n\t"
      "buffer_load_dwordx4 v6, s[8:11], s61 offen lds\n\t"
      "ds_read_b128 a[56:59], v250 offset:32768\n\t"
      "ds_read_b128 a[184:187], v254 offset:32768\n\t"
      "ds_read_b64_tr_b16 v[112:113], v27 offset:36864\n\t"
      "ds_read_b64_tr_b16 v[114:115], v27 offset:37376\n\t"
      "s_waitcnt lgkmcnt(12)\n\t"
      "v_mfma_f32_32x32x16_bf16 v[128:143], v[96:99], a[48:51], v[128:143]\n\t"
      "v_mfma_f32_32x32x16_bf16 v[144:159], v[96:99], a[176:179], v[144:159]\n\t"
      "buffer_load_dwordx4 v7, s[8:11], s61 offen offset:1024 lds\n\t"
      "ds_read_b64_tr_b16 v[116:117], v27 offset:45056\n\t"
      "ds_read_b64_tr_b16 v[118:119], v27 offset:45568\n\t"
      "s_waitcnt lgkmcnt(12)\n\t"
      "v_mfma_f32_32x32x16_bf16 v[160:175], a[48:51], v[100:103], v[160:175]\n\t"
      "v_mfma_f32_32x32x16_bf16 v[176:191], a[176:179], v[100:103], v[176:191]\n\t"
      "s_add_u32 s60, s46, 6\n\t"
      "s_and_b32 s60, s60, 7\n\t"
      "s_lshl_b32 s61, s60, 7\n\t"
      "s_add_u32 m0, s58, 65536\n\t"
      "s_add_u32 s61, s61, s52\n\t"
      "buffer_load_dwordx4 v2, s[4:7], s61 offen lds\n\t"
      "ds_read_b128 a[60:63], v251 offset:32768\n\t"
      "ds_read_b128 a[188:191], v255 offset:32768\n\t"
      "ds_read_b64_tr_b16 v[120:121], v27 offset:38912\n\t"
      "ds_read_b64_tr_b16 v[122:123], v27 offset:39424\n\t"
      "s_waitcnt lgkmcnt(12)\n\t"
      "v_mfma_f32_32x32x16_bf16 v[128:143], v[104:107], a[52:55], v[128:143]\n\t"
      "v_mfma_f32_32x32x16_bf16 v[144:159], v[104:107], a[180:183], v[144:159]\n\t"
      "buffer_load_dwordx4 v3, s[4:7], s61 offen offset:1024 lds\n\t"
      "ds_read_b64_tr_b16 v[124:125], v27 offset:47104\n\t"
      "ds_read_b64_tr_b16 v[126:127], v27 offset:47616\n\t"
      "s_waitcnt lgkmcnt(12)\n\t"
      "v_mfma_f32_32x32x16_bf16 v[160:175], a[52:55], v[108:111], v[160:175]\n\t"
      "v_mfma_f32_32x32x16_bf16 v[176:191], a[180:183], v[108:111], v[176:191]\n\t"
      "buffer_load_dwordx4 v4, s[4:7], s61 offen offset:2048 lds\n\t"
      "s_waitcnt lgkmcnt(8)\n\t"
      "v_mfma_f32_32x32x16_bf16 v[128:143], v[112:115], a[56:59], v[128:143]\n\t"
      "v_mfma_f32_32x32x16_bf16 v[144:159], v[112:115], a[184:187], v[144:159]\n\t"
      "buffer_load_dwordx4 v5, s[4:7], s61 offen offset:3072 lds\n\t"
      "s_waitcnt vmcnt(12)\n\t"
      "s_waitcnt lgkmcnt(0)\n\t"
      "s_barrier\n\t"
      "ds_read_b128 a[64:67], v240 offset:0\n\t"
      "ds_read_b128 a[192:195], v244 offset:0\n\t"
      "ds_read_b64_tr_b16 v[64:65], v26 offset:0\n\t"
      "ds_read_b64_tr_b16 v[66:67], v26 offset:512\n\t"
      "ds_read_b64_tr_b16 v[68:69], v26 offset:8192\n\t"
      "ds_read_b64_tr_b16 v[70:71], v26 offset:8704\n\t"
      "v_mfma_f32_32x32x16_bf16 v[160:175], a[56:59], v[116:119], v[160:175]\n\t"
      "v_mfma_f32_32x32x16_bf16 v[176:191], a[184:187], v[116:119], v[176:191]\n\t"
      "s_add_u32 s60, s46, 6\n\t"
      "s_and_b32 s60, s60, 7\n\t"
      "s_mul_i32 s61, s60, 196608\n\t"
      "s_add_u32 m0, s57, 65536\n\t"
      "s_add_u32 s61, s61, s54\n\t"
      "buffer_load_dwordx4 v6, s[8:11], s61 offen lds\n\t"
      "ds_read_b128 a[68:71], v241 offset:0\n\t"
      "ds_read_b128 a[196:199], v245 offset:0\n\t"
      "ds_read_b64_tr_b16 v[72:73], v26 offset:2048\n\t"
      "ds_read_b64_tr_b16 v[74:75], v26 offset:2560\n\t"
      "v_mfma_f32_32x32x16_bf16 v[128:143], v[120:123], a[60:63], v[128:143]\n\t"
      "v_mfma_f32_32x32x16_bf16 v[144:159], v[120:123], a[188:191], v[144:159]\n\t"
      "buffer_load_dwordx4 v7, s[8:11], s61 offen offset:1024 lds\n\t"
      "ds_read_b64_tr_b16 v[76:77], v26 offset:10240\n\t"
      "ds_read_b64_tr_b16 v[78:79], v26 offset:10752\n\t"
      "v_mfma_f32_32x32x16_bf16 v[160:175], a[60:63], v[124:127], v[160:175]\n\t"
      "v_mfma_f32_32x32x16_bf16 v[176:191], a[188:191], v[124:127], v[176:191]\n\t"
      "s_add_u32 s60, s46, 6\n\t"
      "s_and_b32 s60, s60, 7\n\t"
      "s_mul_i32 s61, s60, 196608\n\t"
      "s_add_u32 m0, s57, 73728\n\t"
      "s_add_u32 s61, s61, s55\n\t"
      "buffer_load_dwordx4 v6, s[8:11], s61 offen lds\n\t"
      "ds_read_b128 a[72:75], v242 offset:0\n\t"
      "ds_read_b128 a[200:203], v246 offset:0\n\t"
      "ds_read_b64_tr_b16 v[80:81], v26 offset:4096\n\t"
      "ds_read_b64_tr_b16 v[82:83], v26 offset:4608\n\t"
      "s_waitcnt lgkmcnt(12)\n\t"
      "v_mfma_f32_32x32x16_bf16 v[128:143], v[64:67], a[64:67], v[128:143]\n\t"
      "v_mfma_f32_32x32x16_bf16 v[144:159], v[64:67], a[192:195], v[144:159]\n\t"
      "buffer_load_dwordx4 v7, s[8:11], s61 offen offset:1024 lds\n\t"
      "ds_read_b64_tr_b16 v[84:85], v26 offset:12288\n\t"
      "ds_read_b64_tr_b16 v[86:87], v26 offset:12800\n\t"
      "s_waitcnt lgkmcnt(12)\n\t"
      "v_mfma_f32_32x32x16_bf16 v[160:175], a[64:67], v[68:71], v[160:175]\n\t"
      "v_mfma_f32_32x32x16_bf16 v[176:191], a[192:195], v[68:71], v[176:191]\n\t"
      "s_add_u32 s60, s46, 7\n\t"
      "s_and_b32 s60, s60, 7\n\t"
      "s_lshl_b32 s61, s60, 7\n\t"
      "s_add_u32 m0, s58, 98304\n\t"
      "s_add_u32 s61, s61, s52\n\t"
      "buffer_load_dwordx4 v2, s[4:7], s61 offen lds\n\t"
      "ds_read_b128 a[76:79], v243 offset:0\n\t"
      "ds_read_b128 a[204:207], v247 offset:0\n\t"
      "ds_read_b64_tr_b16 v[88:89], v26 offset:6144\n\t"
      "ds_read_b64_tr_b16 v[90:91], v26 offset:6656\n\t"
      "s_waitcnt lgkmcnt(12)\n\t"
      "v_mfma_f32_32x32x16_bf16 v[128:143], v[72:75], a[68:71], v[128:143]\n\t"
      "v_mfma_f32_32x32x16_bf16 v[144:159], v[72:75], a[196:199], v[144:159]\n\t"
      "buffer_load_dwordx4 v3, s[4:7], s61 offen offset:1024 lds\n\t"
      "ds_read_b64_tr_b16 v[92:93], v26 offset:14336\n\t"
      "ds_read_b64_tr_b16 v[94:95], v26 offset:14848\n\t"
      "s_waitcnt lgkmcnt(12)\n\t"
      "v_mfma_f32_32x32x16_bf16 v[160:175], a[68:71], v[76:79], v[160:175]\n\t"
      "v_mfma_f32_32x32x16_bf16 v[176:191], a[196:199], v[76:79], v[176:191]\n\t"
      "buffer_load_dwordx4 v4, s[4:7], s61 offen offset:2048 lds\n\t"
      "s_waitcnt lgkmcnt(8)\n\t"
      "v_mfma_f32_32x32x16_bf16 v[128:143], v[80:83], a[72:75], v[128:143]\n\t"
      "v_mfma_f32_32x32x16_bf16 v[144:159], v[80:83], a[200:203], v[144:159]\n\t"
      "buffer_load_dwordx4 v5, s[4:7], s61 offen offset:3072 lds\n\t"
      "s_waitcnt vmcnt(12)\n\t"
      "s_waitcnt lgkmcnt(0)\n\t"
      "s_barrier\n\t"
      "ds_read_b128 a[80:83], v240 offset:32768\n\t"
      "ds_read_b128 a[208:211], v244 offset:32768\n\t"
      "ds_read_b64_tr_b16 v[96:97], v26 offset:32768\n\t"
      "ds_read_b64_tr_b16 v[98:99], v26 offset:33280\n\t"
      "ds_read_b64_tr_b16 v[100:101], v26 offset:40960\n\t"
      "ds_read_b64_tr_b16 v[102:103], v26 offset:41472\n\t"
      "v_mfma_f32_32x32x16_bf16 v[160:175], a[72:75], v[84:87], v[160:175]\n\t"
      "v_mfma_f32_32x32x16_bf16 v[176:191], a[200:203], v[84:87], v[176:191]\n\t"
      "s_add_u32 s60, s46, 7\n\t"
      "s_and_b32 s60, s60, 7\n\t"
      "s_mul_i32 s61, s60, 196608\n\t"
      "s_add_u32 m0, s57, 98304\n\t"
      "s_add_u32 s61, s61, s54\n\t"
      "buffer_load_dwordx4 v6, s[8:11], s61 offen lds\n\t"
      "ds_read_b128 a[84:87], v241 offset:32768\n\t"
      "ds_read_b128 a[212:215], v245 offset:32768\n\t"
      "ds_read_b64_tr_b16 v[104:105], v26 offset:34816\n\t"
      "ds_read_b64_tr_b16 v[106:107], v26 offset:35328\n\t"
      "v_mfma_f32_32x32x16_bf16 v[128:143], v[88:91], a[76:79], v[128:143]\n\t"
      "v_mfma_f32_32x32x16_bf16 v[144:159], v[88:91], a[204:207], v[144:159]\n\t"
      "buffer_load_dwordx4 v7, s[8:11], s61 offen offset:1024 lds\n\t"
      "ds_read_b64_tr_b16 v[108:109], v26 offset:43008\n\t"
      "ds_read_b64_tr_b16 v[110:111], v26 offset:43520\n\t"
      "v_mfma_f32_32x32x16_bf16 v[160:175], a[76:79], v[92:95], v[160:175]\n\t"
      "v_mfma_f32_32x32x16_bf16 v[176:191], a[204:207], v[92:95], v[176:191]\n\t"
      "s_add_u32 s60, s46, 7\n\t"
      "s_and_b32 s60, s60, 7\n\t"
      "s_mul_i32 s61, s60, 196608\n\t"
      "s_add_u32 m0, s57, 106496\n\t"
      "s_add_u32 s61, s61, s55\n\t"
      "buffer_load_dwordx4 v6, s[8:11], s61 offen lds\n\t"
      "ds_read_b128 a[88:91], v242 offset:32768\n\t"
      "ds_read_b128 a[216:219], v246 offset:32768\n\t"
      "ds_read_b64_tr_b16 v[112:113], v26 offset:36864\n\t"
      "ds_read_b64_tr_b16 v[114:115], v26 offset:37376\n\t"
      "s_waitcnt lgkmcnt(12)\n\t"
      "v_mfma_f32_32x32x16_bf16 v[128:143], v[96:99], a[80:83], v[128:143]\n\t"
      "v_mfma_f32_32x32x16_bf16 v[144:159], v[96:99], a[208:211], v[144:159]\n\t"
      "ds_read_b64_tr_b16 v[116:117], v26 offset:45056\n\t"
      "ds_read_b64_tr_b16 v[118:119], v26 offset:45568\n\t"
      "s_waitcnt lgkmcnt(12)\n\t"
      "v_mfma_f32_32x32x16_bf16 v[160:175], a[80:83], v[100:103], v[160:175]\n\t"
      "buffer_load_dwordx4 v7, s[8:11], s61 offen offset:1024 lds\n\t"
      "v_mfma_f32_32x32x16_bf16 v[176:191], a[208:211], v[100:103], v[176:191]\n\t"
      "ds_read_b128 a[92:95], v243 offset:32768\n\t"
      "ds_read_b128 a[220:223], v247 offset:32768\n\t"
      "ds_read_b64_tr_b16 v[120:121], v26 offset:38912\n\t"
      "ds_read_b64_tr_b16 v[122:123], v26 offset:39424\n\t"
      "s_waitcnt lgkmcnt(12)\n\t"
      "v_mfma_f32_32x32x16_bf16 v[128:143], v[104:107], a[84:87], v[128:143]\n\t"
      "v_mfma_f32_32x32x16_bf16 v[144:159], v[104:107], a[212:215], v[144:159]\n\t"
      "s_lshl_b32 s60, s2, 2\n\t"
      "s_add_u32 s60, s60, s40\n\t"
      "s_mul_i32 s61, s60, 1792\n\t"
      "s_lshl_b32 s60, s60, 9\n\t"
      "v_lshlrev_b32 v36, 7, v0\n\t"
      "s_mov_b64 exec, 0x3fff\n\t"
      "buffer_load_dword v39, v36, s[16:19], s61 offen\n\t"
      "s_mov_b64 exec, 0xf\n\t"
      "buffer_load_dword v39, v36, s[20:23], s60 offen\n\t"
      "s_mov_b64 exec, -1\n\t"
      "ds_read_b64_tr_b16 v[124:125], v26 offset:47104\n\t"
      "ds_read_b64_tr_b16 v[126:127], v26 offset:47616\n\t"
      "s_waitcnt lgkmcnt(12)\n\t"
      "v_mfma_f32_32x32x16_bf16 v[160:175], a[84:87], v[108:111], v[160:175]\n\t"
      "v_mfma_f32_32x32x16_bf16 v[176:191], a[212:215], v[108:111], v[176:191]\n\t"
      "s_waitcnt lgkmcnt(8)\n\t"
      "v_mfma_f32_32x32x16_bf16 v[128:143], v[112:115], a[88:91], v[128:143]\n\t"
      "s_lshl_b32 s60, s41, 4\n\t"
      "s_add_u32 s60, s60, s42\n\t"
      "s_add_u32 s60, s60, 0\n\t"
      "s_lshl_b32 s60, s60, 2\n\t"
      "s_add_u32 s60, s60, s40\n\t"
      "s_lshl_b32 s60, s60, 7\n\t"
      "s_add_u32 s60, s60, 3145728\n\t"
      "v_lshlrev_b32 v36, 17, v0\n\t"
      "v_add_u32 v36, s60, v36\n\t"
      "s_lshl_b32 s60, s42, 2\n\t"
      "s_add_u32 s60, s60, s40\n\t"
      "s_lshl_b32 s60, s60, 6\n\t"
      "s_add_u32 s60, s60, s41\n\t"
      "s_lshl_b32 s60, s60, 10\n\t"
      "s_add_u32 s60, s60, 1048832\n\t"
      "v_bfe_u32 v37, v0, 1, 2\n\t"
      "v_lshlrev_b32 v37, 14, v37\n\t"
      "v_and_b32 v202, 1, v0\n\t"
      "v_lshl_add_u32 v37, v202, 9, v37\n\t"
      "v_add_u32 v37, s60, v37\n\t"
      "v_cmp_lt_u32 vcc, 7, v0\n\t"
      "s_nop 1\n\t"
      "v_cndmask_b32 v36, v36, v37, vcc\n\t"
      "s_lshl_b32 s60, s42, 4\n\t"
      "s_add_u32 s60, s60, s41\n\t"
      "s_lshl_b32 s60, s60, 10\n\t"
      "s_add_u32 s60, s60, 786688\n\t"
      "v_lshlrev_b32 v202, 9, v202\n\t"
      "v_add_u32 v37, s60, v202\n\t"
      "v_cmp_lt_u32 vcc, 15, v0\n\t"
      "s_nop 1\n\t"
      "v_cndmask_b32 v36, v36, v37, vcc\n\t"
      "s_mov_b64 exec, 0x3ff3e\n\t"
      "buffer_load_dword v39, v36, s[24:27], 0 offen sc1\n\t"
      "s_mov_b64 exec, -1\n\t"
      "v_mfma_f32_32x32x16_bf16 v[144:159], v[112:115], a[216:219], v[144:159]\n\t"
      "s_waitcnt vmcnt(11)\n\t"
      "s_waitcnt lgkmcnt(0)\n\t"
      "s_barrier\n\t"
      "ds_read_b128 a[96:99], v248 offset:0\n\t"
      "ds_read_b128 a[224:227], v252 offset:0\n\t"
      "ds_read_b64_tr_b16 v[64:65], v27 offset:0\n\t"
      "ds_read_b64_tr_b16 v[66:67], v27 offset:512\n\t"
      "ds_read_b64_tr_b16 v[68:69], v27 offset:8192\n\t"
      "ds_read_b64_tr_b16 v[70:71], v27 offset:8704\n\t"
      "v_mfma_f32_32x32x16_bf16 v[160:175], a[88:91], v[116:119], v[160:175]\n\t"
      "v_mfma_f32_32x32x16_bf16 v[176:191], a[216:219], v[116:119], v[176:191]\n\t"
      "s_add_u32 s60, s46, 0\n\t"
      "s_and_b32 s60, s60, 7\n\t"
      "s_mul_i32 s61, s60, 229376\n\t"
      "s_add_u32 m0, s57, 0\n\t"
      "s_add_u32 s61, s61, s53\n\t"
      "buffer_load_dwordx4 v8, s[12:15], s61 offen lds\n\t"
      "ds_read_b128 a[100:103], v249 offset:0\n\t"
      "ds_read_b128 a[228:231], v253 offset:0\n\t"
      "ds_read_b64_tr_b16 v[72:73], v27 offset:2048\n\t"
      "ds_read_b64_tr_b16 v[74:75], v27 offset:2560\n\t"
      "v_mfma_f32_32x32x16_bf16 v[128:143], v[120:123], a[92:95], v[128:143]\n\t"
      "v_mfma_f32_32x32x16_bf16 v[144:159], v[120:123], a[220:223], v[144:159]\n\t"
      "ds_read_b64_tr_b16 v[76:77], v27 offset:10240\n\t"
      "ds_read_b64_tr_b16 v[78:79], v27 offset:10752\n\t"
      "v_mfma_f32_32x32x16_bf16 v[160:175], a[92:95], v[124:127], v[160:175]\n\t"
      "buffer_load_dwordx4 v9, s[12:15], s61 offen offset:1024 lds\n\t"
      "v_mfma_f32_32x32x16_bf16 v[176:191], a[220:223], v[124:127], v[176:191]\n\t"
      "ds_read_b128 a[104:107], v250 offset:0\n\t"
      "ds_read_b128 a[232:235], v254 offset:0\n\t"
      "ds_read_b64_tr_b16 v[80:81], v27 offset:4096\n\t"
      "ds_read_b64_tr_b16 v[82:83], v27 offset:4608\n\t"
      "s_waitcnt lgkmcnt(12)\n\t"
      "v_mfma_f32_32x32x16_bf16 v[128:143], v[64:67], a[96:99], v[128:143]\n\t"
      "v_mfma_f32_32x32x16_bf16 v[144:159], v[64:67], a[224:227], v[144:159]\n\t"
      "s_add_u32 s60, s46, 1\n\t"
      "s_and_b32 s60, s60, 7\n\t"
      "s_mul_i32 s61, s60, 229376\n\t"
      "s_add_u32 m0, s57, 8192\n\t"
      "s_add_u32 s61, s61, s53\n\t"
      "buffer_load_dwordx4 v8, s[12:15], s61 offen lds\n\t"
      "ds_read_b64_tr_b16 v[84:85], v27 offset:12288\n\t"
      "ds_read_b64_tr_b16 v[86:87], v27 offset:12800\n\t"
      "s_waitcnt lgkmcnt(12)\n\t"
      "v_mfma_f32_32x32x16_bf16 v[160:175], a[96:99], v[68:71], v[160:175]\n\t"
      "v_mfma_f32_32x32x16_bf16 v[176:191], a[224:227], v[68:71], v[176:191]\n\t"
      "ds_read_b128 a[108:111], v251 offset:0\n\t"
      "ds_read_b128 a[236:239], v255 offset:0\n\t"
      "ds_read_b64_tr_b16 v[88:89], v27 offset:6144\n\t"
      "ds_read_b64_tr_b16 v[90:91], v27 offset:6656\n\t"
      "s_waitcnt lgkmcnt(12)\n\t"
      "v_mfma_f32_32x32x16_bf16 v[128:143], v[72:75], a[100:103], v[128:143]\n\t"
      "buffer_load_dwordx4 v9, s[12:15], s61 offen offset:1024 lds\n\t"
      "v_mfma_f32_32x32x16_bf16 v[144:159], v[72:75], a[228:231], v[144:159]\n\t"
      "ds_read_b64_tr_b16 v[92:93], v27 offset:14336\n\t"
      "ds_read_b64_tr_b16 v[94:95], v27 offset:14848\n\t"
      "s_waitcnt lgkmcnt(12)\n\t"
      "v_mfma_f32_32x32x16_bf16 v[160:175], a[100:103], v[76:79], v[160:175]\n\t"
      "v_mfma_f32_32x32x16_bf16 v[176:191], a[228:231], v[76:79], v[176:191]\n\t"
      "s_add_u32 s60, s46, 2\n\t"
      "s_and_b32 s60, s60, 7\n\t"
      "s_mul_i32 s61, s60, 229376\n\t"
      "s_add_u32 m0, s57, 16384\n\t"
      "s_add_u32 s61, s61, s53\n\t"
      "buffer_load_dwordx4 v8, s[12:15], s61 offen lds\n\t"
      "s_waitcnt lgkmcnt(8)\n\t"
      "v_mfma_f32_32x32x16_bf16 v[128:143], v[80:83], a[104:107], v[128:143]\n\t"
      "v_mfma_f32_32x32x16_bf16 v[144:159], v[80:83], a[232:235], v[144:159]\n\t"
      "s_waitcnt vmcnt(8)\n\t"
      "s_waitcnt lgkmcnt(0)\n\t"
      "s_barrier\n\t"
      "ds_read_b128 a[112:115], v248 offset:32768\n\t"
      "ds_read_b128 a[240:243], v252 offset:32768\n\t"
      "ds_read_b64_tr_b16 v[96:97], v27 offset:32768\n\t"
      "ds_read_b64_tr_b16 v[98:99], v27 offset:33280\n\t"
      "ds_read_b64_tr_b16 v[100:101], v27 offset:40960\n\t"
      "ds_read_b64_tr_b16 v[102:103], v27 offset:41472\n\t"
      "v_mfma_f32_32x32x16_bf16 v[160:175], a[104:107], v[84:87], v[160:175]\n\t"
      "buffer_load_dwordx4 v9, s[12:15], s61 offen offset:1024 lds\n\t"
      "v_mfma_f32_32x32x16_bf16 v[176:191], a[232:235], v[84:87], v[176:191]\n\t"
      "ds_read_b128 a[116:119], v249 offset:32768\n\t"
      "ds_read_b128 a[244:247], v253 offset:32768\n\t"
      "ds_read_b64_tr_b16 v[104:105], v27 offset:34816\n\t"
      "ds_read_b64_tr_b16 v[106:107], v27 offset:35328\n\t"
      "v_mfma_f32_32x32x16_bf16 v[128:143], v[88:91], a[108:111], v[128:143]\n\t"
      "v_mfma_f32_32x32x16_bf16 v[144:159], v[88:91], a[236:239], v[144:159]\n\t"
      "s_add_u32 s60, s46, 3\n\t"
      "s_and_b32 s60, s60, 7\n\t"
      "s_mul_i32 s61, s60, 229376\n\t"
      "s_add_u32 m0, s57, 24576\n\t"
      "s_add_u32 s61, s61, s53\n\t"
      "buffer_load_dwordx4 v8, s[12:15], s61 offen lds\n\t"
      "ds_read_b64_tr_b16 v[108:109], v27 offset:43008\n\t"
      "ds_read_b64_tr_b16 v[110:111], v27 offset:43520\n\t"
      "v_mfma_f32_32x32x16_bf16 v[160:175], a[108:111], v[92:95], v[160:175]\n\t"
      "v_mfma_f32_32x32x16_bf16 v[176:191], a[236:239], v[92:95], v[176:191]\n\t"
      "ds_read_b128 a[120:123], v250 offset:32768\n\t"
      "ds_read_b128 a[248:251], v254 offset:32768\n\t"
      "ds_read_b64_tr_b16 v[112:113], v27 offset:36864\n\t"
      "ds_read_b64_tr_b16 v[114:115], v27 offset:37376\n\t"
      "s_waitcnt lgkmcnt(12)\n\t"
      "v_mfma_f32_32x32x16_bf16 v[128:143], v[96:99], a[112:115], v[128:143]\n\t"
      "buffer_load_dwordx4 v9, s[12:15], s61 offen offset:1024 lds\n\t"
      "v_mfma_f32_32x32x16_bf16 v[144:159], v[96:99], a[240:243], v[144:159]\n\t"
      "ds_read_b128 a[124:127], v251 offset:32768\n\t"
      "ds_read_b128 a[252:255], v255 offset:32768\n\t"
      "ds_read_b64_tr_b16 v[116:117], v27 offset:38912\n\t"
      "ds_read_b64_tr_b16 v[118:119], v27 offset:39424\n\t"
      "s_waitcnt lgkmcnt(14)\n\t"
      "v_mfma_f32_32x32x16_bf16 v[160:175], a[112:115], v[100:103], v[160:175]\n\t"
      "v_mfma_f32_32x32x16_bf16 v[176:191], a[240:243], v[100:103], v[176:191]\n\t"
      "s_add_u32 s60, s46, 4\n\t"
      "s_and_b32 s60, s60, 7\n\t"
      "s_mul_i32 s61, s60, 229376\n\t"
      "s_add_u32 m0, s57, 32768\n\t"
      "s_add_u32 s61, s61, s53\n\t"
      "buffer_load_dwordx4 v8, s[12:15], s61 offen lds\n\t"
      "ds_read_b64_tr_b16 v[120:121], v27 offset:45056\n\t"
      "ds_read_b64_tr_b16 v[122:123], v27 offset:45568\n\t"
      "s_waitcnt lgkmcnt(12)\n\t"
      "v_mfma_f32_32x32x16_bf16 v[128:143], v[104:107], a[116:119], v[128:143]\n\t"
      "v_mfma_f32_32x32x16_bf16 v[144:159], v[104:107], a[244:247], v[144:159]\n\t"
      "ds_read_b64_tr_b16 v[124:125], v27 offset:47104\n\t"
      "ds_read_b64_tr_b16 v[126:127], v27 offset:47616\n\t"
      "s_waitcnt lgkmcnt(0)\n\t"
      "s_barrier\n\t"
      "v_mov_b32 v2, v29\n\t"
      "v_add_u32 v3, 3072, v29\n\t"
      "v_add_u32 v4, 6144, v29\n\t"
      "v_add_u32 v5, 9216, v29\n\t"
      "v_mfma_f32_32x32x16_bf16 v[160:175], a[116:119], v[108:111], v[160:175]\n\t"
      "buffer_load_dwordx4 v9, s[12:15], s61 offen offset:1024 lds\n\t"
      "v_mfma_f32_32x32x16_bf16 v[176:191], a[244:247], v[108:111], v[176:191]\n\t"
      "v_mfma_f32_32x32x16_bf16 v[128:143], v[112:115], a[120:123], v[128:143]\n\t"
      "v_mfma_f32_32x32x16_bf16 v[144:159], v[112:115], a[248:251], v[144:159]\n\t"
      "s_add_u32 s60, s46, 5\n\t"
      "s_and_b32 s60, s60, 7\n\t"
      "s_mul_i32 s61, s60, 229376\n\t"
      "s_add_u32 m0, s57, 40960\n\t"
      "s_add_u32 s61, s61, s53\n\t"
      "buffer_load_dwordx4 v8, s[12:15], s61 offen lds\n\t"
      "v_mfma_f32_32x32x16_bf16 v[128:143], v[116:119], a[124:127], v[128:143]\n\t"
      "v_mfma_f32_32x32x16_bf16 v[144:159], v[116:119], a[252:255], v[144:159]\n\t"
      "v_mfma_f32_32x32x16_bf16 v[160:175], a[120:123], v[120:123], v[160:175]\n\t"
      "buffer_load_dwordx4 v9, s[12:15], s61 offen offset:1024 lds\n\t"
      "s_nop 0\n\t"
      "s_nop 0\n\t"
      "s_nop 6\n\t"
      "v_cvt_pk_bf16_f32 v240, v128, v129\n\t"
      "v_cvt_pk_bf16_f32 v241, v130, v131\n\t"
      "v_cvt_pk_bf16_f32 v242, v132, v133\n\t"
      "v_mfma_f32_32x32x16_bf16 v[176:191], a[248:251], v[120:123], v[176:191]\n\t"
      "v_cvt_pk_bf16_f32 v243, v134, v135\n\t"
      "s_nop 1\n\t"
      "v_mfma_f32_32x32x16_bf16 v[208:223], v[40:43], v[240:243], 0\n\t"
      "v_cvt_pk_bf16_f32 v244, v136, v137\n\t"
      "v_cvt_pk_bf16_f32 v245, v138, v139\n\t"
      "v_cvt_pk_bf16_f32 v246, v140, v141\n\t"
      "v_mfma_f32_32x32x16_bf16 v[160:175], a[124:127], v[124:127], v[160:175]\n\t"
      "v_cvt_pk_bf16_f32 v247, v142, v143\n\t"
      "s_nop 1\n\t"
      "v_mfma_f32_32x32x16_bf16 v[224:239], v[44:47], v[244:247], 0\n\t"
      "v_cvt_pk_bf16_f32 v248, v144, v145\n\t"
      "v_cvt_pk_bf16_f32 v249, v146, v147\n\t"
      "v_cvt_pk_bf16_f32 v250, v148, v149\n\t"
      "v_mfma_f32_32x32x16_bf16 v[176:191], a[252:255], v[124:127], v[176:191]\n\t"
      "s_add_u32 s60, s46, 6\n\t"
      "s_and_b32 s60, s60, 7\n\t"
      "s_mul_i32 s61, s60, 229376\n\t"
      "s_add_u32 m0, s57, 49152\n\t"
      "s_add_u32 s61, s61, s53\n\t"
      "buffer_load_dwordx4 v8, s[12:15], s61 offen lds\n\t"
      "v_cvt_pk_bf16_f32 v251, v150, v151\n\t"
      "v_cvt_pk_bf16_f32 v252, v152, v153\n\t"
      "v_cvt_pk_bf16_f32 v253, v154, v155\n\t"
      "v_cvt_pk_bf16_f32 v254, v156, v157\n\t"
      "v_cvt_pk_bf16_f32 v255, v158, v159\n\t"
      "v_mfma_f32_32x32x16_bf16 v[128:143], v[40:43], v[248:251], 0\n\t"
      "s_nop 0\n\t"
      "v_mfma_f32_32x32x16_bf16 v[144:159], v[44:47], v[252:255], 0\n\t"
      "v_max3_f32 v56, v208, v209, v210\n\t"
      "v_max3_f32 v56, v56, v211, v212\n\t"
      "v_max3_f32 v56, v56, v213, v214\n\t"
      "v_max3_f32 v56, v56, v215, v216\n\t"
      "v_max3_f32 v56, v56, v217, v218\n\t"
      "v_max3_f32 v56, v56, v219, v220\n\t"
      "v_max3_f32 v56, v56, v221, v222\n\t"
      "v_max_f32 v56, v56, v223\n\t"
      "v_mul_f32 v56, s38, v56\n\t"
      "v_pk_fma_f32 v[208:209], v[208:209], s[38:39], v[56:57] op_sel:[0,1,0] op_sel_hi:[1,1,0]\n\t"
      "v_pk_fma_f32 v[210:211], v[210:211], s[38:39], v[56:57] op_sel:[0,1,0] op_sel_hi:[1,1,0]\n\t"
      "v_pk_fma_f32 v[212:213], v[212:213], s[38:39], v[56:57] op_sel:[0,1,0] op_sel_hi:[1,1,0]\n\t"
      "buffer_load_dwordx4 v9, s[12:15], s61 offen offset:1024 lds\n\t"
      "v_pk_fma_f32 v[214:215], v[214:215], s[38:39], v[56:57] op_sel:[0,1,0] op_sel_hi:[1,1,0]\n\t"
      "v_pk_fma_f32 v[216:217], v[216:217], s[38:39], v[56:57] op_sel:[0,1,0] op_sel_hi:[1,1,0]\n\t"
      "v_pk_fma_f32 v[218:219], v[218:219], s[38:39], v[56:57] op_sel:[0,1,0] op_sel_hi:[1,1,0]\n\t"
      "v_pk_fma_f32 v[220:221], v[220:221], s[38:39], v[56:57] op_sel:[0,1,0] op_sel_hi:[1,1,0]\n\t"
      "v_pk_fma_f32 v[222:223], v[222:223], s[38:39], v[56:57] op_sel:[0,1,0] op_sel_hi:[1,1,0]\n\t"
      "v_exp_f32 v208, v208\n\t"
      "v_exp_f32 v209, v209\n\t"
      "v_exp_f32 v210, v210\n\t"
      "v_exp_f32 v211, v211\n\t"
      "v_exp_f32 v212, v212\n\t"
      "v_pk_add_f32 v[56:57], v[208:209], v[210:211]\n\t"
      "v_exp_f32 v213, v213\n\t"
      "v_exp_f32 v214, v214\n\t"
      "v_pk_add_f32 v[56:57], v[56:57], v[212:213]\n\t"
      "s_add_u32 s60, s46, 7\n\t"
      "s_and_b32 s60, s60, 7\n\t"
      "s_mul_i32 s61, s60, 229376\n\t"
      "s_add_u32 m0, s57, 57344\n\t"
      "s_add_u32 s61, s61, s53\n\t"
      "buffer_load_dwordx4 v8, s[12:15], s61 offen lds\n\t"
      "v_exp_f32 v215, v215\n\t"
      "v_exp_f32 v216, v216\n\t"
      "v_pk_add_f32 v[56:57], v[56:57], v[214:215]\n\t"
      "v_exp_f32 v217, v217\n\t"
      "v_exp_f32 v218, v218\n\t"
      "v_pk_add_f32 v[56:57], v[56:57], v[216:217]\n\t"
      "v_exp_f32 v219, v219\n\t"
      "v_exp_f32 v220, v220\n\t"
      "v_pk_add_f32 v[56:57], v[56:57], v[218:219]\n\t"
      "v_exp_f32 v221, v221\n\t"
      "v_exp_f32 v222, v222\n\t"
      "v_pk_add_f32 v[56:57], v[56:57], v[220:221]\n\t"
      "v_exp_f32 v223, v223\n\t"
      "s_nop 0\n\t"
      "v_pk_add_f32 v[56:57], v[56:57], v[222:223]\n\t"
      "buffer_load_dwordx4 v9, s[12:15], s61 offen offset:1024 lds\n\t"
      "v_add_f32 v56, v56, v57\n\t"
      "v_rcp_f32 v56, v56\n\t"
      "s_nop 0\n\t"
      "v_pk_mul_f32 v[208:209], v[208:209], v[56:57] op_sel_hi:[1,0]\n\t"
      "v_pk_mul_f32 v[210:211], v[210:211], v[56:57] op_sel_hi:[1,0]\n\t"
      "v_pk_mul_f32 v[212:213], v[212:213], v[56:57] op_sel_hi:[1,0]\n\t"
      "v_pk_mul_f32 v[214:215], v[214:215], v[56:57] op_sel_hi:[1,0]\n\t"
      "v_pk_mul_f32 v[216:217], v[216:217], v[56:57] op_sel_hi:[1,0]\n\t"
      "v_pk_mul_f32 v[218:219], v[218:219], v[56:57] op_sel_hi:[1,0]\n\t"
      "v_pk_mul_f32 v[220:221], v[220:221], v[56:57] op_sel_hi:[1,0]\n\t"
      "v_pk_mul_f32 v[222:223], v[222:223], v[56:57] op_sel_hi:[1,0]\n\t"
      "v_cvt_pk_bf16_f32 v64, v208, v209\n\t"
      "v_cvt_pk_bf16_f32 v65, v210, v211\n\t"
      "v_cvt_pk_bf16_f32 v66, v212, v213\n\t"
      "v_cvt_pk_bf16_f32 v67, v214, v215\n\t"
      "s_add_u32 s60, s74, 0\n\t"
      "s_lshl_b32 s60, s60, 16\n\t"
      "s_add_u32 m0, s58, 65536\n\t"
      "s_add_u32 s60, s60, s73\n\t"
      "buffer_load_dwordx4 v2, s[16:19], s60 offen lds\n\t"
      "v_cvt_pk_bf16_f32 v68, v216, v217\n\t"
      "v_cvt_pk_bf16_f32 v69, v218, v219\n\t"
      "v_cvt_pk_bf16_f32 v70, v220, v221\n\t"
      "v_cvt_pk_bf16_f32 v71, v222, v223\n\t"
      "ds_write_b128 v10, v[64:67] offset:0\n\t"
      "ds_write_b128 v11, v[68:71] offset:0\n\t"
      "v_cvt_pk_bf16_f32 v80, v160, v161\n\t"
      "v_cvt_pk_bf16_f32 v81, v162, v163\n\t"
      "v_cvt_pk_bf16_f32 v82, v164, v165\n\t"
      "v_cvt_pk_bf16_f32 v83, v166, v167\n\t"
      "v_cvt_pk_bf16_f32 v84, v168, v169\n\t"
      "v_cvt_pk_bf16_f32 v85, v170, v171\n\t"
      "v_cvt_pk_bf16_f32 v86, v172, v173\n\t"
      "buffer_load_dwordx4 v3, s[16:19], s60 offen offset:1024 lds\n\t"
      "v_cvt_pk_bf16_f32 v87, v174, v175\n\t"
      "v_cvt_pk_bf16_f32 v88, v176, v177\n\t"
      "v_cvt_pk_bf16_f32 v89, v178, v179\n\t"
      "v_cvt_pk_bf16_f32 v90, v180, v181\n\t"
      "v_cvt_pk_bf16_f32 v91, v182, v183\n\t"
      "v_cvt_pk_bf16_f32 v92, v184, v185\n\t"
      "v_cvt_pk_bf16_f32 v93, v186, v187\n\t"
      "v_cvt_pk_bf16_f32 v94, v188, v189\n\t"
      "v_cvt_pk_bf16_f32 v95, v190, v191\n\t"
      "ds_write_b128 v192, v[80:83] offset:0\n\t"
      "ds_write_b128 v192, v[84:87] offset:1024\n\t"
      "ds_write_b128 v193, v[88:91] offset:0\n\t"
      "ds_write_b128 v193, v[92:95] offset:1024\n\t"
      "v_max3_f32 v56, v224, v225, v226\n\t"
      "buffer_load_dwordx4 v4, s[16:19], s60 offen offset:2048 lds\n\t"
      "v_max3_f32 v56, v56, v227, v228\n\t"
      "v_max3_f32 v56, v56, v229, v230\n\t"
      "v_max3_f32 v56, v56, v231, v232\n\t"
      "v_max3_f32 v56, v56, v233, v234\n\t"
      "v_max3_f32 v56, v56, v235, v236\n\t"
      "v_max3_f32 v56, v56, v237, v238\n\t"
      "v_max_f32 v56, v56, v239\n\t"
      "v_mul_f32 v56, s38, v56\n\t"
      "v_pk_fma_f32 v[224:225], v[224:225], s[38:39], v[56:57] op_sel:[0,1,0] op_sel_hi:[1,1,0]\n\t"
      "v_pk_fma_f32 v[226:227], v[226:227], s[38:39], v[56:57] op_sel:[0,1,0] op_sel_hi:[1,1,0]\n\t"
      "v_pk_fma_f32 v[228:229], v[228:229], s[38:39], v[56:57] op_sel:[0,1,0] op_sel_hi:[1,1,0]\n\t"
      "v_pk_fma_f32 v[230:231], v[230:231], s[38:39], v[56:57] op_sel:[0,1,0] op_sel_hi:[1,1,0]\n\t"
      "v_pk_fma_f32 v[232:233], v[232:233], s[38:39], v[56:57] op_sel:[0,1,0] op_sel_hi:[1,1,0]\n\t"
      "v_pk_fma_f32 v[234:235], v[234:235], s[38:39], v[56:57] op_sel:[0,1,0] op_sel_hi:[1,1,0]\n\t"
      "buffer_load_dwordx4 v5, s[16:19], s60 offen offset:3072 lds\n\t"
      "v_pk_fma_f32 v[236:237], v[236:237], s[38:39], v[56:57] op_sel:[0,1,0] op_sel_hi:[1,1,0]\n\t"
      "v_pk_fma_f32 v[238:239], v[238:239], s[38:39], v[56:57] op_sel:[0,1,0] op_sel_hi:[1,1,0]\n\t"
      "v_exp_f32 v224, v224\n\t"
      "v_exp_f32 v225, v225\n\t"
      "v_exp_f32 v226, v226\n\t"
      "v_exp_f32 v227, v227\n\t"
      "v_exp_f32 v228, v228\n\t"
      "v_pk_add_f32 v[56:57], v[224:225], v[226:227]\n\t"
      "v_exp_f32 v229, v229\n\t"
      "v_exp_f32 v230, v230\n\t"
      "v_pk_add_f32 v[56:57], v[56:57], v[228:229]\n\t"
      "v_exp_f32 v231, v231\n\t"
      "v_exp_f32 v232, v232\n\t"
      "v_pk_add_f32 v[56:57], v[56:57], v[230:231]\n\t"
      "s_add_u32 s60, s74, 3\n\t"
      "s_lshl_b32 s60, s60, 16\n\t"
      "s_add_u32 m0, s58, 81920\n\t"
      "s_add_u32 s60, s60, s73\n\t"
      "buffer_load_dwordx4 v2, s[16:19], s60 offen lds\n\t"
      "v_exp_f32 v233, v233\n\t"
      "v_exp_f32 v234, v234\n\t"
      "v_pk_add_f32 v[56:57], v[56:57], v[232:233]\n\t"
      "v_exp_f32 v235, v235\n\t"
      "v_exp_f32 v236, v236\n\t"
      "v_pk_add_f32 v[56:57], v[56:57], v[234:235]\n\t"
      "v_exp_f32 v237, v237\n\t"
      "v_exp_f32 v238, v238\n\t"
      "v_pk_add_f32 v[56:57], v[56:57], v[236:237]\n\t"
      "v_exp_f32 v239, v239\n\t"
      "s_nop 0\n\t"
      "v_pk_add_f32 v[56:57], v[56:57], v[238:239]\n\t"
      "v_add_f32 v56, v56, v57\n\t"
      "v_rcp_f32 v56, v56\n\t"
      "s_nop 0\n\t"
      "v_pk_mul_f32 v[224:225], v[224:225], v[56:57] op_sel_hi:[1,0]\n\t"
      "buffer_load_dwordx4 v3, s[16:19], s60 offen offset:1024 lds\n\t"
      "v_pk_mul_f32 v[226:227], v[226:227], v[56:57] op_sel_hi:[1,0]\n\t"
      "v_pk_mul_f32 v[228:229], v[228:229], v[56:57] op_sel_hi:[1,0]\n\t"
      "v_pk_mul_f32 v[230:231], v[230:231], v[56:57] op_sel_hi:[1,0]\n\t"
      "v_pk_mul_f32 v[232:233], v[232:233], v[56:57] op_sel_hi:[1,0]\n\t"
      "v_pk_mul_f32 v[234:235], v[234:235], v[56:57] op_sel_hi:[1,0]\n\t"
      "v_pk_mul_f32 v[236:237], v[236:237], v[56:57] op_sel_hi:[1,0]\n\t"
      "v_pk_mul_f32 v[238:239], v[238:239], v[56:57] op_sel_hi:[1,0]\n\t"
      "v_cvt_pk_bf16_f32 v48, v224, v225\n\t"
      "v_cvt_pk_bf16_f32 v49, v226, v227\n\t"
      "v_cvt_pk_bf16_f32 v50, v228, v229\n\t"
      "v_cvt_pk_bf16_f32 v51, v230, v231\n\t"
      "v_cvt_pk_bf16_f32 v52, v232, v233\n\t"
      "v_cvt_pk_bf16_f32 v53, v234, v235\n\t"
      "v_cvt_pk_bf16_f32 v54, v236, v237\n\t"
      "buffer_load_dwordx4 v4, s[16:19], s60 offen offset:2048 lds\n\t"
      "v_cvt_pk_bf16_f32 v55, v238, v239\n\t"
      "ds_write_b128 v10, v[48:51] offset:8192\n\t"
      "ds_write_b128 v11, v[52:55] offset:8192\n\t"
      "v_mov_b32 v226, v62\n\t"
      "v_mov_b32 v227, v63\n\t"
      "v_mov_b32 v230, v62\n\t"
      "v_mov_b32 v231, v63\n\t"
      "v_mov_b32 v234, v62\n\t"
      "v_mov_b32 v235, v63\n\t"
      "v_mov_b32 v238, v62\n\t"
      "v_mov_b32 v239, v63\n\t"
      "v_max3_f32 v56, v128, v129, v130\n\t"
      "v_max3_f32 v56, v56, v131, v132\n\t"
      "buffer_load_dwordx4 v5, s[16:19], s60 offen offset:3072 lds\n\t"
      "v_max3_f32 v56, v56, v133, v134\n\t"
      "v_max3_f32 v56, v56, v135, v136\n\t"
      "v_max3_f32 v56, v56, v137, v138\n\t"
      "v_max3_f32 v56, v56, v139, v140\n\t"
      "v_max3_f32 v56, v56, v141, v142\n\t"
      "v_max_f32 v56, v56, v143\n\t"
      "v_mul_f32 v56, s38, v56\n\t"
      "v_pk_fma_f32 v[128:129], v[128:129], s[38:39], v[56:57] op_sel:[0,1,0] op_sel_hi:[1,1,0]\n\t"
      "v_pk_fma_f32 v[130:131], v[130:131], s[38:39], v[56:57] op_sel:[0,1,0] op_sel_hi:[1,1,0]\n\t"
      "v_pk_fma_f32 v[132:133], v[132:133], s[38:39], v[56:57] op_sel:[0,1,0] op_sel_hi:[1,1,0]\n\t"
      "v_pk_fma_f32 v[134:135], v[134:135], s[38:39], v[56:57] op_sel:[0,1,0] op_sel_hi:[1,1,0]\n\t"
      "v_pk_fma_f32 v[136:137], v[136:137], s[38:39], v[56:57] op_sel:[0,1,0] op_sel_hi:[1,1,0]\n\t"
      "v_pk_fma_f32 v[138:139], v[138:139], s[38:39], v[56:57] op_sel:[0,1,0] op_sel_hi:[1,1,0]\n\t"
      "v_pk_fma_f32 v[140:141], v[140:141], s[38:39], v[56:57] op_sel:[0,1,0] op_sel_hi:[1,1,0]\n\t"
      "s_add_u32 s60, s74, 1\n\t"
      "s_lshl_b32 s60, s60, 16\n\t"
      "s_add_u32 m0, s58, 98304\n\t"
      "s_add_u32 s60, s60, s73\n\t"
      "buffer_load_dwordx4 v2, s[16:19], s60 offen lds\n\t"
      "v_pk_fma_f32 v[142:143], v[142:143], s[38:39], v[56:57] op_sel:[0,1,0] op_sel_hi:[1,1,0]\n\t"
      "v_exp_f32 v128, v128\n\t"
      "v_exp_f32 v129, v129\n\t"
      "v_exp_f32 v130, v130\n\t"
      "v_exp_f32 v131, v131\n\t"
      "v_exp_f32 v132, v132\n\t"
      "v_pk_add_f32 v[56:57], v[128:129], v[130:131]\n\t"
      "v_exp_f32 v133, v133\n\t"
      "v_exp_f32 v134, v134\n\t"
      "v_pk_add_f32 v[56:57], v[56:57], v[132:133]\n\t"
      "v_exp_f32 v135, v135\n\t"
      "v_exp_f32 v136, v136\n\t"
      "v_pk_add_f32 v[56:57], v[56:57], v[134:135]\n\t"
      "v_exp_f32 v137, v137\n\t"
      "buffer_load_dwordx4 v3, s[16:19], s60 offen offset:1024 lds\n\t"
      "v_exp_f32 v138, v138\n\t"
      "v_pk_add_f32 v[56:57], v[56:57], v[136:137]\n\t"
      "v_exp_f32 v139, v139\n\t"
      "v_exp_f32 v140, v140\n\t"
      "v_pk_add_f32 v[56:57], v[56:57], v[138:139]\n\t"
      "v_exp_f32 v141, v141\n\t"
      "v_exp_f32 v142, v142\n\t"
      "v_pk_add_f32 v[56:57], v[56:57], v[140:141]\n\t"
      "v_exp_f32 v143, v143\n\t"
      "s_nop 0\n\t"
      "v_pk_add_f32 v[56:57], v[56:57], v[142:143]\n\t"
      "v_add_f32 v56, v56, v57\n\t"
      "v_rcp_f32 v56, v56\n\t"
      "s_nop 0\n\t"
      "v_pk_mul_f32 v[128:129], v[128:129], v[56:57] op_sel_hi:[1,0]\n\t"
      "v_pk_mul_f32 v[130:131], v[130:131], v[56:57] op_sel_hi:[1,0]\n\t"
      "buffer_load_dwordx4 v4, s[16:19], s60 offen offset:2048 lds\n\t"
      "v_pk_mul_f32 v[132:133], v[132:133], v[56:57] op_sel_hi:[1,0]\n\t"
      "v_pk_mul_f32 v[134:135], v[134:135], v[56:57] op_sel_hi:[1,0]\n\t"
      "v_pk_mul_f32 v[136:137], v[136:137], v[56:57] op_sel_hi:[1,0]\n\t"
      "v_pk_mul_f32 v[138:139], v[138:139], v[56:57] op_sel_hi:[1,0]\n\t"
      "v_pk_mul_f32 v[140:141], v[140:141], v[56:57] op_sel_hi:[1,0]\n\t"
      "v_pk_mul_f32 v[142:143], v[142:143], v[56:57] op_sel_hi:[1,0]\n\t"
      "v_cvt_pk_bf16_f32 v64, v128, v129\n\t"
      "v_cvt_pk_bf16_f32 v65, v130, v131\n\t"
      "v_cvt_pk_bf16_f32 v66, v132, v133\n\t"
      "v_cvt_pk_bf16_f32 v67, v134, v135\n\t"
      "v_cvt_pk_bf16_f32 v68, v136, v137\n\t"
      "v_cvt_pk_bf16_f32 v69, v138, v139\n\t"
      "v_cvt_pk_bf16_f32 v70, v140, v141\n\t"
      "v_cvt_pk_bf16_f32 v71, v142, v143\n\t"
      "buffer_load_dwordx4 v5, s[16:19], s60 offen offset:3072 lds\n\t"
      "ds_write_b128 v12, v[64:67] offset:0\n\t"
      "ds_write_b128 v13, v[68:71] offset:0\n\t"
      "v_max3_f32 v56, v144, v145, v146\n\t"
      "v_max3_f32 v56, v56, v147, v148\n\t"
      "v_max3_f32 v56, v56, v149, v150\n\t"
      "v_max3_f32 v56, v56, v151, v152\n\t"
      "v_max3_f32 v56, v56, v153, v154\n\t"
      "v_max3_f32 v56, v56, v155, v156\n\t"
      "v_max3_f32 v56, v56, v157, v158\n\t"
      "v_max_f32 v56, v56, v159\n\t"
      "v_mul_f32 v56, s38, v56\n\t"
      "v_pk_fma_f32 v[144:145], v[144:145], s[38:39], v[56:57] op_sel:[0,1,0] op_sel_hi:[1,1,0]\n\t"
      "v_pk_fma_f32 v[146:147], v[146:147], s[38:39], v[56:57] op_sel:[0,1,0] op_sel_hi:[1,1,0]\n\t"
      "v_pk_fma_f32 v[148:149], v[148:149], s[38:39], v[56:57] op_sel:[0,1,0] op_sel_hi:[1,1,0]\n\t"
      "v_pk_fma_f32 v[150:151], v[150:151], s[38:39], v[56:57] op_sel:[0,1,0] op_sel_hi:[1,1,0]\n\t"
      "v_pk_fma_f32 v[152:153], v[152:153], s[38:39], v[56:57] op_sel:[0,1,0] op_sel_hi:[1,1,0]\n\t"
      "v_pk_fma_f32 v[154:155], v[154:155], s[38:39], v[56:57] op_sel:[0,1,0] op_sel_hi:[1,1,0]\n\t"
      "v_pk_fma_f32 v[156:157], v[156:157], s[38:39], v[56:57] op_sel:[0,1,0] op_sel_hi:[1,1,0]\n\t"
      "v_pk_fma_f32 v[158:159], v[158:159], s[38:39], v[56:57] op_sel:[0,1,0] op_sel_hi:[1,1,0]\n\t"
      "v_exp_f32 v144, v144\n\t"
      "v_exp_f32 v145, v145\n\t"
      "v_exp_f32 v146, v146\n\t"
      "v_exp_f32 v147, v147\n\t"
      "v_exp_f32 v148, v148\n\t"
      "v_pk_add_f32 v[56:57], v[144:145], v[146:147]\n\t"
      "v_exp_f32 v149, v149\n\t"
      "v_exp_f32 v150, v150\n\t"
      "v_pk_add_f32 v[56:57], v[56:57], v[148:149]\n\t"
      "v_exp_f32 v151, v151\n\t"
      "v_exp_f32 v152, v152\n\t"
      "v_pk_add_f32 v[56:57], v[56:57], v[150:151]\n\t"
      "v_exp_f32 v153, v153\n\t"
      "v_exp_f32 v154, v154\n\t"
      "v_pk_add_f32 v[56:57], v[56:57], v[152:153]\n\t"
      "v_exp_f32 v155, v155\n\t"
      "v_exp_f32 v156, v156\n\t"
      "v_pk_add_f32 v[56:57], v[56:57], v[154:155]\n\t"
      "v_exp_f32 v157, v157\n\t"
      "v_exp_f32 v158, v158\n\t"
      "v_pk_add_f32 v[56:57], v[56:57], v[156:157]\n\t"
      "v_exp_f32 v159, v159\n\t"
      "s_nop 0\n\t"
      "v_pk_add_f32 v[56:57], v[56:57], v[158:159]\n\t"
      "v_add_f32 v56, v56, v57\n\t"
      "v_rcp_f32 v56, v56\n\t"
      "s_nop 0\n\t"
      "v_pk_mul_f32 v[144:145], v[144:145], v[56:57] op_sel_hi:[1,0]\n\t"
      "v_pk_mul_f32 v[146:147], v[146:147], v[56:57] op_sel_hi:[1,0]\n\t"
      "v_pk_mul_f32 v[148:149], v[148:149], v[56:57] op_sel_hi:[1,0]\n\t"
      "v_pk_mul_f32 v[150:151], v[150:151], v[56:57] op_sel_hi:[1,0]\n\t"
      "v_pk_mul_f32 v[152:153], v[152:153], v[56:57] op_sel_hi:[1,0]\n\t"
      "v_pk_mul_f32 v[154:155], v[154:155], v[56:57] op_sel_hi:[1,0]\n\t"
      "v_pk_mul_f32 v[156:157], v[156:157], v[56:57] op_sel_hi:[1,0]\n\t"
      "v_pk_mul_f32 v[158:159], v[158:159], v[56:57] op_sel_hi:[1,0]\n\t"
      "v_cvt_pk_bf16_f32 v48, v144, v145\n\t"
      "v_cvt_pk_bf16_f32 v49, v146, v147\n\t"
      "v_cvt_pk_bf16_f32 v50, v148, v149\n\t"
      "v_cvt_pk_bf16_f32 v51, v150, v151\n\t"
      "v_cvt_pk_bf16_f32 v52, v152, v153\n\t"
      "v_cvt_pk_bf16_f32 v53, v154, v155\n\t"
      "v_cvt_pk_bf16_f32 v54, v156, v157\n\t"
      "v_cvt_pk_bf16_f32 v55, v158, v159\n\t"
      "ds_write_b128 v12, v[48:51] offset:8192\n\t"
      "ds_write_b128 v13, v[52:55] offset:8192\n\t"
      "s_add_u32 s64, s84, 0\n\t"
      "s_add_u32 s65, s84, 16384\n\t"
      "s_add_u32 s66, s84, 32768\n\t"
      "s_add_u32 s67, s84, 49152\n\t"
      "s_waitcnt vmcnt(20)\n\t"
      "s_waitcnt lgkmcnt(0)\n\t"
      "s_barrier\n\t"
      "ds_read_b64_tr_b16 v[128:129], v194 offset:0\n\t"
      "ds_read_b64_tr_b16 v[130:131], v194 offset:512\n\t"
      "ds_read_b128 v[132:135], v195 offset:0\n\t"
      "ds_read_b64_tr_b16 v[136:137], v194 offset:1024\n\t"
      "ds_read_b64_tr_b16 v[138:139], v194 offset:1536\n\t"
      "ds_read_b128 v[140:143], v195 offset:1024\n\t"
      "ds_read_b64_tr_b16 v[144:145], v194 offset:2048\n\t"
      "ds_read_b64_tr_b16 v[146:147], v194 offset:2560\n\t"
      "ds_read_b128 v[148:151], v195 offset:2048\n\t"
      "ds_read_b64_tr_b16 v[152:153], v194 offset:3072\n\t"
      "ds_read_b64_tr_b16 v[154:155], v194 offset:3584\n\t"
      "ds_read_b128 v[156:159], v195 offset:3072\n\t"
      "s_waitcnt lgkmcnt(9)\n\t"
      "v_cndmask_b32_e64 v132, v196, v132, s[82:83]\n\t"
      "v_cndmask_b32_e64 v133, v196, v133, s[82:83]\n\t"
      "v_cndmask_b32_e64 v134, v196, v134, s[82:83]\n\t"
      "v_cndmask_b32_e64 v135, v196, v135, s[82:83]\n\t"
      "s_nop 1\n\t"
      "v_mfma_f32_32x32x16_bf16 v[208:223], v[128:131], v[132:135], 0\n\t"
      "ds_read_b64_tr_b16 v[160:161], v194 offset:4096\n\t"
      "ds_read_b64_tr_b16 v[162:163], v194 offset:4608\n\t"
      "ds_read_b128 v[164:167], v195 offset:4096\n\t"
      "s_waitcnt lgkmcnt(9)\n\t"
      "v_cndmask_b32_e64 v140, v196, v140, s[82:83]\n\t"
      "v_cndmask_b32_e64 v141, v196, v141, s[82:83]\n\t"
      "v_cndmask_b32_e64 v142, v196, v142, s[82:83]\n\t"
      "v_cndmask_b32_e64 v143, v196, v143, s[82:83]\n\t"
      "s_nop 1\n\t"
      "v_mfma_f32_32x32x16_bf16 v[208:223], v[136:139], v[140:143], v[208:223]\n\t"
      "ds_read_b64_tr_b16 v[168:169], v194 offset:5120\n\t"
      "ds_read_b64_tr_b16 v[170:171], v194 offset:5632\n\t"
      "ds_read_b128 v[172:175], v195 offset:5120\n\t"
      "s_waitcnt lgkmcnt(9)\n\t"
      "v_cndmask_b32_e64 v148, v196, v148, s[82:83]\n\t"
      "v_cndmask_b32_e64 v149, v196, v149, s[82:83]\n\t"
      "v_cndmask_b32_e64 v150, v196, v150, s[82:83]\n\t"
      "v_cndmask_b32_e64 v151, v196, v151, s[82:83]\n\t"
      "s_nop 1\n\t"
      "v_mfma_f32_32x32x16_bf16 v[208:223], v[144:147], v[148:151], v[208:223]\n\t"
      "ds_read_b64_tr_b16 v[176:177], v194 offset:6144\n\t"
      "ds_read_b64_tr_b16 v[178:179], v194 offset:6656\n\t"
      "ds_read_b128 v[180:183], v195 offset:6144\n\t"
      "s_waitcnt lgkmcnt(9)\n\t"
      "v_cndmask_b32_e64 v156, v196, v156, s[82:83]\n\t"
      "v_cndmask_b32_e64 v157, v196, v157, s[82:83]\n\t"
      "v_cndmask_b32_e64 v158, v196, v158, s[82:83]\n\t"
      "v_cndmask_b32_e64 v159, v196, v159, s[82:83]\n\t"
      "s_nop 1\n\t"
      "v_mfma_f32_32x32x16_bf16 v[208:223], v[152:155], v[156:159], v[208:223]\n\t"
      "ds_read_b64_tr_b16 v[184:185], v194 offset:7168\n\t"
      "ds_read_b64_tr_b16 v[186:187], v194 offset:7680\n\t"
      "ds_read_b128 v[188:191], v195 offset:7168\n\t"
      "s_waitcnt lgkmcnt(9)\n\t"
      "v_cndmask_b32_e64 v164, v196, v164, s[82:83]\n\t"
      "v_cndmask_b32_e64 v165, v196, v165, s[82:83]\n\t"
      "v_cndmask_b32_e64 v166, v196, v166, s[82:83]\n\t"
      "v_cndmask_b32_e64 v167, v196, v167, s[82:83]\n\t"
      "s_nop 1\n\t"
      "v_mfma_f32_32x32x16_bf16 v[208:223], v[160:163], v[164:167], v[208:223]\n\t"
      "s_waitcnt lgkmcnt(6)\n\t"
      "v_cndmask_b32_e64 v172, v196, v172, s[82:83]\n\t"
      "v_cndmask_b32_e64 v173, v196, v173, s[82:83]\n\t"
      "v_cndmask_b32_e64 v174, v196, v174, s[82:83]\n\t"
      "v_cndmask_b32_e64 v175, v196, v175, s[82:83]\n\t"
      "s_nop 1\n\t"
      "v_mfma_f32_32x32x16_bf16 v[208:223], v[168:171], v[172:175], v[208:223]\n\t"
      "s_waitcnt lgkmcnt(3)\n\t"
      "v_cndmask_b32_e64 v180, v196, v180, s[82:83]\n\t"
      "v_cndmask_b32_e64 v181, v196, v181, s[82:83]\n\t"
      "v_cndmask_b32_e64 v182, v196, v182, s[82:83]\n\t"
      "v_cndmask_b32_e64 v183, v196, v183, s[82:83]\n\t"
      "s_nop 1\n\t"
      "v_mfma_f32_32x32x16_bf16 v[208:223], v[176:179], v[180:183], v[208:223]\n\t"
      "s_waitcnt lgkmcnt(0)\n\t"
      "v_cndmask_b32_e64 v188, v196, v188, s[82:83]\n\t"
      "v_cndmask_b32_e64 v189, v196, v189, s[82:83]\n\t"
      "v_cndmask_b32_e64 v190, v196, v190, s[82:83]\n\t"
      "v_cndmask_b32_e64 v191, v196, v191, s[82:83]\n\t"
      "s_nop 1\n\t"
      "v_mfma_f32_32x32x16_bf16 v[208:223], v[184:187], v[188:191], v[208:223]\n\t"
      "ds_read_b64_tr_b16 v[64:65], v26 offset:0\n\t"
      "ds_read_b64_tr_b16 v[66:67], v26 offset:512\n\t"
      "ds_read_b64_tr_b16 v[68:69], v26 offset:2048\n\t"
      "ds_read_b64_tr_b16 v[70:71], v26 offset:2560\n\t"
      "ds_read_b64_tr_b16 v[72:73], v26 offset:4096\n\t"
      "ds_read_b64_tr_b16 v[74:75], v26 offset:4608\n\t"
      "ds_read_b64_tr_b16 v[76:77], v26 offset:6144\n\t"
      "ds_read_b64_tr_b16 v[78:79], v26 offset:6656\n\t"
      "ds_read_b64_tr_b16 v[80:81], v26 offset:8192\n\t"
      "ds_read_b64_tr_b16 v[82:83], v26 offset:8704\n\t"
      "s_nop 1\n\t"
      "v_cvt_pk_bf16_f32 v224, v208, v209\n\t"
      "v_cvt_pk_bf16_f32 v225, v210, v211\n\t"
      "v_cvt_pk_bf16_f32 v228, v212, v213\n\t"
      "v_cvt_pk_bf16_f32 v229, v214, v215\n\t"
      "v_cvt_pk_bf16_f32 v232, v216, v217\n\t"
      "v_cvt_pk_bf16_f32 v233, v218, v219\n\t"
      "v_cvt_pk_bf16_f32 v236, v220, v221\n\t"
      "v_cvt_pk_bf16_f32 v237, v222, v223\n\t"
      "s_mov_b64 exec, s[50:51]\n\t"
      "buffer_store_dwordx4 v[224:227], v1, s[24:27], s64 offen sc1\n\t"
      "buffer_store_dwordx4 v[228:231], v1, s[24:27], s65 offen sc1\n\t"
      "buffer_store_dwordx4 v[232:235], v1, s[24:27], s66 offen sc1\n\t"
      "buffer_store_dwordx4 v[236:239], v1, s[24:27], s67 offen sc1\n\t"
      "s_mov_b64 exec, -1\n\t"
      "s_waitcnt lgkmcnt(8)\n\t"
      "v_mfma_f32_32x32x16_bf16 v[128:143], v[64:67], a[0:3], 0\n\t"
      "v_mfma_f32_32x32x16_bf16 v[144:159], v[64:67], a[128:131], 0\n\t"
      "s_waitcnt lgkmcnt(6)\n\t"
      "v_mfma_f32_32x32x16_bf16 v[128:143], v[68:71], a[4:7], v[128:143]\n\t"
      "v_mfma_f32_32x32x16_bf16 v[144:159], v[68:71], a[132:135], v[144:159]\n\t"
      "s_waitcnt lgkmcnt(0)\n\t"
      "s_barrier\n\t"
      "ds_read_b64_tr_b16 v[84:85], v26 offset:10240\n\t"
      "ds_read_b64_tr_b16 v[86:87], v26 offset:10752\n\t"
      "ds_read_b64_tr_b16 v[88:89], v26 offset:12288\n\t"
      "ds_read_b64_tr_b16 v[90:91], v26 offset:12800\n\t"
      "v_mfma_f32_32x32x16_bf16 v[128:143], v[72:75], a[8:11], v[128:143]\n\t"
      "s_add_u32 s60, s74, 4\n\t"
      "s_lshl_b32 s60, s60, 16\n\t"
      "s_add_u32 m0, s58, 114688\n\t"
      "s_add_u32 s60, s60, s73\n\t"
      "buffer_load_dwordx4 v2, s[16:19], s60 offen lds\n\t"
      "v_mfma_f32_32x32x16_bf16 v[144:159], v[72:75], a[136:139], v[144:159]\n\t"
      "ds_read_b64_tr_b16 v[92:93], v26 offset:14336\n\t"
      "ds_read_b64_tr_b16 v[94:95], v26 offset:14848\n\t"
      "v_mfma_f32_32x32x16_bf16 v[128:143], v[76:79], a[12:15], v[128:143]\n\t"
      "buffer_load_dwordx4 v3, s[16:19], s60 offen offset:1024 lds\n\t"
      "v_mfma_f32_32x32x16_bf16 v[144:159], v[76:79], a[140:143], v[144:159]\n\t"
      "ds_read_b64_tr_b16 v[96:97], v26 offset:16384\n\t"
      "ds_read_b64_tr_b16 v[98:99], v26 offset:16896\n\t"
      "v_mfma_f32_32x32x16_bf16 v[128:143], v[80:83], a[16:19], v[128:143]\n\t"
      "buffer_load_dwordx4 v4, s[16:19], s60 offen offset:2048 lds\n\t"
      "v_mfma_f32_32x32x16_bf16 v[144:159], v[80:83], a[144:147], v[144:159]\n\t"
      "ds_read_b64_tr_b16 v[100:101], v26 offset:18432\n\t"
      "ds_read_b64_tr_b16 v[102:103], v26 offset:18944\n\t"
      "s_waitcnt lgkmcnt(8)\n\t"
      "v_mfma_f32_32x32x16_bf16 v[128:143], v[84:87], a[20:23], v[128:143]\n\t"
      "buffer_load_dwordx4 v5, s[16:19], s60 offen offset:3072 lds\n\t"
      "v_mfma_f32_32x32x16_bf16 v[144:159], v[84:87], a[148:151], v[144:159]\n\t"
      "ds_read_b64_tr_b16 v[104:105], v26 offset:20480\n\t"
      "ds_read_b64_tr_b16 v[106:107], v26 offset:20992\n\t"
      "s_waitcnt lgkmcnt(8)\n\t"
      "v_mfma_f32_32x32x16_bf16 v[128:143], v[88:91], a[24:27], v[128:143]\n\t"
      "s_add_u32 s60, s74, 2\n\t"
      "s_lshl_b32 s60, s60, 16\n\t"
      "s_add_u32 m0, s58, 131072\n\t"
      "s_add_u32 s60, s60, s73\n\t"
      "buffer_load_dwordx4 v2, s[16:19], s60 offen lds\n\t"
      "v_mfma_f32_32x32x16_bf16 v[144:159], v[88:91], a[152:155], v[144:159]\n\t"
      "ds_read_b64_tr_b16 v[108:109], v26 offset:22528\n\t"
      "ds_read_b64_tr_b16 v[110:111], v26 offset:23040\n\t"
      "s_waitcnt lgkmcnt(8)\n\t"
      "v_mfma_f32_32x32x16_bf16 v[128:143], v[92:95], a[28:31], v[128:143]\n\t"
      "buffer_load_dwordx4 v3, s[16:19], s60 offen offset:1024 lds\n\t"
      "v_mfma_f32_32x32x16_bf16 v[144:159], v[92:95], a[156:159], v[144:159]\n\t"
      "ds_read_b64_tr_b16 v[112:113], v26 offset:24576\n\t"
      "ds_read_b64_tr_b16 v[114:115], v26 offset:25088\n\t"
      "s_waitcnt lgkmcnt(8)\n\t"
      "v_mfma_f32_32x32x16_bf16 v[128:143], v[96:99], a[32:35], v[128:143]\n\t"
      "buffer_load_dwordx4 v4, s[16:19], s60 offen offset:2048 lds\n\t"
      "v_mfma_f32_32x32x16_bf16 v[144:159], v[96:99], a[160:163], v[144:159]\n\t"
      "ds_read_b64_tr_b16 v[116:117], v26 offset:26624\n\t"
      "ds_read_b64_tr_b16 v[118:119], v26 offset:27136\n\t"
      "s_waitcnt lgkmcnt(8)\n\t"
      "v_mfma_f32_32x32x16_bf16 v[128:143], v[100:103], a[36:39], v[128:143]\n\t"
      "buffer_load_dwordx4 v5, s[16:19], s60 offen offset:3072 lds\n\t"
      "v_mfma_f32_32x32x16_bf16 v[144:159], v[100:103], a[164:167], v[144:159]\n\t"
      "ds_read_b64_tr_b16 v[120:121], v26 offset:28672\n\t"
      "ds_read_b64_tr_b16 v[122:123], v26 offset:29184\n\t"
      "s_waitcnt lgkmcnt(8)\n\t"
      "v_mfma_f32_32x32x16_bf16 v[128:143], v[104:107], a[40:43], v[128:143]\n\t"
      "s_add_u32 s60, s75, 0\n\t"
      "s_lshl_b32 s60, s60, 16\n\t"
      "s_add_u32 m0, s58, 49152\n\t"
      "s_add_u32 s60, s60, s73\n\t"
      "buffer_load_dwordx4 v2, s[16:19], s60 offen lds\n\t"
      "v_mfma_f32_32x32x16_bf16 v[144:159], v[104:107], a[168:171], v[144:159]\n\t"
      "ds_read_b64_tr_b16 v[124:125], v26 offset:30720\n\t"
      "ds_read_b64_tr_b16 v[126:127], v26 offset:31232\n\t"
      "s_waitcnt lgkmcnt(8)\n\t"
      "v_mfma_f32_32x32x16_bf16 v[128:143], v[108:111], a[44:47], v[128:143]\n\t"
      "buffer_load_dwordx4 v3, s[16:19], s60 offen offset:1024 lds\n\t"
      "v_mfma_f32_32x32x16_bf16 v[144:159], v[108:111], a[172:175], v[144:159]\n\t"
      "s_waitcnt lgkmcnt(6)\n\t"
      "v_mfma_f32_32x32x16_bf16 v[128:143], v[112:115], a[48:51], v[128:143]\n\t"
      "s_add_u32 s78, s36, 4096\n\t"
      "buffer_load_dwordx4 v[240:243], v198, s[24:27], s36 offen sc1\n\t"
      "buffer_load_dwordx4 v[244:247], v198, s[24:27], s78 offen sc1\n\t"
      "buffer_load_dwordx4 v[248:251], v199, s[24:27], s36 offen sc1\n\t"
      "buffer_load_dwordx4 v[252:255], v199, s[24:27], s78 offen sc1\n\t"
      "v_mfma_f32_32x32x16_bf16 v[144:159], v[112:115], a[176:179], v[144:159]\n\t"
      "s_waitcnt vmcnt(30)\n\t"
      "s_waitcnt lgkmcnt(0)\n\t"
      "s_barrier\n\t"
      "ds_read_b64_tr_b16 v[64:65], v26 offset:32768\n\t"
      "ds_read_b64_tr_b16 v[66:67], v26 offset:33280\n\t"
      "ds_read_b64_tr_b16 v[68:69], v26 offset:34816\n\t"
      "ds_read_b64_tr_b16 v[70:71], v26 offset:35328\n\t"
      "v_mfma_f32_32x32x16_bf16 v[128:143], v[116:119], a[52:55], v[128:143]\n\t"
      "buffer_load_dwordx4 v4, s[16:19], s60 offen offset:2048 lds\n\t"
      "v_mfma_f32_32x32x16_bf16 v[144:159], v[116:119], a[180:183], v[144:159]\n\t"
      "ds_read_b64_tr_b16 v[72:73], v26 offset:36864\n\t"
      "ds_read_b64_tr_b16 v[74:75], v26 offset:37376\n\t"
      "v_mfma_f32_32x32x16_bf16 v[128:143], v[120:123], a[56:59], v[128:143]\n\t"
      "buffer_load_dwordx4 v5, s[16:19], s60 offen offset:3072 lds\n\t"
      "v_mfma_f32_32x32x16_bf16 v[144:159], v[120:123], a[184:187], v[144:159]\n\t"
      "ds_read_b64_tr_b16 v[76:77], v26 offset:38912\n\t"
      "ds_read_b64_tr_b16 v[78:79], v26 offset:39424\n\t"
      "v_mfma_f32_32x32x16_bf16 v[128:143], v[124:127], a[60:63], v[128:143]\n\t"
      "v_mfma_f32_32x32x16_bf16 v[144:159], v[124:127], a[188:191], v[144:159]\n\t"
      "ds_read_b64_tr_b16 v[80:81], v26 offset:40960\n\t"
      "ds_read_b64_tr_b16 v[82:83], v26 offset:41472\n\t"
      "s_waitcnt lgkmcnt(8)\n\t"
      "v_mfma_f32_32x32x16_bf16 v[128:143], v[64:67], a[64:67], v[128:143]\n\t"
      "s_mov_b32 s78, 131072\n\t"
      "s_mov_b32 exec_lo, 16776975\n\t"
      "s_mov_b32 exec_hi, 0\n\t"
      "buffer_load_dwordx2 v[36:37], v38, s[24:27], s78 offen sc1\n\t"
      "s_mov_b64 exec, -1\n\t"
      "v_mfma_f32_32x32x16_bf16 v[144:159], v[64:67], a[192:195], v[144:159]\n\t"
      "ds_read_b64_tr_b16 v[84:85], v26 offset:43008\n\t"
      "ds_read_b64_tr_b16 v[86:87], v26 offset:43520\n\t"
      "s_waitcnt lgkmcnt(8)\n\t"
      "v_mfma_f32_32x32x16_bf16 v[128:143], v[68:71], a[68:71], v[128:143]\n\t"
      "v_mfma_f32_32x32x16_bf16 v[144:159], v[68:71], a[196:199], v[144:159]\n\t"
      "ds_read_b64_tr_b16 v[88:89], v26 offset:45056\n\t"
      "ds_read_b64_tr_b16 v[90:91], v26 offset:45568\n\t"
      "s_waitcnt lgkmcnt(8)\n\t"
      "v_mfma_f32_32x32x16_bf16 v[128:143], v[72:75], a[72:75], v[128:143]\n\t"
      "v_mfma_f32_32x32x16_bf16 v[144:159], v[72:75], a[200:203], v[144:159]\n\t"
      "ds_read_b64_tr_b16 v[92:93], v26 offset:47104\n\t"
      "ds_read_b64_tr_b16 v[94:95], v26 offset:47616\n\t"
      "s_waitcnt lgkmcnt(8)\n\t"
      "v_mfma_f32_32x32x16_bf16 v[128:143], v[76:79], a[76:79], v[128:143]\n\t"
      "s_waitcnt vmcnt(6)\n\t"
      "v_cmp_eq_u64 vcc, s[48:49], v[242:243]\n\t"
      "s_waitcnt vmcnt(5)\n\t"
      "v_cmp_eq_u64_e64 s[76:77], s[48:49], v[246:247]\n\t"
      "s_nop 0\n\t"
      "s_and_b64 vcc, vcc, s[76:77]\n\t"
      "s_waitcnt vmcnt(4)\n\t"
      "v_cmp_eq_u64_e64 s[76:77], s[48:49], v[250:251]\n\t"
      "s_nop 0\n\t"
      "s_and_b64 vcc, vcc, s[76:77]\n\t"
      "s_waitcnt vmcnt(3)\n\t"
      "v_cmp_eq_u64_e64 s[76:77], s[48:49], v[254:255]\n\t"
      "s_nop 0\n\t"
      "s_and_b64 vcc, vcc, s[76:77]\n\t"
      "s_cmp_eq_u64 vcc, exec\n\t"
      "s_cbranch_scc1 .Lskip_4_%=\n\t"
      "s_mov_b32 s79, 32768\n\t"
      ".Lspin_5_%=:\n\t"
      "s_sleep 2\n\t"
      "s_add_u32 s78, s36, 4096\n\t"
      "buffer_load_dwordx4 v[240:243], v198, s[24:27], s36 offen sc1\n\t"
      "buffer_load_dwordx4 v[244:247], v198, s[24:27], s78 offen sc1\n\t"
      "buffer_load_dwordx4 v[248:251], v199, s[24:27], s36 offen sc1\n\t"
      "buffer_load_dwordx4 v[252:255], v199, s[24:27], s78 offen sc1\n\t"
      "s_waitcnt vmcnt(0)\n\t"
      "v_cmp_eq_u64 vcc, s[48:49], v[242:243]\n\t"
      "v_cmp_eq_u64_e64 s[76:77], s[48:49], v[246:247]\n\t"
      "s_nop 0\n\t"
      "s_and_b64 vcc, vcc, s[76:77]\n\t"
      "v_cmp_eq_u64_e64 s[76:77], s[48:49], v[250:251]\n\t"
      "s_nop 0\n\t"
      "s_and_b64 vcc, vcc, s[76:77]\n\t"
      "v_cmp_eq_u64_e64 s[76:77], s[48:49], v[254:255]\n\t"
      "s_nop 0\n\t"
      "s_and_b64 vcc, vcc, s[76:77]\n\t"
      "s_cmp_eq_u64 vcc, exec\n\t"
      "s_cbranch_scc1 .Lskip_4_%=\n\t"
      "s_sub_u32 s79, s79, 1\n\t"
      "s_cmp_lg_u32 s79, 0\n\t"
      "s_cbranch_scc1 .Lspin_5_%=\n\t"
      ".Lskip_4_%=:\n\t"
      "v_dot2_f32_bf16 v208, v240, s85, 0\n\t"
      "v_dot2_f32_bf16 v209, v240, s88, 0\n\t"
      "v_dot2_f32_bf16 v210, v241, s85, 0\n\t"
      "v_dot2_f32_bf16 v211, v241, s88, 0\n\t"
      "v_dot2_f32_bf16 v208, v244, s85, v208\n\t"
      "v_dot2_f32_bf16 v209, v244, s88, v209\n\t"
      "v_mfma_f32_32x32x16_bf16 v[144:159], v[76:79], a[204:207], v[144:159]\n\t"
      "v_dot2_f32_bf16 v210, v245, s85, v210\n\t"
      "v_dot2_f32_bf16 v211, v245, s88, v211\n\t"
      "v_dot2_f32_bf16 v208, v248, s85, v208\n\t"
      "v_dot2_f32_bf16 v209, v248, s88, v209\n\t"
      "v_dot2_f32_bf16 v210, v249, s85, v210\n\t"
      "v_dot2_f32_bf16 v211, v249, s88, v211\n\t"
      "v_dot2_f32_bf16 v208, v252, s85, v208\n\t"
      "v_dot2_f32_bf16 v209, v252, s88, v209\n\t"
      "ds_read_b64_tr_b16 v[96:97], v26 offset:49152\n\t"
      "ds_read_b64_tr_b16 v[98:99], v26 offset:49664\n\t"
      "s_waitcnt lgkmcnt(8)\n\t"
      "v_mfma_f32_32x32x16_bf16 v[128:143], v[80:83], a[80:83], v[128:143]\n\t"
      "v_dot2_f32_bf16 v210, v253, s85, v210\n\t"
      "v_dot2_f32_bf16 v211, v253, s88, v211\n\t"
      "v_mov_b32 v212, v208\n\t"
      "v_mov_b32 v213, v209\n\t"
      "v_mov_b32 v214, v210\n\t"
      "v_mov_b32 v215, v211\n\t"
      "v_permlane32_swap_b32 v208, v212\n\t"
      "v_permlane32_swap_b32 v209, v213\n\t"
      "v_mfma_f32_32x32x16_bf16 v[144:159], v[80:83], a[208:211], v[144:159]\n\t"
      "v_permlane32_swap_b32 v210, v214\n\t"
      "v_permlane32_swap_b32 v211, v215\n\t"
      "v_add_f32 v208, v208, v212\n\t"
      "v_add_f32 v209, v209, v213\n\t"
      "v_add_f32 v210, v210, v214\n\t"
      "v_add_f32 v211, v211, v215\n\t"
      "v_mov_b32 v212, v208\n\t"
      "v_mov_b32 v213, v209\n\t"
      "ds_read_b64_tr_b16 v[100:101], v26 offset:51200\n\t"
      "ds_read_b64_tr_b16 v[102:103], v26 offset:51712\n\t"
      "s_waitcnt lgkmcnt(8)\n\t"
      "v_mfma_f32_32x32x16_bf16 v[128:143], v[84:87], a[84:87], v[128:143]\n\t"
      "v_mov_b32 v214, v210\n\t"
      "v_mov_b32 v215, v211\n\t"
      "v_permlane16_swap_b32 v208, v212\n\t"
      "v_permlane16_swap_b32 v209, v213\n\t"
      "v_permlane16_swap_b32 v210, v214\n\t"
      "v_permlane16_swap_b32 v211, v215\n\t"
      "v_add_f32 v208, v208, v212\n\t"
      "v_add_f32 v209, v209, v213\n\t"
      "v_mfma_f32_32x32x16_bf16 v[144:159], v[84:87], a[212:215], v[144:159]\n\t"
      "v_add_f32 v210, v210, v214\n\t"
      "v_add_f32 v211, v211, v215\n\t"
      "v_cvt_pk_bf16_f32 v216, v208, v209\n\t"
      "v_cvt_pk_bf16_f32 v217, v210, v211\n\t"
      "v_mov_b32 v218, v62\n\t"
      "v_mov_b32 v219, v63\n\t"
      "s_mov_b64 exec, 0xffff\n\t"
      "buffer_store_dwordx4 v[216:219], v197, s[24:27], s37 offen sc1\n\t"
      "s_mov_b64 exec, -1\n\t"
      "ds_read_b64_tr_b16 v[104:105], v26 offset:53248\n\t"
      "ds_read_b64_tr_b16 v[106:107], v26 offset:53760\n\t"
      "s_waitcnt lgkmcnt(8)\n\t"
      "v_mfma_f32_32x32x16_bf16 v[128:143], v[88:91], a[88:91], v[128:143]\n\t"
      "s_waitcnt vmcnt(1)\n\t"
      "v_cmp_eq_u64 vcc, s[48:49], v[36:37]\n\t"
      "s_nop 0\n\t"
      "s_and_b32 s78, vcc_lo, 16776975\n\t"
      "s_cmp_eq_u32 s78, 16776975\n\t"
      "s_cbranch_scc1 .Lskip_6_%=\n\t"
      "s_mov_b32 s79, 32768\n\t"
      ".Lspin_7_%=:\n\t"
      "s_sleep 1\n\t"
      "s_mov_b32 s78, 131072\n\t"
      "s_mov_b32 exec_lo, 16776975\n\t"
      "s_mov_b32 exec_hi, 0\n\t"
      "buffer_load_dwordx2 v[36:37], v38, s[24:27], s78 offen sc1\n\t"
      "s_mov_b64 exec, -1\n\t"
      "s_waitcnt vmcnt(0)\n\t"
      "v_cmp_eq_u64 vcc, s[48:49], v[36:37]\n\t"
      "s_nop 0\n\t"
      "s_and_b32 s78, vcc_lo, 16776975\n\t"
      "s_cmp_eq_u32 s78, 16776975\n\t"
      "s_cbranch_scc1 .Lskip_6_%=\n\t"
      "s_sub_u32 s79, s79, 1\n\t"
      "s_cmp_lg_u32 s79, 0\n\t"
      "s_cbranch_scc1 .Lspin_7_%=\n\t"
      ".Lskip_6_%=:\n\t"
      "s_add_u32 s62, s74, 0\n\t"
      "s_lshl_b32 s62, s62, 12\n\t"
      "s_add_u32 s62, s62, s72\n\t"
      "buffer_load_dwordx4 a[128:131], v1, s[24:27], s62 offen sc1\n\t"
      "v_mfma_f32_32x32x16_bf16 v[144:159], v[88:91], a[216:219], v[144:159]\n\t"
      "ds_read_b64_tr_b16 v[108:109], v26 offset:55296\n\t"
      "ds_read_b64_tr_b16 v[110:111], v26 offset:55808\n\t"
      "s_waitcnt lgkmcnt(8)\n\t"
      "v_mfma_f32_32x32x16_bf16 v[128:143], v[92:95], a[92:95], v[128:143]\n\t"
      "buffer_load_dwordx4 a[132:135], v1, s[24:27], s62 offen offset:1024 sc1\n\t"
      "v_mfma_f32_32x32x16_bf16 v[144:159], v[92:95], a[220:223], v[144:159]\n\t"
      "ds_read_b64_tr_b16 v[112:113], v26 offset:57344\n\t"
      "ds_read_b64_tr_b16 v[114:115], v26 offset:57856\n\t"
      "s_waitcnt lgkmcnt(8)\n\t"
      "v_mfma_f32_32x32x16_bf16 v[128:143], v[96:99], a[96:99], v[128:143]\n\t"
      "buffer_load_dwordx4 a[136:139], v1, s[24:27], s62 offen offset:2048 sc1\n\t"
      "v_mfma_f32_32x32x16_bf16 v[144:159], v[96:99], a[224:227], v[144:159]\n\t"
      "ds_read_b64_tr_b16 v[116:117], v26 offset:59392\n\t"
      "ds_read_b64_tr_b16 v[118:119], v26 offset:59904\n\t"
      "s_waitcnt lgkmcnt(8)\n\t"
      "v_mfma_f32_32x32x16_bf16 v[128:143], v[100:103], a[100:103], v[128:143]\n\t"
      "buffer_load_dwordx4 a[140:143], v1, s[24:27], s62 offen offset:3072 sc1\n\t"
      "v_mfma_f32_32x32x16_bf16 v[144:159], v[100:103], a[228:231], v[144:159]\n\t"
      "ds_read_b64_tr_b16 v[120:121], v26 offset:61440\n\t"
      "ds_read_b64_tr_b16 v[122:123], v26 offset:61952\n\t"
      "s_waitcnt lgkmcnt(8)\n\t"
      "v_mfma_f32_32x32x16_bf16 v[128:143], v[104:107], a[104:107], v[128:143]\n\t"
      "s_add_u32 s62, s74, 3\n\t"
      "s_lshl_b32 s62, s62, 12\n\t"
      "s_add_u32 s62, s62, s72\n\t"
      "buffer_load_dwordx4 a[144:147], v1, s[24:27], s62 offen sc1\n\t"
      "v_mfma_f32_32x32x16_bf16 v[144:159], v[104:107], a[232:235], v[144:159]\n\t"
      "ds_read_b64_tr_b16 v[124:125], v26 offset:63488\n\t"
      "ds_read_b64_tr_b16 v[126:127], v26 offset:64000\n\t"
      "s_waitcnt lgkmcnt(0)\n\t"
      "s_barrier\n\t"
      "v_mfma_f32_32x32x16_bf16 v[128:143], v[108:111], a[108:111], v[128:143]\n\t"
      "buffer_load_dwordx4 a[148:151], v1, s[24:27], s62 offen offset:1024 sc1\n\t"
      "v_mfma_f32_32x32x16_bf16 v[144:159], v[108:111], a[236:239], v[144:159]\n\t"
      "v_mfma_f32_32x32x16_bf16 v[128:143], v[112:115], a[112:115], v[128:143]\n\t"
      "buffer_load_dwordx4 a[152:155], v1, s[24:27], s62 offen offset:2048 sc1\n\t"
      "v_mfma_f32_32x32x16_bf16 v[144:159], v[112:115], a[240:243], v[144:159]\n\t"
      "v_mfma_f32_32x32x16_bf16 v[128:143], v[116:119], a[116:119], v[128:143]\n\t"
      "buffer_load_dwordx4 a[156:159], v1, s[24:27], s62 offen offset:3072 sc1\n\t"
      "v_mfma_f32_32x32x16_bf16 v[144:159], v[116:119], a[244:247], v[144:159]\n\t"
      "v_mfma_f32_32x32x16_bf16 v[128:143], v[120:123], a[120:123], v[128:143]\n\t"
      "buffer_load_dwordx4 v[6:9], v200, s[24:27], s35 offen sc1\n\t"
      "v_mfma_f32_32x32x16_bf16 v[144:159], v[120:123], a[248:251], v[144:159]\n\t"
      "v_mfma_f32_32x32x16_bf16 v[128:143], v[124:127], a[124:127], v[128:143]\n\t"
      "v_mfma_f32_32x32x16_bf16 v[144:159], v[124:127], a[252:255], v[144:159]\n\t"
      "s_add_u32 s64, s89, s70\n\t"
      "s_add_u32 s65, s89, s71\n\t"
      "s_nop 8\n\t"
      "v_cvt_pk_bf16_f32 v48, v128, v129\n\t"
      "v_cvt_pk_bf16_f32 v49, v130, v131\n\t"
      "v_cvt_pk_bf16_f32 v50, v132, v133\n\t"
      "v_cvt_pk_bf16_f32 v51, v134, v135\n\t"
      "v_cvt_pk_bf16_f32 v52, v136, v137\n\t"
      "v_cvt_pk_bf16_f32 v53, v138, v139\n\t"
      "v_cvt_pk_bf16_f32 v54, v140, v141\n\t"
      "v_cvt_pk_bf16_f32 v55, v142, v143\n\t"
      "v_pk_max_i16 v48, v48, 0\n\t"
      "v_pk_max_i16 v49, v49, 0\n\t"
      "v_pk_max_i16 v50, v50, 0\n\t"
      "v_pk_max_i16 v51, v51, 0\n\t"
      "v_pk_max_i16 v52, v52, 0\n\t"
      "v_pk_max_i16 v53, v53, 0\n\t"
      "v_pk_max_i16 v54, v54, 0\n\t"
      "v_pk_max_i16 v55, v55, 0\n\t"
      "v_cvt_pk_bf16_f32 v40, v144, v145\n\t"
      "v_cvt_pk_bf16_f32 v41, v146, v147\n\t"
      "v_cvt_pk_bf16_f32 v42, v148, v149\n\t"
      "v_cvt_pk_bf16_f32 v43, v150, v151\n\t"
      "v_cvt_pk_bf16_f32 v44, v152, v153\n\t"
      "v_cvt_pk_bf16_f32 v45, v154, v155\n\t"
      "v_cvt_pk_bf16_f32 v46, v156, v157\n\t"
      "v_cvt_pk_bf16_f32 v47, v158, v159\n\t"
      "v_pk_max_i16 v40, v40, 0\n\t"
      "v_pk_max_i16 v41, v41, 0\n\t"
      "v_pk_max_i16 v42, v42, 0\n\t"
      "v_pk_max_i16 v43, v43, 0\n\t"
      "v_pk_max_i16 v44, v44, 0\n\t"
      "v_pk_max_i16 v45, v45, 0\n\t"
      "v_pk_max_i16 v46, v46, 0\n\t"
      "v_pk_max_i16 v47, v47, 0\n\t"
      "buffer_store_dwordx4 v[48:51], v1, s[24:27], s64 offen sc1\n\t"
      "buffer_store_dwordx4 v[52:55], v1, s[24:27], s64 offen offset:1024 sc1\n\t"
      "buffer_store_dwordx4 v[40:43], v1, s[24:27], s65 offen sc1\n\t"
      "buffer_store_dwordx4 v[44:47], v1, s[24:27], s65 offen offset:1024 sc1\n\t"
      "ds_read_b64_tr_b16 v[64:65], v18 offset:16384\n\t"
      "ds_read_b64_tr_b16 v[66:67], v18 offset:18432\n\t"
      "ds_read_b64_tr_b16 v[68:69], v19 offset:16384\n\t"
      "ds_read_b64_tr_b16 v[70:71], v19 offset:18432\n\t"
      "ds_read_b64_tr_b16 v[72:73], v20 offset:16384\n\t"
      "ds_read_b64_tr_b16 v[74:75], v20 offset:18432\n\t"
      "ds_read_b64_tr_b16 v[76:77], v21 offset:16384\n\t"
      "ds_read_b64_tr_b16 v[78:79], v21 offset:18432\n\t"
      "ds_read_b64_tr_b16 v[80:81], v18 offset:20480\n\t"
      "ds_read_b64_tr_b16 v[82:83], v18 offset:22528\n\t"
      "ds_read_b64_tr_b16 v[84:85], v19 offset:20480\n\t"
      "ds_read_b64_tr_b16 v[86:87], v19 offset:22528\n\t"
      "ds_read_b64_tr_b16 v[88:89], v20 offset:20480\n\t"
      "ds_read_b64_tr_b16 v[90:91], v20 offset:22528\n\t"
      "s_waitcnt vmcnt(12) lgkmcnt(12)\n\t"
      "v_mfma_f32_32x32x16_bf16 v[128:143], v[64:67], a[128:131], 0\n\t"
      "ds_read_b64_tr_b16 v[92:93], v21 offset:20480\n\t"
      "ds_read_b64_tr_b16 v[94:95], v21 offset:22528\n\t"
      "s_waitcnt lgkmcnt(12)\n\t"
      "v_mfma_f32_32x32x16_bf16 v[144:159], v[68:71], a[128:131], 0\n\t"
      "s_mov_b32 s78, 262144\n\t"
      "s_mov_b32 exec_lo, 15\n\t"
      "s_mov_b32 exec_hi, 0\n\t"
      "buffer_load_dwordx2 v[36:37], v38, s[24:27], s78 offen sc1\n\t"
      "s_mov_b64 exec, -1\n\t"
      "ds_read_b64_tr_b16 v[96:97], v18 offset:24576\n\t"
      "ds_read_b64_tr_b16 v[98:99], v18 offset:26624\n\t"
      "s_waitcnt lgkmcnt(12)\n\t"
      "v_mfma_f32_32x32x16_bf16 v[160:175], v[72:75], a[128:131], 0\n\t"
      "ds_read_b64_tr_b16 v[100:101], v19 offset:24576\n\t"
      "ds_read_b64_tr_b16 v[102:103], v19 offset:26624\n\t"
      "s_waitcnt lgkmcnt(12)\n\t"
      "v_mfma_f32_32x32x16_bf16 v[176:191], v[76:79], a[128:131], 0\n\t"
      "s_add_u32 s60, s75, 1\n\t"
      "s_lshl_b32 s60, s60, 16\n\t"
      "s_add_u32 m0, s58, 16384\n\t"
      "s_add_u32 s60, s60, s73\n\t"
      "buffer_load_dwordx4 v2, s[16:19], s60 offen lds\n\t"
      "ds_read_b64_tr_b16 v[104:105], v20 offset:24576\n\t"
      "ds_read_b64_tr_b16 v[106:107], v20 offset:26624\n\t"
      "s_waitcnt vmcnt(13) lgkmcnt(12)\n\t"
      "v_mfma_f32_32x32x16_bf16 v[128:143], v[80:83], a[132:135], v[128:143]\n\t"
      "ds_read_b64_tr_b16 v[108:109], v21 offset:24576\n\t"
      "ds_read_b64_tr_b16 v[110:111], v21 offset:26624\n\t"
      "s_waitcnt lgkmcnt(12)\n\t"
      "v_mfma_f32_32x32x16_bf16 v[144:159], v[84:87], a[132:135], v[144:159]\n\t"
      "buffer_load_dwordx4 v3, s[16:19], s60 offen offset:1024 lds\n\t"
      "s_waitcnt vmcnt(7)\n\t"
      "v_add_f32 v6, v6, v7\n\t"
      "v_add_f32 v8, v8, v9\n\t"
      "ds_read_b64_tr_b16 v[112:113], v18 offset:28672\n\t"
      "ds_read_b64_tr_b16 v[114:115], v18 offset:30720\n\t"
      "s_waitcnt lgkmcnt(12)\n\t"
      "v_mfma_f32_32x32x16_bf16 v[160:175], v[88:91], a[132:135], v[160:175]\n\t"
      "v_add_f32 v6, v6, v8\n\t"
      "v_mov_b32 v7, v6\n\t"
      "s_nop 1\n\t"
      "v_permlane32_swap_b32 v6, v7\n\t"
      "ds_read_b64_tr_b16 v[116:117], v19 offset:28672\n\t"
      "ds_read_b64_tr_b16 v[118:119], v19 offset:30720\n\t"
      "s_waitcnt lgkmcnt(12)\n\t"
      "v_mfma_f32_32x32x16_bf16 v[176:191], v[92:95], a[132:135], v[176:191]\n\t"
      "buffer_load_dwordx4 v4, s[16:19], s60 offen offset:2048 lds\n\t"
      "v_add_f32 v6, v6, v7\n\t"
      "v_mul_f32 v6, s38, v6\n\t"
      "v_exp_f32 v6, v6\n\t"
      "ds_read_b64_tr_b16 v[120:121], v20 offset:28672\n\t"
      "ds_read_b64_tr_b16 v[122:123], v20 offset:30720\n\t"
      "s_waitcnt lgkmcnt(12)\n\t"
      "v_mfma_f32_32x32x16_bf16 v[128:143], v[96:99], a[136:139], v[128:143]\n\t"
      "v_add_f32 v6, 1.0, v6\n\t"
      "v_rcp_f32 v60, v6\n\t"
      "ds_read_b64_tr_b16 v[124:125], v21 offset:28672\n\t"
      "ds_read_b64_tr_b16 v[126:127], v21 offset:30720\n\t"
      "s_waitcnt lgkmcnt(12)\n\t"
      "v_mfma_f32_32x32x16_bf16 v[144:159], v[100:103], a[136:139], v[144:159]\n\t"
      "buffer_load_dwordx4 v5, s[16:19], s60 offen offset:3072 lds\n\t"
      "v_sub_f32 v60, 1.0, v60\n\t"
      "ds_read_b64_tr_b16 v[64:65], v18 offset:32768\n\t"
      "ds_read_b64_tr_b16 v[66:67], v18 offset:34816\n\t"
      "s_waitcnt lgkmcnt(12)\n\t"
      "v_mfma_f32_32x32x16_bf16 v[160:175], v[104:107], a[136:139], v[160:175]\n\t"
      "ds_read_b64_tr_b16 v[68:69], v19 offset:32768\n\t"
      "ds_read_b64_tr_b16 v[70:71], v19 offset:34816\n\t"
      "s_waitcnt lgkmcnt(12)\n\t"
      "v_mfma_f32_32x32x16_bf16 v[176:191], v[108:111], a[136:139], v[176:191]\n\t"
      "s_add_u32 s60, s75, 0\n\t"
      "s_lshl_b32 s60, s60, 16\n\t"
      "s_add_u32 m0, s58, 4294950912\n\t"
      "s_add_u32 s60, s60, s73\n\t"
      "buffer_load_dwordx4 v2, s[20:23], s60 offen lds\n\t"
      "s_waitcnt vmcnt(6)\n\t"
      "s_add_u32 s78, s47, 393216\n\t"
      "s_mov_b64 exec, 1\n\t"
      "buffer_store_dwordx2 v[62:63], off, s[24:27], s78 sc1\n\t"
      "s_mov_b64 exec, -1\n\t"
      "ds_read_b64_tr_b16 v[72:73], v20 offset:32768\n\t"
      "ds_read_b64_tr_b16 v[74:75], v20 offset:34816\n\t"
      "s_waitcnt lgkmcnt(12)\n\t"
      "v_mfma_f32_32x32x16_bf16 v[128:143], v[112:115], a[140:143], v[128:143]\n\t"
      "ds_read_b64_tr_b16 v[76:77], v21 offset:32768\n\t"
      "ds_read_b64_tr_b16 v[78:79], v21 offset:34816\n\t"
      "s_waitcnt lgkmcnt(12)\n\t"
      "v_mfma_f32_32x32x16_bf16 v[144:159], v[116:119], a[140:143], v[144:159]\n\t"
      "buffer_load_dwordx4 v3, s[20:23], s60 offen offset:1024 lds\n\t"
      "ds_read_b64_tr_b16 v[80:81], v18 offset:36864\n\t"
      "ds_read_b64_tr_b16 v[82:83], v18 offset:38912\n\t"
      "s_waitcnt lgkmcnt(12)\n\t"
      "v_mfma_f32_32x32x16_bf16 v[160:175], v[120:123], a[140:143], v[160:175]\n\t"
      "ds_read_b64_tr_b16 v[84:85], v19 offset:36864\n\t"
      "ds_read_b64_tr_b16 v[86:87], v19 offset:38912\n\t"
      "s_waitcnt lgkmcnt(12)\n\t"
      "v_mfma_f32_32x32x16_bf16 v[176:191], v[124:127], a[140:143], v[176:191]\n\t"
      "buffer_load_dwordx4 v4, s[20:23], s60 offen offset:2048 lds\n\t"
      "ds_read_b64_tr_b16 v[88:89], v20 offset:36864\n\t"
      "ds_read_b64_tr_b16 v[90:91], v20 offset:38912\n\t"
      "s_waitcnt lgkmcnt(12)\n\t"
      "v_mfma_f32_32x32x16_bf16 v[128:143], v[64:67], a[144:147], v[128:143]\n\t"
      "ds_read_b64_tr_b16 v[92:93], v21 offset:36864\n\t"
      "ds_read_b64_tr_b16 v[94:95], v21 offset:38912\n\t"
      "s_waitcnt lgkmcnt(12)\n\t"
      "v_mfma_f32_32x32x16_bf16 v[144:159], v[68:71], a[144:147], v[144:159]\n\t"
      "buffer_load_dwordx4 v5, s[20:23], s60 offen offset:3072 lds\n\t"
      "ds_read_b64_tr_b16 v[96:97], v18 offset:40960\n\t"
      "ds_read_b64_tr_b16 v[98:99], v18 offset:43008\n\t"
      "s_waitcnt lgkmcnt(12)\n\t"
      "v_mfma_f32_32x32x16_bf16 v[160:175], v[72:75], a[144:147], v[160:175]\n\t"
      "ds_read_b64_tr_b16 v[100:101], v19 offset:40960\n\t"
      "ds_read_b64_tr_b16 v[102:103], v19 offset:43008\n\t"
      "s_waitcnt lgkmcnt(12)\n\t"
      "v_mfma_f32_32x32x16_bf16 v[176:191], v[76:79], a[144:147], v[176:191]\n\t"
      "s_add_u32 s60, s75, 1\n\t"
      "s_lshl_b32 s60, s60, 16\n\t"
      "s_add_u32 m0, s58, 0\n\t"
      "s_add_u32 s60, s60, s73\n\t"
      "buffer_load_dwordx4 v2, s[20:23], s60 offen lds\n\t"
      "ds_read_b64_tr_b16 v[104:105], v20 offset:40960\n\t"
      "ds_read_b64_tr_b16 v[106:107], v20 offset:43008\n\t"
      "s_waitcnt lgkmcnt(12)\n\t"
      "v_mfma_f32_32x32x16_bf16 v[128:143], v[80:83], a[148:151], v[128:143]\n\t"
      "ds_read_b64_tr_b16 v[108:109], v21 offset:40960\n\t"
      "ds_read_b64_tr_b16 v[110:111], v21 offset:43008\n\t"
      "s_waitcnt lgkmcnt(12)\n\t"
      "v_mfma_f32_32x32x16_bf16 v[144:159], v[84:87], a[148:151], v[144:159]\n\t"
      "buffer_load_dwordx4 v3, s[20:23], s60 offen offset:1024 lds\n\t"
      "ds_read_b64_tr_b16 v[112:113], v18 offset:45056\n\t"
      "ds_read_b64_tr_b16 v[114:115], v18 offset:47104\n\t"
      "s_waitcnt lgkmcnt(12)\n\t"
      "v_mfma_f32_32x32x16_bf16 v[160:175], v[88:91], a[148:151], v[160:175]\n\t"
      "ds_read_b64_tr_b16 v[116:117], v19 offset:45056\n\t"
      "ds_read_b64_tr_b16 v[118:119], v19 offset:47104\n\t"
      "s_waitcnt lgkmcnt(12)\n\t"
      "v_mfma_f32_32x32x16_bf16 v[176:191], v[92:95], a[148:151], v[176:191]\n\t"
      "buffer_load_dwordx4 v4, s[20:23], s60 offen offset:2048 lds\n\t"
      "ds_read_b64_tr_b16 v[120:121], v20 offset:45056\n\t"
      "ds_read_b64_tr_b16 v[122:123], v20 offset:47104\n\t"
      "s_waitcnt lgkmcnt(12)\n\t"
      "v_mfma_f32_32x32x16_bf16 v[128:143], v[96:99], a[152:155], v[128:143]\n\t"
      "ds_read_b64_tr_b16 v[124:125], v21 offset:45056\n\t"
      "ds_read_b64_tr_b16 v[126:127], v21 offset:47104\n\t"
      "s_waitcnt lgkmcnt(12)\n\t"
      "v_mfma_f32_32x32x16_bf16 v[144:159], v[100:103], a[152:155], v[144:159]\n\t"
      "buffer_load_dwordx4 v5, s[20:23], s60 offen offset:3072 lds\n\t"
      "v_and_b32 v56, 1, v0\n\t"
      "v_lshlrev_b32 v56, 4, v56\n\t"
      "v_mov_b32 v57, 0x3f80\n\t"
      "ds_read_b64_tr_b16 v[64:65], v19 offset:49152\n\t"
      "ds_read_b64_tr_b16 v[66:67], v19 offset:51200\n\t"
      "s_waitcnt lgkmcnt(12)\n\t"
      "v_mfma_f32_32x32x16_bf16 v[160:175], v[104:107], a[152:155], v[160:175]\n\t"
      "v_lshlrev_b32 v56, v56, v57\n\t"
      "v_bfe_u32 v57, v0, 1, 1\n\t"
      "v_bfe_u32 v58, v0, 3, 2\n\t"
      "ds_read_b64_tr_b16 v[68:69], v20 offset:49152\n\t"
      "ds_read_b64_tr_b16 v[70:71], v20 offset:51200\n\t"
      "s_waitcnt lgkmcnt(12)\n\t"
      "v_mfma_f32_32x32x16_bf16 v[176:191], v[108:111], a[152:155], v[176:191]\n\t"
      "s_waitcnt vmcnt(13)\n\t"
      "v_cmp_eq_u64 vcc, s[48:49], v[36:37]\n\t"
      "s_nop 0\n\t"
      "s_and_b32 s78, vcc_lo, 15\n\t"
      "s_cmp_eq_u32 s78, 15\n\t"
      "s_cbranch_scc1 .Lskip_8_%=\n\t"
      "s_mov_b32 s79, 32768\n\t"
      ".Lspin_9_%=:\n\t"
      "s_sleep 1\n\t"
      "s_mov_b32 s78, 262144\n\t"
      "s_mov_b32 exec_lo, 15\n\t"
      "s_mov_b32 exec_hi, 0\n\t"
      "buffer_load_dwordx2 v[36:37], v38, s[24:27], s78 offen sc1\n\t"
      "s_mov_b64 exec, -1\n\t"
      "s_waitcnt vmcnt(0)\n\t"
      "v_cmp_eq_u64 vcc, s[48:49], v[36:37]\n\t"
      "s_nop 0\n\t"
      "s_and_b32 s78, vcc_lo, 15\n\t"
      "s_cmp_eq_u32 s78, 15\n\t"
      "s_cbranch_scc1 .Lskip_8_%=\n\t"
      "s_sub_u32 s79, s79, 1\n\t"
      "s_cmp_lg_u32 s79, 0\n\t"
      "s_cbranch_scc1 .Lspin_9_%=\n\t"
      ".Lskip_8_%=:\n\t"
      "s_add_u32 s62, s74, 1\n\t"
      "s_lshl_b32 s62, s62, 12\n\t"
      "s_add_u32 s62, s62, s72\n\t"
      "buffer_load_dwordx4 a[160:163], v1, s[24:27], s62 offen sc1\n\t"
      "v_lshl_add_u32 v57, v58, 1, v57\n\t"
      "v_bfe_u32 v58, v0, 2, 1\n\t"
      "v_lshrrev_b32 v59, 5, v0\n\t"
      "ds_read_b64_tr_b16 v[72:73], v21 offset:49152\n\t"
      "ds_read_b64_tr_b16 v[74:75], v21 offset:51200\n\t"
      "s_waitcnt lgkmcnt(12)\n\t"
      "v_mfma_f32_32x32x16_bf16 v[128:143], v[112:115], a[156:159], v[128:143]\n\t"
      "v_xor_b32 v58, v58, v59\n\t"
      "v_lshl_add_u32 v57, v58, 3, v57\n\t"
      "v_cmp_eq_u32 vcc, 0, v57\n\t"
      "s_nop 1\n\t"
      "v_cndmask_b32 v40, 0, v56, vcc\n\t"
      "ds_read_b64_tr_b16 v[76:77], v19 offset:53248\n\t"
      "ds_read_b64_tr_b16 v[78:79], v19 offset:55296\n\t"
      "s_waitcnt lgkmcnt(12)\n\t"
      "v_mfma_f32_32x32x16_bf16 v[144:159], v[116:119], a[156:159], v[144:159]\n\t"
      "buffer_load_dwordx4 a[164:167], v1, s[24:27], s62 offen offset:1024 sc1\n\t"
      "v_cmp_eq_u32 vcc, 1, v57\n\t"
      "s_nop 1\n\t"
      "v_cndmask_b32 v41, 0, v56, vcc\n\t"
      "v_cmp_eq_u32 vcc, 2, v57\n\t"
      "s_nop 1\n\t"
      "v_cndmask_b32 v42, 0, v56, vcc\n\t"
      "v_cmp_eq_u32 vcc, 3, v57\n\t"
      "s_nop 1\n\t"
      "v_cndmask_b32 v43, 0, v56, vcc\n\t"
      "ds_read_b64_tr_b16 v[80:81], v20 offset:53248\n\t"
      "ds_read_b64_tr_b16 v[82:83], v20 offset:55296\n\t"
      "s_waitcnt lgkmcnt(12)\n\t"
      "v_mfma_f32_32x32x16_bf16 v[160:175], v[120:123], a[156:159], v[160:175]\n\t"
      "v_cmp_eq_u32 vcc, 4, v57\n\t"
      "s_nop 1\n\t"
      "v_cndmask_b32 v44, 0, v56, vcc\n\t"
      "v_cmp_eq_u32 vcc, 5, v57\n\t"
      "s_nop 1\n\t"
      "v_cndmask_b32 v45, 0, v56, vcc\n\t"
      "v_cmp_eq_u32 vcc, 6, v57\n\t"
      "s_nop 1\n\t"
      "v_cndmask_b32 v46, 0, v56, vcc\n\t"
      "ds_read_b64_tr_b16 v[84:85], v21 offset:53248\n\t"
      "ds_read_b64_tr_b16 v[86:87], v21 offset:55296\n\t"
      "s_waitcnt lgkmcnt(12)\n\t"
      "v_mfma_f32_32x32x16_bf16 v[176:191], v[124:127], a[156:159], v[176:191]\n\t"
      "buffer_load_dwordx4 a[168:171], v1, s[24:27], s62 offen offset:2048 sc1\n\t"
      "v_cmp_eq_u32 vcc, 7, v57\n\t"
      "s_nop 1\n\t"
      "v_cndmask_b32 v47, 0, v56, vcc\n\t"
      "ds_read_b64_tr_b16 v[88:89], v19 offset:57344\n\t"
      "ds_read_b64_tr_b16 v[90:91], v19 offset:59392\n\t"
      "s_waitcnt vmcnt(2) lgkmcnt(12)\n\t"
      "v_mfma_f32_32x32x16_bf16 v[144:159], v[64:67], a[160:163], v[144:159]\n\t"
      "ds_read_b64_tr_b16 v[92:93], v20 offset:57344\n\t"
      "ds_read_b64_tr_b16 v[94:95], v20 offset:59392\n\t"
      "s_waitcnt lgkmcnt(12)\n\t"
      "v_mfma_f32_32x32x16_bf16 v[160:175], v[68:71], a[160:163], v[160:175]\n\t"
      "buffer_load_dwordx4 a[172:175], v1, s[24:27], s62 offen offset:3072 sc1\n\t"
      "ds_read_b64_tr_b16 v[96:97], v21 offset:57344\n\t"
      "ds_read_b64_tr_b16 v[98:99], v21 offset:59392\n\t"
      "s_waitcnt lgkmcnt(12)\n\t"
      "v_mfma_f32_32x32x16_bf16 v[176:191], v[72:75], a[160:163], v[176:191]\n\t"
      "ds_read_b64_tr_b16 v[100:101], v19 offset:61440\n\t"
      "ds_read_b64_tr_b16 v[102:103], v19 offset:63488\n\t"
      "s_waitcnt vmcnt(2) lgkmcnt(12)\n\t"
      "v_mfma_f32_32x32x16_bf16 v[144:159], v[76:79], a[164:167], v[144:159]\n\t"
      "s_add_u32 s62, s74, 4\n\t"
      "s_lshl_b32 s62, s62, 12\n\t"
      "s_add_u32 s62, s62, s72\n\t"
      "buffer_load_dwordx4 a[176:179], v1, s[24:27], s62 offen sc1\n\t"
      "ds_read_b64_tr_b16 v[104:105], v20 offset:61440\n\t"
      "ds_read_b64_tr_b16 v[106:107], v20 offset:63488\n\t"
      "s_waitcnt lgkmcnt(12)\n\t"
      "v_mfma_f32_32x32x16_bf16 v[160:175], v[80:83], a[164:167], v[160:175]\n\t"
      "ds_read_b64_tr_b16 v[108:109], v21 offset:61440\n\t"
      "ds_read_b64_tr_b16 v[110:111], v21 offset:63488\n\t"
      "s_waitcnt lgkmcnt(12)\n\t"
      "v_mfma_f32_32x32x16_bf16 v[176:191], v[84:87], a[164:167], v[176:191]\n\t"
      "buffer_load_dwordx4 a[180:183], v1, s[24:27], s62 offen offset:1024 sc1\n\t"
      "ds_read_b64_tr_b16 v[112:113], v23 offset:0\n\t"
      "ds_read_b64_tr_b16 v[114:115], v23 offset:2048\n\t"
      "s_waitcnt vmcnt(3) lgkmcnt(12)\n\t"
      "v_mfma_f32_32x32x16_bf16 v[144:159], v[88:91], a[168:171], v[144:159]\n\t"
      "ds_read_b64_tr_b16 v[116:117], v24 offset:0\n\t"
      "ds_read_b64_tr_b16 v[118:119], v24 offset:2048\n\t"
      "s_waitcnt lgkmcnt(12)\n\t"
      "v_mfma_f32_32x32x16_bf16 v[160:175], v[92:95], a[168:171], v[160:175]\n\t"
      "buffer_load_dwordx4 a[184:187], v1, s[24:27], s62 offen offset:2048 sc1\n\t"
      "ds_read_b64_tr_b16 v[120:121], v25 offset:0\n\t"
      "ds_read_b64_tr_b16 v[122:123], v25 offset:2048\n\t"
      "s_waitcnt lgkmcnt(12)\n\t"
      "v_mfma_f32_32x32x16_bf16 v[176:191], v[96:99], a[168:171], v[176:191]\n\t"
      "ds_read_b64_tr_b16 v[124:125], v23 offset:4096\n\t"
      "ds_read_b64_tr_b16 v[126:127], v23 offset:6144\n\t"
      "s_waitcnt vmcnt(3) lgkmcnt(12)\n\t"
      "v_mfma_f32_32x32x16_bf16 v[144:159], v[100:103], a[172:175], v[144:159]\n\t"
      "buffer_load_dwordx4 a[188:191], v1, s[24:27], s62 offen offset:3072 sc1\n\t"
      "ds_read_b64_tr_b16 v[64:65], v24 offset:4096\n\t"
      "ds_read_b64_tr_b16 v[66:67], v24 offset:6144\n\t"
      "s_waitcnt lgkmcnt(12)\n\t"
      "v_mfma_f32_32x32x16_bf16 v[160:175], v[104:107], a[172:175], v[160:175]\n\t"
      "ds_read_b64_tr_b16 v[68:69], v25 offset:4096\n\t"
      "ds_read_b64_tr_b16 v[70:71], v25 offset:6144\n\t"
      "s_waitcnt lgkmcnt(12)\n\t"
      "v_mfma_f32_32x32x16_bf16 v[176:191], v[108:111], a[172:175], v[176:191]\n\t"
      "s_mov_b32 s78, 393216\n\t"
      "s_mov_b32 exec_lo, 15\n\t"
      "s_mov_b32 exec_hi, 0\n\t"
      "buffer_load_dwordx2 v[202:203], v38, s[24:27], s78 offen sc1\n\t"
      "s_mov_b64 exec, -1\n\t"
      "ds_read_b64_tr_b16 v[72:73], v23 offset:8192\n\t"
      "ds_read_b64_tr_b16 v[74:75], v23 offset:10240\n\t"
      "s_waitcnt vmcnt(4) lgkmcnt(12)\n\t"
      "v_mfma_f32_32x32x16_bf16 v[144:159], v[112:115], a[176:179], v[144:159]\n\t"
      "ds_read_b64_tr_b16 v[76:77], v24 offset:8192\n\t"
      "ds_read_b64_tr_b16 v[78:79], v24 offset:10240\n\t"
      "s_waitcnt lgkmcnt(12)\n\t"
      "v_mfma_f32_32x32x16_bf16 v[160:175], v[116:119], a[176:179], v[160:175]\n\t"
      "s_mov_b32 s78, 393216\n\t"
      "s_mov_b32 exec_lo, 240\n\t"
      "s_mov_b32 exec_hi, 0\n\t"
      "buffer_load_dwordx2 v[36:37], v38, s[24:27], s78 offen sc1\n\t"
      "s_mov_b64 exec, -1\n\t"
      "ds_read_b64_tr_b16 v[80:81], v25 offset:8192\n\t"
      "ds_read_b64_tr_b16 v[82:83], v25 offset:10240\n\t"
      "s_waitcnt lgkmcnt(12)\n\t"
      "v_mfma_f32_32x32x16_bf16 v[176:191], v[120:123], a[176:179], v[176:191]\n\t"
      "ds_read_b64_tr_b16 v[84:85], v23 offset:12288\n\t"
      "ds_read_b64_tr_b16 v[86:87], v23 offset:14336\n\t"
      "s_waitcnt vmcnt(4) lgkmcnt(12)\n\t"
      "v_mfma_f32_32x32x16_bf16 v[144:159], v[124:127], a[180:183], v[144:159]\n\t"
      "s_waitcnt vmcnt(1)\n\t"
      "v_cmp_eq_u64 vcc, s[48:49], v[202:203]\n\t"
      "s_nop 0\n\t"
      "s_and_b32 s78, vcc_lo, 15\n\t"
      "s_cmp_eq_u32 s78, 15\n\t"
      "s_cbranch_scc1 .Lskip_10_%=\n\t"
      "s_mov_b32 s79, 32768\n\t"
      ".Lspin_11_%=:\n\t"
      "s_sleep 1\n\t"
      "s_mov_b32 s78, 393216\n\t"
      "s_mov_b32 exec_lo, 15\n\t"
      "s_mov_b32 exec_hi, 0\n\t"
      "buffer_load_dwordx2 v[202:203], v38, s[24:27], s78 offen sc1\n\t"
      "s_mov_b64 exec, -1\n\t"
      "s_waitcnt vmcnt(0)\n\t"
      "v_cmp_eq_u64 vcc, s[48:49], v[202:203]\n\t"
      "s_nop 0\n\t"
      "s_and_b32 s78, vcc_lo, 15\n\t"
      "s_cmp_eq_u32 s78, 15\n\t"
      "s_cbranch_scc1 .Lskip_10_%=\n\t"
      "s_sub_u32 s79, s79, 1\n\t"
      "s_cmp_lg_u32 s79, 0\n\t"
      "s_cbranch_scc1 .Lspin_11_%=\n\t"
      ".Lskip_10_%=:\n\t"
      "s_add_u32 s62, s74, 2\n\t"
      "s_lshl_b32 s62, s62, 12\n\t"
      "s_add_u32 s62, s62, s72\n\t"
      "buffer_load_dwordx4 a[192:195], v1, s[24:27], s62 offen sc1\n\t"
      "ds_read_b64_tr_b16 v[88:89], v24 offset:12288\n\t"
      "ds_read_b64_tr_b16 v[90:91], v24 offset:14336\n\t"
      "s_waitcnt lgkmcnt(12)\n\t"
      "v_mfma_f32_32x32x16_bf16 v[160:175], v[64:67], a[180:183], v[160:175]\n\t"
      "ds_read_b64_tr_b16 v[92:93], v25 offset:12288\n\t"
      "ds_read_b64_tr_b16 v[94:95], v25 offset:14336\n\t"
      "s_waitcnt lgkmcnt(12)\n\t"
      "v_mfma_f32_32x32x16_bf16 v[176:191], v[68:71], a[180:183], v[176:191]\n\t"
      "buffer_load_dwordx4 a[196:199], v1, s[24:27], s62 offen offset:1024 sc1\n\t"
      "ds_read_b64_tr_b16 v[96:97], v23 offset:16384\n\t"
      "ds_read_b64_tr_b16 v[98:99], v23 offset:18432\n\t"
      "s_waitcnt lgkmcnt(12)\n\t"
      "v_mfma_f32_32x32x16_bf16 v[144:159], v[72:75], a[184:187], v[144:159]\n\t"
      "ds_read_b64_tr_b16 v[100:101], v24 offset:16384\n\t"
      "ds_read_b64_tr_b16 v[102:103], v24 offset:18432\n\t"
      "s_waitcnt lgkmcnt(12)\n\t"
      "v_mfma_f32_32x32x16_bf16 v[160:175], v[76:79], a[184:187], v[160:175]\n\t"
      "buffer_load_dwordx4 a[200:203], v1, s[24:27], s62 offen offset:2048 sc1\n\t"
      "ds_read_b64_tr_b16 v[104:105], v25 offset:16384\n\t"
      "ds_read_b64_tr_b16 v[106:107], v25 offset:18432\n\t"
      "s_waitcnt lgkmcnt(12)\n\t"
      "v_mfma_f32_32x32x16_bf16 v[176:191], v[80:83], a[184:187], v[176:191]\n\t"
      "ds_read_b64_tr_b16 v[108:109], v23 offset:20480\n\t"
      "ds_read_b64_tr_b16 v[110:111], v23 offset:22528\n\t"
      "s_waitcnt lgkmcnt(12)\n\t"
      "v_mfma_f32_32x32x16_bf16 v[144:159], v[84:87], a[188:191], v[144:159]\n\t"
      "buffer_load_dwordx4 a[204:207], v1, s[24:27], s62 offen offset:3072 sc1\n\t"
      "ds_read_b64_tr_b16 v[112:113], v24 offset:20480\n\t"
      "ds_read_b64_tr_b16 v[114:115], v24 offset:22528\n\t"
      "s_waitcnt lgkmcnt(12)\n\t"
      "v_mfma_f32_32x32x16_bf16 v[160:175], v[88:91], a[188:191], v[160:175]\n\t"
      "ds_read_b64_tr_b16 v[116:117], v25 offset:20480\n\t"
      "ds_read_b64_tr_b16 v[118:119], v25 offset:22528\n\t"
      "s_waitcnt lgkmcnt(12)\n\t"
      "v_mfma_f32_32x32x16_bf16 v[176:191], v[92:95], a[188:191], v[176:191]\n\t"
      "s_waitcnt vmcnt(4)\n\t"
      "v_cmp_eq_u64 vcc, s[48:49], v[36:37]\n\t"
      "s_nop 0\n\t"
      "s_and_b32 s78, vcc_lo, 240\n\t"
      "s_cmp_eq_u32 s78, 240\n\t"
      "s_cbranch_scc1 .Lskip_12_%=\n\t"
      "s_mov_b32 s79, 32768\n\t"
      ".Lspin_13_%=:\n\t"
      "s_sleep 1\n\t"
      "s_mov_b32 s78, 393216\n\t"
      "s_mov_b32 exec_lo, 240\n\t"
      "s_mov_b32 exec_hi, 0\n\t"
      "buffer_load_dwordx2 v[36:37], v38, s[24:27], s78 offen sc1\n\t"
      "s_mov_b64 exec, -1\n\t"
      "s_waitcnt vmcnt(0)\n\t"
      "v_cmp_eq_u64 vcc, s[48:49], v[36:37]\n\t"
      "s_nop 0\n\t"
      "s_and_b32 s78, vcc_lo, 240\n\t"
      "s_cmp_eq_u32 s78, 240\n\t"
      "s_cbranch_scc1 .Lskip_12_%=\n\t"
      "s_sub_u32 s79, s79, 1\n\t"
      "s_cmp_lg_u32 s79, 0\n\t"
      "s_cbranch_scc1 .Lspin_13_%=\n\t"
      ".Lskip_12_%=:\n\t"
      "s_add_u32 s62, s75, 0\n\t"
      "s_lshl_b32 s62, s62, 12\n\t"
      "s_add_u32 s62, s62, s72\n\t"
      "buffer_load_dwordx4 a[208:211], v1, s[24:27], s62 offen sc1\n\t"
      "ds_read_b64_tr_b16 v[120:121], v23 offset:24576\n\t"
      "ds_read_b64_tr_b16 v[122:123], v23 offset:26624\n\t"
      "s_waitcnt vmcnt(4) lgkmcnt(12)\n\t"
      "v_mfma_f32_32x32x16_bf16 v[144:159], v[96:99], a[192:195], v[144:159]\n\t"
      "ds_read_b64_tr_b16 v[124:125], v24 offset:24576\n\t"
      "ds_read_b64_tr_b16 v[126:127], v24 offset:26624\n\t"
      "s_waitcnt lgkmcnt(12)\n\t"
      "v_mfma_f32_32x32x16_bf16 v[160:175], v[100:103], a[192:195], v[160:175]\n\t"
      "buffer_load_dwordx4 a[212:215], v1, s[24:27], s62 offen offset:1024 sc1\n\t"
      "ds_read_b64_tr_b16 v[64:65], v25 offset:24576\n\t"
      "ds_read_b64_tr_b16 v[66:67], v25 offset:26624\n\t"
      "s_waitcnt lgkmcnt(12)\n\t"
      "v_mfma_f32_32x32x16_bf16 v[176:191], v[104:107], a[192:195], v[176:191]\n\t"
      "ds_read_b64_tr_b16 v[68:69], v23 offset:28672\n\t"
      "ds_read_b64_tr_b16 v[70:71], v23 offset:30720\n\t"
      "s_waitcnt vmcnt(4) lgkmcnt(12)\n\t"
      "v_mfma_f32_32x32x16_bf16 v[144:159], v[108:111], a[196:199], v[144:159]\n\t"
      "buffer_load_dwordx4 a[216:219], v1, s[24:27], s62 offen offset:2048 sc1\n\t"
      "ds_read_b64_tr_b16 v[72:73], v24 offset:28672\n\t"
      "ds_read_b64_tr_b16 v[74:75], v24 offset:30720\n\t"
      "s_waitcnt lgkmcnt(12)\n\t"
      "v_mfma_f32_32x32x16_bf16 v[160:175], v[112:115], a[196:199], v[160:175]\n\t"
      "ds_read_b64_tr_b16 v[76:77], v25 offset:28672\n\t"
      "ds_read_b64_tr_b16 v[78:79], v25 offset:30720\n\t"
      "s_waitcnt lgkmcnt(12)\n\t"
      "v_mfma_f32_32x32x16_bf16 v[176:191], v[116:119], a[196:199], v[176:191]\n\t"
      "buffer_load_dwordx4 a[220:223], v1, s[24:27], s62 offen offset:3072 sc1\n\t"
      "ds_read_b64_tr_b16 v[80:81], v19 offset:0\n\t"
      "ds_read_b64_tr_b16 v[82:83], v19 offset:2048\n\t"
      "s_waitcnt vmcnt(5) lgkmcnt(12)\n\t"
      "v_mfma_f32_32x32x16_bf16 v[144:159], v[120:123], a[200:203], v[144:159]\n\t"
      "ds_read_b64_tr_b16 v[84:85], v19 offset:4096\n\t"
      "ds_read_b64_tr_b16 v[86:87], v19 offset:6144\n\t"
      "s_waitcnt lgkmcnt(12)\n\t"
      "v_mfma_f32_32x32x16_bf16 v[160:175], v[124:127], a[200:203], v[160:175]\n\t"
      "s_add_u32 s62, s75, 1\n\t"
      "s_lshl_b32 s62, s62, 12\n\t"
      "s_add_u32 s62, s62, s72\n\t"
      "buffer_load_dwordx4 a[224:227], v1, s[24:27], s62 offen sc1\n\t"
      "ds_read_b64_tr_b16 v[88:89], v19 offset:8192\n\t"
      "ds_read_b64_tr_b16 v[90:91], v19 offset:10240\n\t"
      "s_waitcnt lgkmcnt(12)\n\t"
      "v_mfma_f32_32x32x16_bf16 v[176:191], v[64:67], a[200:203], v[176:191]\n\t"
      "ds_read_b64_tr_b16 v[92:93], v19 offset:12288\n\t"
      "ds_read_b64_tr_b16 v[94:95], v19 offset:14336\n\t"
      "s_waitcnt vmcnt(5) lgkmcnt(12)\n\t"
      "v_mfma_f32_32x32x16_bf16 v[144:159], v[68:71], a[204:207], v[144:159]\n\t"
      "buffer_load_dwordx4 a[228:231], v1, s[24:27], s62 offen offset:1024 sc1\n\t"
      "s_waitcnt lgkmcnt(10)\n\t"
      "v_mfma_f32_32x32x16_bf16 v[160:175], v[72:75], a[204:207], v[160:175]\n\t"
      "s_waitcnt lgkmcnt(0)\n\t"
      "s_barrier\n\t"
      "ds_read_b64_tr_b16 v[96:97], v15 offset:32768\n\t"
      "ds_read_b64_tr_b16 v[98:99], v15 offset:34816\n\t"
      "ds_read_b64_tr_b16 v[100:101], v15 offset:36864\n\t"
      "ds_read_b64_tr_b16 v[102:103], v15 offset:38912\n\t"
      "v_mfma_f32_32x32x16_bf16 v[176:191], v[76:79], a[204:207], v[176:191]\n\t"
      "buffer_load_dwordx4 a[232:235], v1, s[24:27], s62 offen offset:2048 sc1\n\t"
      "ds_read_b64_tr_b16 v[104:105], v15 offset:40960\n\t"
      "ds_read_b64_tr_b16 v[106:107], v15 offset:43008\n\t"
      "s_waitcnt vmcnt(6)\n\t"
      "v_mfma_f32_32x32x16_bf16 v[144:159], v[80:83], a[208:211], v[144:159]\n\t"
      "ds_read_b64_tr_b16 v[108:109], v15 offset:45056\n\t"
      "ds_read_b64_tr_b16 v[110:111], v15 offset:47104\n\t"
      "s_waitcnt vmcnt(5)\n\t"
      "v_mfma_f32_32x32x16_bf16 v[144:159], v[84:87], a[212:215], v[144:159]\n\t"
      "buffer_load_dwordx4 a[236:239], v1, s[24:27], s62 offen offset:3072 sc1\n\t"
      "ds_read_b64_tr_b16 v[112:113], v20 offset:0\n\t"
      "ds_read_b64_tr_b16 v[114:115], v20 offset:2048\n\t"
      "s_waitcnt vmcnt(5)\n\t"
      "v_mfma_f32_32x32x16_bf16 v[144:159], v[88:91], a[216:219], v[144:159]\n\t"
      "ds_read_b64_tr_b16 v[116:117], v20 offset:4096\n\t"
      "ds_read_b64_tr_b16 v[118:119], v20 offset:6144\n\t"
      "s_waitcnt vmcnt(4)\n\t"
      "v_mfma_f32_32x32x16_bf16 v[144:159], v[92:95], a[220:223], v[144:159]\n\t"
      "s_mov_b32 s78, 524288\n\t"
      "s_mov_b32 exec_lo, 15\n\t"
      "s_mov_b32 exec_hi, 0\n\t"
      "buffer_load_dwordx2 v[36:37], v38, s[24:27], s78 offen sc1\n\t"
      "s_mov_b64 exec, -1\n\t"
      "ds_read_b64_tr_b16 v[120:121], v20 offset:8192\n\t"
      "ds_read_b64_tr_b16 v[122:123], v20 offset:10240\n\t"
      "s_waitcnt vmcnt(4) lgkmcnt(12)\n\t"
      "v_mfma_f32_32x32x16_bf16 v[144:159], v[96:99], a[224:227], v[144:159]\n\t"
      "ds_read_b64_tr_b16 v[124:125], v20 offset:12288\n\t"
      "ds_read_b64_tr_b16 v[126:127], v20 offset:14336\n\t"
      "s_waitcnt vmcnt(3) lgkmcnt(12)\n\t"
      "v_mfma_f32_32x32x16_bf16 v[144:159], v[100:103], a[228:231], v[144:159]\n\t"
      "ds_read_b64_tr_b16 v[64:65], v16 offset:32768\n\t"
      "ds_read_b64_tr_b16 v[66:67], v16 offset:34816\n\t"
      "s_waitcnt vmcnt(2) lgkmcnt(12)\n\t"
      "v_mfma_f32_32x32x16_bf16 v[144:159], v[104:107], a[232:235], v[144:159]\n\t"
      "ds_read_b64_tr_b16 v[68:69], v16 offset:36864\n\t"
      "ds_read_b64_tr_b16 v[70:71], v16 offset:38912\n\t"
      "s_waitcnt vmcnt(1) lgkmcnt(12)\n\t"
      "v_mfma_f32_32x32x16_bf16 v[144:159], v[108:111], a[236:239], v[144:159]\n\t"
      "ds_read_b64_tr_b16 v[72:73], v16 offset:40960\n\t"
      "ds_read_b64_tr_b16 v[74:75], v16 offset:43008\n\t"
      "s_waitcnt lgkmcnt(12)\n\t"
      "v_mfma_f32_32x32x16_bf16 v[160:175], v[112:115], a[208:211], v[160:175]\n\t"
      "s_nop 0\n\t"
      "s_nop 0\n\t"
      "s_nop 0\n\t"
      "ds_read_b64_tr_b16 v[76:77], v16 offset:45056\n\t"
      "ds_read_b64_tr_b16 v[78:79], v16 offset:47104\n\t"
      "s_waitcnt lgkmcnt(12)\n\t"
      "v_mfma_f32_32x32x16_bf16 v[160:175], v[116:119], a[212:215], v[160:175]\n\t"
      "s_waitcnt vmcnt(0)\n\t"
      "v_cmp_eq_u64 vcc, s[48:49], v[36:37]\n\t"
      "s_nop 0\n\t"
      "s_and_b32 s78, vcc_lo, 15\n\t"
      "s_cmp_eq_u32 s78, 15\n\t"
      "s_cbranch_scc1 .Lskip_14_%=\n\t"
      "s_mov_b32 s79, 32768\n\t"
      ".Lspin_15_%=:\n\t"
      "s_sleep 1\n\t"
      "s_mov_b32 s78, 524288\n\t"
      "s_mov_b32 exec_lo, 15\n\t"
      "s_mov_b32 exec_hi, 0\n\t"
      "buffer_load_dwordx2 v[36:37], v38, s[24:27], s78 offen sc1\n\t"
      "s_mov_b64 exec, -1\n\t"
      "s_waitcnt vmcnt(0)\n\t"
      "v_cmp_eq_u64 vcc, s[48:49], v[36:37]\n\t"
      "s_nop 0\n\t"
      "s_and_b32 s78, vcc_lo, 15\n\t"
      "s_cmp_eq_u32 s78, 15\n\t"
      "s_cbranch_scc1 .Lskip_14_%=\n\t"
      "s_sub_u32 s79, s79, 1\n\t"
      "s_cmp_lg_u32 s79, 0\n\t"
      "s_cbranch_scc1 .Lspin_15_%=\n\t"
      ".Lskip_14_%=:\n\t"
      "s_add_u32 s62, s75, 28\n\t"
      "s_lshl_b32 s62, s62, 12\n\t"
      "s_add_u32 s62, s62, s72\n\t"
      "buffer_load_dwordx4 a[240:243], v1, s[24:27], s62 offen sc1\n\t"
      "v_mul_f32 v144, v144, v60\n\t"
      "v_mul_f32 v145, v145, v60\n\t"
      "v_mul_f32 v146, v146, v60\n\t"
      "ds_read_b64_tr_b16 v[80:81], v21 offset:0\n\t"
      "ds_read_b64_tr_b16 v[82:83], v21 offset:2048\n\t"
      "s_waitcnt lgkmcnt(12)\n\t"
      "v_mfma_f32_32x32x16_bf16 v[160:175], v[120:123], a[216:219], v[160:175]\n\t"
      "v_mul_f32 v147, v147, v60\n\t"
      "v_mul_f32 v148, v148, v60\n\t"
      "v_mul_f32 v149, v149, v60\n\t"
      "ds_read_b64_tr_b16 v[84:85], v21 offset:4096\n\t"
      "ds_read_b64_tr_b16 v[86:87], v21 offset:6144\n\t"
      "s_waitcnt lgkmcnt(12)\n\t"
      "v_mfma_f32_32x32x16_bf16 v[160:175], v[124:127], a[220:223], v[160:175]\n\t"
      "buffer_load_dwordx4 a[244:247], v1, s[24:27], s62 offen offset:1024 sc1\n\t"
      "v_mul_f32 v150, v150, v60\n\t"
      "v_mul_f32 v151, v151, v60\n\t"
      "v_mul_f32 v152, v152, v60\n\t"
      "ds_read_b64_tr_b16 v[88:89], v21 offset:8192\n\t"
      "ds_read_b64_tr_b16 v[90:91], v21 offset:10240\n\t"
      "s_waitcnt lgkmcnt(12)\n\t"
      "v_mfma_f32_32x32x16_bf16 v[160:175], v[64:67], a[224:227], v[160:175]\n\t"
      "v_mul_f32 v153, v153, v60\n\t"
      "v_mul_f32 v154, v154, v60\n\t"
      "ds_read_b64_tr_b16 v[92:93], v21 offset:12288\n\t"
      "ds_read_b64_tr_b16 v[94:95], v21 offset:14336\n\t"
      "s_waitcnt lgkmcnt(12)\n\t"
      "v_mfma_f32_32x32x16_bf16 v[160:175], v[68:71], a[228:231], v[160:175]\n\t"
      "buffer_load_dwordx4 a[248:251], v1, s[24:27], s62 offen offset:2048 sc1\n\t"
      "v_mul_f32 v155, v155, v60\n\t"
      "v_mul_f32 v156, v156, v60\n\t"
      "v_mul_f32 v157, v157, v60\n\t"
      "ds_read_b64_tr_b16 v[96:97], v17 offset:32768\n\t"
      "ds_read_b64_tr_b16 v[98:99], v17 offset:34816\n\t"
      "s_waitcnt lgkmcnt(12)\n\t"
      "v_mfma_f32_32x32x16_bf16 v[160:175], v[72:75], a[232:235], v[160:175]\n\t"
      "v_mul_f32 v158, v158, v60\n\t"
      "v_mul_f32 v159, v159, v60\n\t"
      "ds_read_b64_tr_b16 v[100:101], v17 offset:36864\n\t"
      "ds_read_b64_tr_b16 v[102:103], v17 offset:38912\n\t"
      "s_waitcnt lgkmcnt(12)\n\t"
      "v_mfma_f32_32x32x16_bf16 v[160:175], v[76:79], a[236:239], v[160:175]\n\t"
      "buffer_load_dwordx4 a[252:255], v1, s[24:27], s62 offen offset:3072 sc1\n\t"
      "ds_read_b64_tr_b16 v[104:105], v17 offset:40960\n\t"
      "ds_read_b64_tr_b16 v[106:107], v17 offset:43008\n\t"
      "s_waitcnt lgkmcnt(12)\n\t"
      "v_mfma_f32_32x32x16_bf16 v[176:191], v[80:83], a[208:211], v[176:191]\n\t"
      "s_nop 0\n\t"
      "s_nop 0\n\t"
      "s_nop 0\n\t"
      "ds_read_b64_tr_b16 v[108:109], v17 offset:45056\n\t"
      "ds_read_b64_tr_b16 v[110:111], v17 offset:47104\n\t"
      "s_waitcnt lgkmcnt(12)\n\t"
      "v_mfma_f32_32x32x16_bf16 v[176:191], v[84:87], a[212:215], v[176:191]\n\t"
      "s_add_u32 s62, s75, 29\n\t"
      "s_lshl_b32 s62, s62, 12\n\t"
      "s_add_u32 s62, s62, s72\n\t"
      "buffer_load_dwordx4 a[0:3], v1, s[24:27], s62 offen sc1\n\t"
      "v_mul_f32 v160, v160, v60\n\t"
      "v_mul_f32 v161, v161, v60\n\t"
      "v_mul_f32 v162, v162, v60\n\t"
      "ds_read_b64_tr_b16 v[112:113], v15 offset:0\n\t"
      "ds_read_b64_tr_b16 v[114:115], v15 offset:2048\n\t"
      "s_waitcnt lgkmcnt(12)\n\t"
      "v_mfma_f32_32x32x16_bf16 v[176:191], v[88:91], a[216:219], v[176:191]\n\t"
      "v_mul_f32 v163, v163, v60\n\t"
      "v_mul_f32 v164, v164, v60\n\t"
      "v_mul_f32 v165, v165, v60\n\t"
      "ds_read_b64_tr_b16 v[116:117], v15 offset:4096\n\t"
      "ds_read_b64_tr_b16 v[118:119], v15 offset:6144\n\t"
      "s_waitcnt lgkmcnt(12)\n\t"
      "v_mfma_f32_32x32x16_bf16 v[176:191], v[92:95], a[220:223], v[176:191]\n\t"
      "buffer_load_dwordx4 a[4:7], v1, s[24:27], s62 offen offset:1024 sc1\n\t"
      "v_mul_f32 v166, v166, v60\n\t"
      "v_mul_f32 v167, v167, v60\n\t"
      "v_mul_f32 v168, v168, v60\n\t"
      "ds_read_b64_tr_b16 v[120:121], v15 offset:8192\n\t"
      "ds_read_b64_tr_b16 v[122:123], v15 offset:10240\n\t"
      "s_waitcnt lgkmcnt(12)\n\t"
      "v_mfma_f32_32x32x16_bf16 v[176:191], v[96:99], a[224:227], v[176:191]\n\t"
      "v_mul_f32 v169, v169, v60\n\t"
      "v_mul_f32 v170, v170, v60\n\t"
      "ds_read_b64_tr_b16 v[124:125], v15 offset:12288\n\t"
      "ds_read_b64_tr_b16 v[126:127], v15 offset:14336\n\t"
      "s_waitcnt lgkmcnt(12)\n\t"
      "v_mfma_f32_32x32x16_bf16 v[176:191], v[100:103], a[228:231], v[176:191]\n\t"
      "buffer_load_dwordx4 a[8:11], v1, s[24:27], s62 offen offset:2048 sc1\n\t"
      "v_mul_f32 v171, v171, v60\n\t"
      "v_mul_f32 v172, v172, v60\n\t"
      "v_mul_f32 v173, v173, v60\n\t"
      "ds_read_b64_tr_b16 v[64:65], v15 offset:16384\n\t"
      "ds_read_b64_tr_b16 v[66:67], v15 offset:18432\n\t"
      "s_waitcnt lgkmcnt(12)\n\t"
      "v_mfma_f32_32x32x16_bf16 v[176:191], v[104:107], a[232:235], v[176:191]\n\t"
      "v_mul_f32 v174, v174, v60\n\t"
      "v_mul_f32 v175, v175, v60\n\t"
      "ds_read_b64_tr_b16 v[68:69], v15 offset:20480\n\t"
      "ds_read_b64_tr_b16 v[70:71], v15 offset:22528\n\t"
      "s_waitcnt lgkmcnt(12)\n\t"
      "v_mfma_f32_32x32x16_bf16 v[176:191], v[108:111], a[236:239], v[176:191]\n\t"
      "buffer_load_dwordx4 a[12:15], v1, s[24:27], s62 offen offset:3072 sc1\n\t"
      "ds_read_b64_tr_b16 v[72:73], v15 offset:24576\n\t"
      "ds_read_b64_tr_b16 v[74:75], v15 offset:26624\n\t"
      "s_nop 0\n\t"
      "s_nop 0\n\t"
      "s_nop 0\n\t"
      "s_waitcnt vmcnt(7) lgkmcnt(12)\n\t"
      "v_mfma_f32_32x32x16_bf16 v[144:159], v[112:115], a[240:243], v[144:159]\n\t"
      "s_or_b32 s78, s2, s40\n\t"
      "s_cmp_lg_u32 s78, 0\n\t"
      "s_cbranch_scc1 .Lskip_16_%=\n\t"
      "v_lshlrev_b32 v248, 9, v0\n\t"
      "v_add_u32 v248, 3145728, v248\n\t"
      "s_mov_b32 s78, 393216\n\t"
      "buffer_load_dwordx2 v[240:241], v248, s[24:27], s78 offen sc1\n\t"
      "v_lshlrev_b32 v249, 9, v0\n\t"
      "v_add_u32 v249, 3178496, v249\n\t"
      "s_mov_b32 s78, 393216\n\t"
      "buffer_load_dwordx2 v[242:243], v249, s[24:27], s78 offen sc1\n\t"
      "v_lshlrev_b32 v250, 9, v0\n\t"
      "v_add_u32 v250, 3211264, v250\n\t"
      "s_mov_b32 s78, 393216\n\t"
      "buffer_load_dwordx2 v[244:245], v250, s[24:27], s78 offen sc1\n\t"
      "v_lshlrev_b32 v251, 9, v0\n\t"
      "v_add_u32 v251, 3244032, v251\n\t"
      "s_mov_b32 s78, 393216\n\t"
      "buffer_load_dwordx2 v[246:247], v251, s[24:27], s78 offen sc1\n\t"
      ".Lskip_16_%=:\n\t"
      "s_nop 0\n\t"
      "v_mul_f32 v176, v176, v60\n\t"
      "v_mul_f32 v177, v177, v60\n\t"
      "v_mul_f32 v178, v178, v60\n\t"
      "ds_read_b64_tr_b16 v[76:77], v15 offset:28672\n\t"
      "ds_read_b64_tr_b16 v[78:79], v15 offset:30720\n\t"
      "s_waitcnt vmcnt(6) lgkmcnt(12)\n\t"
      "v_mfma_f32_32x32x16_bf16 v[144:159], v[116:119], a[244:247], v[144:159]\n\t"
      "v_mul_f32 v179, v179, v60\n\t"
      "v_mul_f32 v180, v180, v60\n\t"
      "v_mul_f32 v181, v181, v60\n\t"
      "ds_read_b64_tr_b16 v[80:81], v16 offset:0\n\t"
      "ds_read_b64_tr_b16 v[82:83], v16 offset:2048\n\t"
      "s_waitcnt vmcnt(5) lgkmcnt(12)\n\t"
      "v_mfma_f32_32x32x16_bf16 v[144:159], v[120:123], a[248:251], v[144:159]\n\t"
      "v_mul_f32 v182, v182, v60\n\t"
      "v_mul_f32 v183, v183, v60\n\t"
      "v_mul_f32 v184, v184, v60\n\t"
      "ds_read_b64_tr_b16 v[84:85], v16 offset:4096\n\t"
      "ds_read_b64_tr_b16 v[86:87], v16 offset:6144\n\t"
      "s_waitcnt vmcnt(4) lgkmcnt(12)\n\t"
      "v_mfma_f32_32x32x16_bf16 v[144:159], v[124:127], a[252:255], v[144:159]\n\t"
      "v_mul_f32 v185, v185, v60\n\t"
      "v_mul_f32 v186, v186, v60\n\t"
      "v_mul_f32 v187, v187, v60\n\t"
      "ds_read_b64_tr_b16 v[88:89], v16 offset:8192\n\t"
      "ds_read_b64_tr_b16 v[90:91], v16 offset:10240\n\t"
      "s_waitcnt vmcnt(3) lgkmcnt(12)\n\t"
      "v_mfma_f32_32x32x16_bf16 v[144:159], v[64:67], a[0:3], v[144:159]\n\t"
      "v_mul_f32 v188, v188, v60\n\t"
      "v_mul_f32 v189, v189, v60\n\t"
      "ds_read_b64_tr_b16 v[92:93], v16 offset:12288\n\t"
      "ds_read_b64_tr_b16 v[94:95], v16 offset:14336\n\t"
      "s_waitcnt vmcnt(2) lgkmcnt(12)\n\t"
      "v_mfma_f32_32x32x16_bf16 v[144:159], v[68:71], a[4:7], v[144:159]\n\t"
      "v_mul_f32 v190, v190, v60\n\t"
      "v_mul_f32 v191, v191, v60\n\t"
      "ds_read_b64_tr_b16 v[96:97], v16 offset:16384\n\t"
      "ds_read_b64_tr_b16 v[98:99], v16 offset:18432\n\t"
      "s_waitcnt vmcnt(1) lgkmcnt(12)\n\t"
      "v_mfma_f32_32x32x16_bf16 v[144:159], v[72:75], a[8:11], v[144:159]\n\t"
      "ds_read_b64_tr_b16 v[100:101], v16 offset:20480\n\t"
      "ds_read_b64_tr_b16 v[102:103], v16 offset:22528\n\t"
      "s_waitcnt vmcnt(0) lgkmcnt(12)\n\t"
      "v_mfma_f32_32x32x16_bf16 v[144:159], v[76:79], a[12:15], v[144:159]\n\t"
      "ds_read_b64_tr_b16 v[104:105], v16 offset:24576\n\t"
      "ds_read_b64_tr_b16 v[106:107], v16 offset:26624\n\t"
      "s_nop 0\n\t"
      "s_nop 0\n\t"
      "s_nop 0\n\t"
      "s_waitcnt lgkmcnt(12)\n\t"
      "v_mfma_f32_32x32x16_bf16 v[160:175], v[80:83], a[240:243], v[160:175]\n\t"
      "s_nop 4\n\t"
      "v_cvt_pk_bf16_f32 v208, v144, v145\n\t"
      "v_cvt_pk_bf16_f32 v209, v146, v147\n\t"
      "v_cvt_pk_bf16_f32 v210, v148, v149\n\t"
      "ds_read_b64_tr_b16 v[108:109], v16 offset:28672\n\t"
      "ds_read_b64_tr_b16 v[110:111], v16 offset:30720\n\t"
      "s_waitcnt lgkmcnt(12)\n\t"
      "v_mfma_f32_32x32x16_bf16 v[160:175], v[84:87], a[244:247], v[160:175]\n\t"
      "s_or_b32 s78, s2, s40\n\t"
      "s_cmp_lg_u32 s78, 0\n\t"
      "s_cbranch_scc1 .Lskip_17_%=\n\t"
      "s_waitcnt vmcnt(0)\n\t"
      "v_cmp_eq_u64 vcc, s[48:49], v[240:241]\n\t"
      "s_nop 0\n\t"
      "s_cmp_eq_u64 vcc, exec\n\t"
      "s_cbranch_scc1 .Lskip_18_%=\n\t"
      "s_mov_b32 s79, 32768\n\t"
      ".Lspin_19_%=:\n\t"
      "s_sleep 1\n\t"
      "s_mov_b32 s78, 393216\n\t"
      "buffer_load_dwordx2 v[240:241], v248, s[24:27], s78 offen sc1\n\t"
      "s_waitcnt vmcnt(0)\n\t"
      "v_cmp_eq_u64 vcc, s[48:49], v[240:241]\n\t"
      "s_nop 0\n\t"
      "s_cmp_eq_u64 vcc, exec\n\t"
      "s_cbranch_scc1 .Lskip_18_%=\n\t"
      "s_sub_u32 s79, s79, 1\n\t"
      "s_cmp_lg_u32 s79, 0\n\t"
      "s_cbranch_scc1 .Lspin_19_%=\n\t"
      ".Lskip_18_%=:\n\t"
      "s_waitcnt vmcnt(0)\n\t"
      "v_cmp_eq_u64 vcc, s[48:49], v[242:243]\n\t"
      "s_nop 0\n\t"
      "s_cmp_eq_u64 vcc, exec\n\t"
      "s_cbranch_scc1 .Lskip_20_%=\n\t"
      "s_mov_b32 s79, 32768\n\t"
      ".Lspin_21_%=:\n\t"
      "s_sleep 1\n\t"
      "s_mov_b32 s78, 393216\n\t"
      "buffer_load_dwordx2 v[242:243], v249, s[24:27], s78 offen sc1\n\t"
      "s_waitcnt vmcnt(0)\n\t"
      "v_cmp_eq_u64 vcc, s[48:49], v[242:243]\n\t"
      "s_nop 0\n\t"
      "s_cmp_eq_u64 vcc, exec\n\t"
      "s_cbranch_scc1 .Lskip_20_%=\n\t"
      "s_sub_u32 s79, s79, 1\n\t"
      "s_cmp_lg_u32 s79, 0\n\t"
      "s_cbranch_scc1 .Lspin_21_%=\n\t"
      ".Lskip_20_%=:\n\t"
      "s_waitcnt vmcnt(0)\n\t"
      "v_cmp_eq_u64 vcc, s[48:49], v[244:245]\n\t"
      "s_nop 0\n\t"
      "s_cmp_eq_u64 vcc, exec\n\t"
      "s_cbranch_scc1 .Lskip_22_%=\n\t"
      "s_mov_b32 s79, 32768\n\t"
      ".Lspin_23_%=:\n\t"
      "s_sleep 1\n\t"
      "s_mov_b32 s78, 393216\n\t"
      "buffer_load_dwordx2 v[244:245], v250, s[24:27], s78 offen sc1\n\t"
      "s_waitcnt vmcnt(0)\n\t"
      "v_cmp_eq_u64 vcc, s[48:49], v[244:245]\n\t"
      "s_nop 0\n\t"
      "s_cmp_eq_u64 vcc, exec\n\t"
      "s_cbranch_scc1 .Lskip_22_%=\n\t"
      "s_sub_u32 s79, s79, 1\n\t"
      "s_cmp_lg_u32 s79, 0\n\t"
      "s_cbranch_scc1 .Lspin_23_%=\n\t"
      ".Lskip_22_%=:\n\t"
      "s_waitcnt vmcnt(0)\n\t"
      "v_cmp_eq_u64 vcc, s[48:49], v[246:247]\n\t"
      "s_nop 0\n\t"
      "s_cmp_eq_u64 vcc, exec\n\t"
      "s_cbranch_scc1 .Lskip_24_%=\n\t"
      "s_mov_b32 s79, 32768\n\t"
      ".Lspin_25_%=:\n\t"
      "s_sleep 1\n\t"
      "s_mov_b32 s78, 393216\n\t"
      "buffer_load_dwordx2 v[246:247], v251, s[24:27], s78 offen sc1\n\t"
      "s_waitcnt vmcnt(0)\n\t"
      "v_cmp_eq_u64 vcc, s[48:49], v[246:247]\n\t"
      "s_nop 0\n\t"
      "s_cmp_eq_u64 vcc, exec\n\t"
      "s_cbranch_scc1 .Lskip_24_%=\n\t"
      "s_sub_u32 s79, s79, 1\n\t"
      "s_cmp_lg_u32 s79, 0\n\t"
      "s_cbranch_scc1 .Lspin_25_%=\n\t"
      ".Lskip_24_%=:\n\t"
      "s_mov_b64 exec, 1\n\t"
      "buffer_store_dwordx2 v[62:63], off, s[24:27], 0 sc1\n\t"
      "s_mov_b64 exec, -1\n\t"
      "s_nop 0\n\t"
      ".Lskip_17_%=:\n\t"
      "v_cvt_pk_bf16_f32 v211, v150, v151\n\t"
      "v_cvt_pk_bf16_f32 v212, v152, v153\n\t"
      "v_cvt_pk_bf16_f32 v213, v154, v155\n\t"
      "ds_read_b64_tr_b16 v[112:113], v17 offset:0\n\t"
      "ds_read_b64_tr_b16 v[114:115], v17 offset:2048\n\t"
      "s_waitcnt lgkmcnt(12)\n\t"
      "v_mfma_f32_32x32x16_bf16 v[160:175], v[88:91], a[248:251], v[160:175]\n\t"
      "v_cvt_pk_bf16_f32 v214, v156, v157\n\t"
      "v_cvt_pk_bf16_f32 v215, v158, v159\n\t"
      "buffer_store_dwordx4 v[208:211], v1, s[24:27], s32 offen sc1\n\t"
      "buffer_store_dwordx4 v[212:215], v1, s[24:27], s32 offen offset:1024 sc1\n\t"
      "ds_read_b64_tr_b16 v[116:117], v17 offset:4096\n\t"
      "ds_read_b64_tr_b16 v[118:119], v17 offset:6144\n\t"
      "s_waitcnt lgkmcnt(12)\n\t"
      "v_mfma_f32_32x32x16_bf16 v[160:175], v[92:95], a[252:255], v[160:175]\n\t"
      "ds_read_b64_tr_b16 v[120:121], v17 offset:8192\n\t"
      "ds_read_b64_tr_b16 v[122:123], v17 offset:10240\n\t"
      "s_waitcnt lgkmcnt(12)\n\t"
      "v_mfma_f32_32x32x16_bf16 v[160:175], v[96:99], a[0:3], v[160:175]\n\t"
      "ds_read_b64_tr_b16 v[124:125], v17 offset:12288\n\t"
      "ds_read_b64_tr_b16 v[126:127], v17 offset:14336\n\t"
      "s_waitcnt lgkmcnt(12)\n\t"
      "v_mfma_f32_32x32x16_bf16 v[160:175], v[100:103], a[4:7], v[160:175]\n\t"
      "ds_read_b64_tr_b16 v[64:65], v17 offset:16384\n\t"
      "ds_read_b64_tr_b16 v[66:67], v17 offset:18432\n\t"
      "s_waitcnt lgkmcnt(12)\n\t"
      "v_mfma_f32_32x32x16_bf16 v[160:175], v[104:107], a[8:11], v[160:175]\n\t"
      "ds_read_b64_tr_b16 v[68:69], v17 offset:20480\n\t"
      "ds_read_b64_tr_b16 v[70:71], v17 offset:22528\n\t"
      "s_waitcnt lgkmcnt(12)\n\t"
      "v_mfma_f32_32x32x16_bf16 v[160:175], v[108:111], a[12:15], v[160:175]\n\t"
      "ds_read_b64_tr_b16 v[72:73], v17 offset:24576\n\t"
      "ds_read_b64_tr_b16 v[74:75], v17 offset:26624\n\t"
      "s_nop 0\n\t"
      "s_nop 0\n\t"
      "s_nop 0\n\t"
      "s_waitcnt lgkmcnt(12)\n\t"
      "v_mfma_f32_32x32x16_bf16 v[176:191], v[112:115], a[240:243], v[176:191]\n\t"
      "s_nop 4\n\t"
      "v_cvt_pk_bf16_f32 v216, v160, v161\n\t"
      "v_cvt_pk_bf16_f32 v217, v162, v163\n\t"
      "v_cvt_pk_bf16_f32 v218, v164, v165\n\t"
      "ds_read_b64_tr_b16 v[76:77], v17 offset:28672\n\t"
      "ds_read_b64_tr_b16 v[78:79], v17 offset:30720\n\t"
      "s_waitcnt lgkmcnt(12)\n\t"
      "v_mfma_f32_32x32x16_bf16 v[176:191], v[116:119], a[244:247], v[176:191]\n\t"
      "v_cvt_pk_bf16_f32 v219, v166, v167\n\t"
      "v_cvt_pk_bf16_f32 v220, v168, v169\n\t"
      "v_cvt_pk_bf16_f32 v221, v170, v171\n\t"
      "ds_read_b64_tr_b16 v[80:81], v18 offset:49152\n\t"
      "ds_read_b64_tr_b16 v[82:83], v18 offset:51200\n\t"
      "s_waitcnt lgkmcnt(12)\n\t"
      "v_mfma_f32_32x32x16_bf16 v[176:191], v[120:123], a[248:251], v[176:191]\n\t"
      "v_cvt_pk_bf16_f32 v222, v172, v173\n\t"
      "v_cvt_pk_bf16_f32 v223, v174, v175\n\t"
      "buffer_store_dwordx4 v[216:219], v1, s[24:27], s33 offen sc1\n\t"
      "buffer_store_dwordx4 v[220:223], v1, s[24:27], s33 offen offset:1024 sc1\n\t"
      "ds_read_b64_tr_b16 v[84:85], v18 offset:53248\n\t"
      "ds_read_b64_tr_b16 v[86:87], v18 offset:55296\n\t"
      "s_waitcnt lgkmcnt(12)\n\t"
      "v_mfma_f32_32x32x16_bf16 v[176:191], v[124:127], a[252:255], v[176:191]\n\t"
      "ds_read_b64_tr_b16 v[88:89], v18 offset:57344\n\t"
      "ds_read_b64_tr_b16 v[90:91], v18 offset:59392\n\t"
      "s_waitcnt lgkmcnt(12)\n\t"
      "v_mfma_f32_32x32x16_bf16 v[176:191], v[64:67], a[0:3], v[176:191]\n\t"
      "ds_read_b64_tr_b16 v[92:93], v18 offset:61440\n\t"
      "ds_read_b64_tr_b16 v[94:95], v18 offset:63488\n\t"
      "s_waitcnt lgkmcnt(12)\n\t"
      "v_mfma_f32_32x32x16_bf16 v[176:191], v[68:71], a[4:7], v[176:191]\n\t"
      "ds_read_b64_tr_b16 v[96:97], v22 offset:0\n\t"
      "ds_read_b64_tr_b16 v[98:99], v22 offset:2048\n\t"
      "s_waitcnt lgkmcnt(12)\n\t"
      "v_mfma_f32_32x32x16_bf16 v[176:191], v[72:75], a[8:11], v[176:191]\n\t"
      "ds_read_b64_tr_b16 v[100:101], v22 offset:4096\n\t"
      "ds_read_b64_tr_b16 v[102:103], v22 offset:6144\n\t"
      "s_waitcnt lgkmcnt(12)\n\t"
      "v_mfma_f32_32x32x16_bf16 v[176:191], v[76:79], a[12:15], v[176:191]\n\t"
      "ds_read_b64_tr_b16 v[104:105], v22 offset:8192\n\t"
      "ds_read_b64_tr_b16 v[106:107], v22 offset:10240\n\t"
      "s_waitcnt lgkmcnt(12)\n\t"
      "v_mfma_f32_32x32x16_bf16 v[128:143], v[80:83], a[160:163], v[128:143]\n\t"
      "s_nop 0\n\t"
      "s_nop 0\n\t"
      "s_nop 0\n\t"
      "ds_read_b64_tr_b16 v[108:109], v22 offset:12288\n\t"
      "ds_read_b64_tr_b16 v[110:111], v22 offset:14336\n\t"
      "s_waitcnt lgkmcnt(12)\n\t"
      "v_mfma_f32_32x32x16_bf16 v[128:143], v[84:87], a[164:167], v[128:143]\n\t"
      "s_nop 0\n\t"
      "v_cvt_pk_bf16_f32 v224, v176, v177\n\t"
      "v_cvt_pk_bf16_f32 v225, v178, v179\n\t"
      "v_cvt_pk_bf16_f32 v226, v180, v181\n\t"
      "ds_read_b64_tr_b16 v[112:113], v22 offset:16384\n\t"
      "ds_read_b64_tr_b16 v[114:115], v22 offset:18432\n\t"
      "s_waitcnt lgkmcnt(12)\n\t"
      "v_mfma_f32_32x32x16_bf16 v[128:143], v[88:91], a[168:171], v[128:143]\n\t"
      "v_cvt_pk_bf16_f32 v227, v182, v183\n\t"
      "v_cvt_pk_bf16_f32 v228, v184, v185\n\t"
      "v_cvt_pk_bf16_f32 v229, v186, v187\n\t"
      "ds_read_b64_tr_b16 v[116:117], v22 offset:20480\n\t"
      "ds_read_b64_tr_b16 v[118:119], v22 offset:22528\n\t"
      "s_waitcnt lgkmcnt(12)\n\t"
      "v_mfma_f32_32x32x16_bf16 v[128:143], v[92:95], a[172:175], v[128:143]\n\t"
      "v_cvt_pk_bf16_f32 v230, v188, v189\n\t"
      "v_cvt_pk_bf16_f32 v231, v190, v191\n\t"
      "buffer_store_dwordx4 v[224:227], v1, s[24:27], s34 offen sc1\n\t"
      "buffer_store_dwordx4 v[228:231], v1, s[24:27], s34 offen offset:1024 sc1\n\t"
      "ds_read_b64_tr_b16 v[120:121], v22 offset:24576\n\t"
      "ds_read_b64_tr_b16 v[122:123], v22 offset:26624\n\t"
      "s_waitcnt lgkmcnt(12)\n\t"
      "v_mfma_f32_32x32x16_bf16 v[128:143], v[96:99], a[176:179], v[128:143]\n\t"
      "ds_read_b64_tr_b16 v[124:125], v22 offset:28672\n\t"
      "ds_read_b64_tr_b16 v[126:127], v22 offset:30720\n\t"
      "s_waitcnt lgkmcnt(12)\n\t"
      "v_mfma_f32_32x32x16_bf16 v[128:143], v[100:103], a[180:183], v[128:143]\n\t"
      "ds_read_b64_tr_b16 v[64:65], v18 offset:0\n\t"
      "ds_read_b64_tr_b16 v[66:67], v18 offset:2048\n\t"
      "s_waitcnt lgkmcnt(12)\n\t"
      "v_mfma_f32_32x32x16_bf16 v[128:143], v[104:107], a[184:187], v[128:143]\n\t"
      "ds_read_b64_tr_b16 v[68:69], v18 offset:4096\n\t"
      "ds_read_b64_tr_b16 v[70:71], v18 offset:6144\n\t"
      "s_waitcnt lgkmcnt(12)\n\t"
      "v_mfma_f32_32x32x16_bf16 v[128:143], v[108:111], a[188:191], v[128:143]\n\t"
      "ds_read_b64_tr_b16 v[72:73], v18 offset:8192\n\t"
      "ds_read_b64_tr_b16 v[74:75], v18 offset:10240\n\t"
      "s_waitcnt lgkmcnt(12)\n\t"
      "v_mfma_f32_32x32x16_bf16 v[128:143], v[112:115], a[192:195], v[128:143]\n\t"
      "ds_read_b64_tr_b16 v[76:77], v18 offset:12288\n\t"
      "ds_read_b64_tr_b16 v[78:79], v18 offset:14336\n\t"
      "s_waitcnt lgkmcnt(12)\n\t"
      "v_mfma_f32_32x32x16_bf16 v[128:143], v[116:119], a[196:199], v[128:143]\n\t"
      "ds_read_b64_tr_b16 v[80:81], v14 offset:32768\n\t"
      "ds_read_b64_tr_b16 v[82:83], v14 offset:34816\n\t"
      "s_waitcnt lgkmcnt(12)\n\t"
      "v_mfma_f32_32x32x16_bf16 v[128:143], v[120:123], a[200:203], v[128:143]\n\t"
      "ds_read_b64_tr_b16 v[84:85], v14 offset:36864\n\t"
      "ds_read_b64_tr_b16 v[86:87], v14 offset:38912\n\t"
      "s_waitcnt lgkmcnt(12)\n\t"
      "v_mfma_f32_32x32x16_bf16 v[128:143], v[124:127], a[204:207], v[128:143]\n\t"
      "ds_read_b64_tr_b16 v[88:89], v14 offset:40960\n\t"
      "ds_read_b64_tr_b16 v[90:91], v14 offset:43008\n\t"
      "s_waitcnt lgkmcnt(12)\n\t"
      "v_mfma_f32_32x32x16_bf16 v[128:143], v[64:67], a[208:211], v[128:143]\n\t"
      "ds_read_b64_tr_b16 v[92:93], v14 offset:45056\n\t"
      "ds_read_b64_tr_b16 v[94:95], v14 offset:47104\n\t"
      "s_waitcnt lgkmcnt(12)\n\t"
      "v_mfma_f32_32x32x16_bf16 v[128:143], v[68:71], a[212:215], v[128:143]\n\t"
      "ds_read_b64_tr_b16 v[96:97], v14 offset:0\n\t"
      "ds_read_b64_tr_b16 v[98:99], v14 offset:2048\n\t"
      "s_waitcnt lgkmcnt(12)\n\t"
      "v_mfma_f32_32x32x16_bf16 v[128:143], v[72:75], a[216:219], v[128:143]\n\t"
      "s_waitcnt vmcnt(0)\n\t"
      "s_add_u32 s78, s47, 655360\n\t"
      "s_mov_b64 exec, 1\n\t"
      "buffer_store_dwordx2 v[62:63], off, s[24:27], s78 sc1\n\t"
      "s_mov_b64 exec, -1\n\t"
      "ds_read_b64_tr_b16 v[100:101], v14 offset:4096\n\t"
      "ds_read_b64_tr_b16 v[102:103], v14 offset:6144\n\t"
      "s_waitcnt lgkmcnt(12)\n\t"
      "v_mfma_f32_32x32x16_bf16 v[128:143], v[76:79], a[220:223], v[128:143]\n\t"
      "ds_read_b64_tr_b16 v[104:105], v14 offset:8192\n\t"
      "ds_read_b64_tr_b16 v[106:107], v14 offset:10240\n\t"
      "s_waitcnt lgkmcnt(12)\n\t"
      "v_mfma_f32_32x32x16_bf16 v[128:143], v[80:83], a[224:227], v[128:143]\n\t"
      "ds_read_b64_tr_b16 v[108:109], v14 offset:12288\n\t"
      "ds_read_b64_tr_b16 v[110:111], v14 offset:14336\n\t"
      "s_waitcnt lgkmcnt(12)\n\t"
      "v_mfma_f32_32x32x16_bf16 v[128:143], v[84:87], a[228:231], v[128:143]\n\t"
      "ds_read_b64_tr_b16 v[112:113], v14 offset:16384\n\t"
      "ds_read_b64_tr_b16 v[114:115], v14 offset:18432\n\t"
      "s_waitcnt lgkmcnt(12)\n\t"
      "v_mfma_f32_32x32x16_bf16 v[128:143], v[88:91], a[232:235], v[128:143]\n\t"
      "ds_read_b64_tr_b16 v[116:117], v14 offset:20480\n\t"
      "ds_read_b64_tr_b16 v[118:119], v14 offset:22528\n\t"
      "s_waitcnt lgkmcnt(12)\n\t"
      "v_mfma_f32_32x32x16_bf16 v[128:143], v[92:95], a[236:239], v[128:143]\n\t"
      "ds_read_b64_tr_b16 v[120:121], v14 offset:24576\n\t"
      "ds_read_b64_tr_b16 v[122:123], v14 offset:26624\n\t"
      "s_nop 9\n\t"
      "v_mul_f32 v128, v128, v60\n\t"
      "v_mul_f32 v129, v129, v60\n\t"
      "v_mul_f32 v130, v130, v60\n\t"
      "v_mul_f32 v131, v131, v60\n\t"
      "v_mul_f32 v132, v132, v60\n\t"
      "v_mul_f32 v133, v133, v60\n\t"
      "v_mul_f32 v134, v134, v60\n\t"
      "v_mul_f32 v135, v135, v60\n\t"
      "v_mul_f32 v136, v136, v60\n\t"
      "v_mul_f32 v137, v137, v60\n\t"
      "v_mul_f32 v138, v138, v60\n\t"
      "v_mul_f32 v139, v139, v60\n\t"
      "v_mul_f32 v140, v140, v60\n\t"
      "v_mul_f32 v141, v141, v60\n\t"
      "v_mul_f32 v142, v142, v60\n\t"
      "v_mul_f32 v143, v143, v60\n\t"
      "s_mov_b32 s78, 655360\n\t"
      "s_mov_b32 exec_lo, 7\n\t"
      "s_mov_b32 exec_hi, 0\n\t"
      "buffer_load_dwordx2 v[202:203], v34, s[24:27], s78 offen sc1\n\t"
      "s_mov_b64 exec, -1\n\t"
      "s_waitcnt lgkmcnt(12)\n\t"
      "v_mfma_f32_32x32x16_bf16 v[128:143], v[96:99], a[240:243], v[128:143]\n\t"
      "ds_read_b64_tr_b16 v[124:125], v14 offset:28672\n\t"
      "ds_read_b64_tr_b16 v[126:127], v14 offset:30720\n\t"
      "s_waitcnt lgkmcnt(12)\n\t"
      "v_mfma_f32_32x32x16_bf16 v[128:143], v[100:103], a[244:247], v[128:143]\n\t"
      "s_waitcnt lgkmcnt(10)\n\t"
      "v_mfma_f32_32x32x16_bf16 v[128:143], v[104:107], a[248:251], v[128:143]\n\t"
      "s_waitcnt lgkmcnt(8)\n\t"
      "v_mfma_f32_32x32x16_bf16 v[128:143], v[108:111], a[252:255], v[128:143]\n\t"
      "s_waitcnt lgkmcnt(6)\n\t"
      "v_mfma_f32_32x32x16_bf16 v[128:143], v[112:115], a[0:3], v[128:143]\n\t"
      "s_waitcnt lgkmcnt(4)\n\t"
      "v_mfma_f32_32x32x16_bf16 v[128:143], v[116:119], a[4:7], v[128:143]\n\t"
      "s_waitcnt lgkmcnt(2)\n\t"
      "v_mfma_f32_32x32x16_bf16 v[128:143], v[120:123], a[8:11], v[128:143]\n\t"
      "s_waitcnt lgkmcnt(0)\n\t"
      "v_mfma_f32_32x32x16_bf16 v[128:143], v[124:127], a[12:15], v[128:143]\n\t"
      "s_waitcnt vmcnt(0)\n\t"
      "v_cmp_eq_u64 vcc, s[48:49], v[202:203]\n\t"
      "s_nop 0\n\t"
      "s_and_b32 s78, vcc_lo, 7\n\t"
      "s_cmp_eq_u32 s78, 7\n\t"
      "s_cbranch_scc1 .Lskip_26_%=\n\t"
      "s_mov_b32 s79, 32768\n\t"
      ".Lspin_27_%=:\n\t"
      "s_sleep 1\n\t"
      "s_mov_b32 s78, 655360\n\t"
      "s_mov_b32 exec_lo, 7\n\t"
      "s_mov_b32 exec_hi, 0\n\t"
      "buffer_load_dwordx2 v[202:203], v34, s[24:27], s78 offen sc1\n\t"
      "s_mov_b64 exec, -1\n\t"
      "s_waitcnt vmcnt(0)\n\t"
      "v_cmp_eq_u64 vcc, s[48:49], v[202:203]\n\t"
      "s_nop 0\n\t"
      "s_and_b32 s78, vcc_lo, 7\n\t"
      "s_cmp_eq_u32 s78, 7\n\t"
      "s_cbranch_scc1 .Lskip_26_%=\n\t"
      "s_sub_u32 s79, s79, 1\n\t"
      "s_cmp_lg_u32 s79, 0\n\t"
      "s_cbranch_scc1 .Lspin_27_%=\n\t"
      ".Lskip_26_%=:\n\t"
      "s_add_u32 s63, s81, 0\n\t"
      "buffer_load_dwordx4 v[232:235], v1, s[24:27], s63 offen sc1\n\t"
      "buffer_load_dwordx4 v[236:239], v1, s[24:27], s63 offen offset:1024 sc1\n\t"
      "s_add_u32 s63, s81, 8192\n\t"
      "buffer_load_dwordx4 v[240:243], v1, s[24:27], s63 offen sc1\n\t"
      "buffer_load_dwordx4 v[244:247], v1, s[24:27], s63 offen offset:1024 sc1\n\t"
      "s_add_u32 s63, s81, 16384\n\t"
      "buffer_load_dwordx4 v[248:251], v1, s[24:27], s63 offen sc1\n\t"
      "buffer_load_dwordx4 v[252:255], v1, s[24:27], s63 offen offset:1024 sc1\n\t"
      "s_waitcnt vmcnt(5)\n\t"
      "v_mfma_f32_32x32x16_bf16 v[128:143], v[40:43], v[232:235], v[128:143]\n\t"
      "s_waitcnt vmcnt(4)\n\t"
      "v_mfma_f32_32x32x16_bf16 v[128:143], v[44:47], v[236:239], v[128:143]\n\t"
      "s_waitcnt vmcnt(3)\n\t"
      "v_mfma_f32_32x32x16_bf16 v[128:143], v[40:43], v[240:243], v[128:143]\n\t"
      "s_waitcnt vmcnt(2)\n\t"
      "v_mfma_f32_32x32x16_bf16 v[128:143], v[44:47], v[244:247], v[128:143]\n\t"
      "s_waitcnt vmcnt(1)\n\t"
      "v_mfma_f32_32x32x16_bf16 v[128:143], v[40:43], v[248:251], v[128:143]\n\t"
      "s_waitcnt vmcnt(0)\n\t"
      "v_mfma_f32_32x32x16_bf16 v[128:143], v[44:47], v[252:255], v[128:143]\n\t"
      "s_nop 11\n\t"
      "v_cvt_pk_bf16_f32 v48, v128, v129\n\t"
      "v_cvt_pk_bf16_f32 v49, v130, v131\n\t"
      "v_cvt_pk_bf16_f32 v50, v132, v133\n\t"
      "v_cvt_pk_bf16_f32 v51, v134, v135\n\t"
      "v_cvt_pk_bf16_f32 v52, v136, v137\n\t"
      "v_cvt_pk_bf16_f32 v53, v138, v139\n\t"
      "v_cvt_pk_bf16_f32 v54, v140, v141\n\t"
      "v_cvt_pk_bf16_f32 v55, v142, v143\n\t"
      "v_permlane32_swap_b32 v48, v50\n\t"
      "v_permlane32_swap_b32 v49, v51\n\t"
      "buffer_store_dwordx4 v[48:51], v35, s[28:31], s80 offen\n\t"
      "v_permlane32_swap_b32 v52, v54\n\t"
      "v_permlane32_swap_b32 v53, v55\n\t"
      "buffer_store_dwordx4 v[52:55], v35, s[28:31], s80 offen offset:32\n\t"
      "s_endpgm\n\t"
      ".Lk_B_%=:\n\t"
      "s_and_b32 s68, s42, 1\n\t"
      "s_mul_i32 s69, s68, 3\n\t"
      "s_add_u32 s82, s74, s69\n\t"
      "s_lshl_b32 s54, s82, 7\n\t"
      "s_add_u32 s55, s54, 128\n\t"
      "s_add_u32 s69, s74, 2\n\t"
      "s_lshl_b32 s88, s69, 7\n\t"
      "s_mov_b32 s3, s88\n\t"
      "s_add_u32 s60, s46, 0\n\t"
      "s_and_b32 s60, s60, 7\n\t"
      "s_lshl_b32 s61, s60, 7\n\t"
      "s_add_u32 m0, s58, 0\n\t"
      "s_add_u32 s61, s61, s52\n\t"
      "s_waitcnt lgkmcnt(0)\n\t"
      "buffer_load_dwordx4 v2, s[4:7], s61 offen lds\n\t"
      "buffer_load_dwordx4 v3, s[4:7], s61 offen offset:1024 lds\n\t"
      "buffer_load_dwordx4 v4, s[4:7], s61 offen offset:2048 lds\n\t"
      "buffer_load_dwordx4 v5, s[4:7], s61 offen offset:3072 lds\n\t"
      "s_add_u32 s60, s46, 0\n\t"
      "s_and_b32 s60, s60, 7\n\t"
      "s_mul_i32 s61, s60, 229376\n\t"
      "s_add_u32 m0, s57, 0\n\t"
      "s_add_u32 s61, s61, s54\n\t"
      "buffer_load_dwordx4 v8, s[12:15], s61 offen lds\n\t"
      "buffer_load_dwordx4 v9, s[12:15], s61 offen offset:1024 lds\n\t"
      "s_add_u32 s60, s46, 0\n\t"
      "s_and_b32 s60, s60, 7\n\t"
      "s_mul_i32 s61, s60, 196608\n\t"
      "s_add_u32 m0, s57, 8192\n\t"
      "s_add_u32 s61, s61, s53\n\t"
      "buffer_load_dwordx4 v6, s[8:11], s61 offen lds\n\t"
      "buffer_load_dwordx4 v7, s[8:11], s61 offen offset:1024 lds\n\t"
      "s_add_u32 s64, s42, 8\n\t"
      "s_lshl_b32 s64, s64, 13\n\t"
      "s_lshl_b32 s65, s41, 17\n\t"
      "s_add_u32 s64, s64, s65\n\t"
      "s_lshl_b32 s65, s40, 11\n\t"
      "s_add_u32 s64, s64, s65\n\t"
      "s_add_u32 s64, s64, 0\n\t"
      "v_and_b32 v48, 1, v0\n\t"
      "v_lshlrev_b32 v48, 3, v48\n\t"
      "v_lshrrev_b32 v49, 1, v0\n\t"
      "v_add_u32 v48, v48, v49\n\t"
      "v_lshlrev_b32 v48, 7, v48\n\t"
      "s_mov_b64 exec, 255\n\t"
      "buffer_load_dword v39, v48, s[4:7], s64 offen\n\t"
      "s_mov_b64 exec, 0xffff\n\t"
      "s_lshl_b32 s64, s41, 4\n\t"
      "s_lshr_b32 s65, s40, 1\n\t"
      "s_lshl_b32 s65, s65, 8\n\t"
      "s_add_u32 s64, s64, s65\n\t"
      "v_add_u32 v48, s64, v0\n\t"
      "s_and_b32 s65, s40, 1\n\t"
      "s_cmp_lg_u32 s65, 0\n\t"
      "s_cbranch_scc1 .Lskip_28_%=\n\t"
      "v_mul_u32_u24 v49, 3584, v48\n\t"
      "buffer_load_dword v39, v49, s[12:15], s54 offen\n\t"
      "s_nop 0\n\t"
      ".Lskip_28_%=:\n\t"
      "s_and_b32 s65, s40, 1\n\t"
      "s_cmp_lg_u32 s65, 1\n\t"
      "s_cbranch_scc1 .Lskip_29_%=\n\t"
      "v_mul_u32_u24 v49, 3072, v48\n\t"
      "buffer_load_dword v39, v49, s[8:11], s53 offen\n\t"
      "s_nop 0\n\t"
      ".Lskip_29_%=:\n\t"
      "s_mov_b64 exec, -1\n\t"
      "s_lshl_b32 s67, s42, 11\n\t"
      "s_lshl_b32 s69, s40, 10\n\t"
      "s_add_u32 s67, s67, s69\n\t"
      "s_add_u32 s36, s36, s67\n\t"
      "s_addc_u32 s37, s37, 0\n\t"
      "s_cmp_lt_u32 s40, 2\n\t"
      "s_cselect_b32 s32, s36, s38\n\t"
      "s_cselect_b32 s33, s37, s39\n\t"
      "s_add_u32 m0, s69, 131072\n\t"
      "s_mov_b32 s67, 0\n\t"
      "buffer_load_dwordx4 v1, s[32:35], s67 offen lds\n\t"
      "buffer_load_dwordx2 v[58:59], off, s[24:27], 0 sc1\n\t"
      "s_and_b32 s45, s40, 1\n\t"
      "s_lshl_b32 s59, s40, 13\n\t"
      "v_and_b32 v48, 31, v0\n\t"
      "v_lshrrev_b32 v49, 5, v0\n\t"
      "v_bfe_u32 v50, v0, 1, 3\n\t"
      "v_xor_b32 v50, v50, v49\n\t"
      "v_lshlrev_b32 v50, 4, v50\n\t"
      "v_lshlrev_b32 v51, 7, v48\n\t"
      "v_add_u32 v51, s58, v51\n\t"
      "v_xor_b32 v52, 0, v50\n\t"
      "v_add_u32 v240, v51, v52\n\t"
      "v_xor_b32 v244, 4096, v240\n\t"
      "v_add_u32 v248, 65536, v240\n\t"
      "v_add_u32 v252, 65536, v244\n\t"
      "v_xor_b32 v52, 32, v50\n\t"
      "v_add_u32 v241, v51, v52\n\t"
      "v_xor_b32 v245, 4096, v241\n\t"
      "v_add_u32 v249, 65536, v241\n\t"
      "v_add_u32 v253, 65536, v245\n\t"
      "v_xor_b32 v52, 64, v50\n\t"
      "v_add_u32 v242, v51, v52\n\t"
      "v_xor_b32 v246, 4096, v242\n\t"
      "v_add_u32 v250, 65536, v242\n\t"
      "v_add_u32 v254, 65536, v246\n\t"
      "v_xor_b32 v52, 96, v50\n\t"
      "v_add_u32 v243, v51, v52\n\t"
      "v_xor_b32 v247, 4096, v243\n\t"
      "v_add_u32 v251, 65536, v243\n\t"
      "v_add_u32 v255, 65536, v247\n\t"
      "v_lshlrev_b32 v35, 10, v48\n\t"
      "v_lshl_add_u32 v35, v49, 4, v35\n\t"
      "v_lshlrev_b32 v200, 5, v48\n\t"
      "v_lshl_add_u32 v200, v49, 4, v200\n\t"
      "v_bfe_u32 v50, v0, 4, 1\n\t"
      "v_bfe_u32 v51, v0, 2, 2\n\t"
      "v_bfe_u32 v52, v0, 1, 1\n\t"
      "v_lshl_add_u32 v52, v50, 1, v52\n\t"
      "v_lshrrev_b32 v53, 1, v51\n\t"
      "v_lshlrev_b32 v53, 2, v53\n\t"
      "v_xor_b32 v53, v52, v53\n\t"
      "v_and_b32 v54, 1, v0\n\t"
      "v_lshlrev_b32 v54, 3, v54\n\t"
      "v_lshl_add_u32 v53, v53, 4, v54\n\t"
      "v_lshl_add_u32 v53, v51, 7, v53\n\t"
      "v_lshl_add_u32 v53, v49, 10, v53\n\t"
      "s_lshl_b32 s69, s45, 6\n\t"
      "v_xor_b32 v26, s69, v53\n\t"
      "v_add_u32 v27, 65536, v26\n\t"
      "v_lshlrev_b32 v55, 2, v51\n\t"
      "v_xor_b32 v55, v55, v52\n\t"
      "v_lshl_add_u32 v55, v55, 4, v54\n\t"
      "v_lshl_add_u32 v55, v51, 8, v55\n\t"
      "v_lshl_add_u32 v55, v49, 10, v55\n\t"
      "s_add_u32 s69, s43, 0\n\t"
      "s_and_b32 s69, s69, 3\n\t"
      "s_lshl_b32 s69, s69, 6\n\t"
      "v_xor_b32 v14, s69, v55\n\t"
      "v_add_u32 v18, 65536, v14\n\t"
      "v_add_u32 v22, 131072, v14\n\t"
      "s_add_u32 s69, s43, 1\n\t"
      "s_and_b32 s69, s69, 3\n\t"
      "s_lshl_b32 s69, s69, 6\n\t"
      "v_xor_b32 v15, s69, v55\n\t"
      "v_add_u32 v19, 65536, v15\n\t"
      "v_add_u32 v23, 131072, v15\n\t"
      "s_add_u32 s69, s43, 2\n\t"
      "s_and_b32 s69, s69, 3\n\t"
      "s_lshl_b32 s69, s69, 6\n\t"
      "v_xor_b32 v16, s69, v55\n\t"
      "v_add_u32 v20, 65536, v16\n\t"
      "v_add_u32 v24, 131072, v16\n\t"
      "s_add_u32 s69, s43, 3\n\t"
      "s_and_b32 s69, s69, 3\n\t"
      "s_lshl_b32 s69, s69, 6\n\t"
      "v_xor_b32 v17, s69, v55\n\t"
      "v_add_u32 v21, 65536, v17\n\t"
      "v_add_u32 v25, 131072, v17\n\t"
      "v_lshrrev_b32 v55, 4, v0\n\t"
      "v_lshlrev_b32 v29, 2, v55\n\t"
      "v_xor_b32 v29, v29, v0\n\t"
      "v_and_b32 v29, 15, v29\n\t"
      "v_lshlrev_b32 v29, 4, v29\n\t"
      "v_lshl_add_u32 v29, v55, 10, v29\n\t"
      "v_and_b32 v54, 3, v0\n\t"
      "v_lshlrev_b32 v54, 3, v54\n\t"
      "v_lshl_add_u32 v54, v50, 5, v54\n\t"
      "v_lshl_add_u32 v54, v51, 6, v54\n\t"
      "v_lshl_add_u32 v53, v49, 9, v54\n\t"
      "v_lshl_add_u32 v57, v49, 2, v51\n\t"
      "v_lshlrev_b32 v57, 5, v57\n\t"
      "v_lshl_add_u32 v57, v52, 3, v57\n\t"
      "s_lshl_b32 s69, s45, 10\n\t"
      "s_add_u32 s69, s69, 131072\n\t"
      "v_add_u32 v57, s69, v57\n\t"
      "v_bfe_u32 v56, v0, 2, 1\n\t"
      "v_mul_u32_u24 v56, 0x8000, v56\n\t"
      "v_lshl_or_b32 v56, v56, 16, v56\n\t"
      "v_lshrrev_b32 v36, 1, v0\n\t"
      "v_and_b32 v38, 1, v36\n\t"
      "v_add_u32 v38, s75, v38\n\t"
      "v_cmp_gt_u32 vcc, 2, v36\n\t"
      "s_nop 1\n\t"
      "v_cndmask_b32_e64 v37, 0, 8, vcc\n\t"
      "v_add_u32 v38, v38, v37\n\t"
      "v_add_u32 v37, 4, v36\n\t"
      "v_cmp_gt_u32 vcc, 4, v36\n\t"
      "s_nop 1\n\t"
      "v_cndmask_b32 v38, v37, v38, vcc\n\t"
      "s_and_b32 s69, s40, 2\n\t"
      "v_and_b32 v36, 1, v0\n\t"
      "v_add_u32 v36, s69, v36\n\t"
      "v_lshl_add_u32 v38, v38, 2, v36\n\t"
      "s_lshl_b32 s68, s41, 13\n\t"
      "s_add_u32 s68, s68, 3145728\n\t"
      "v_lshlrev_b32 v38, 7, v38\n\t"
      "v_add_u32 v38, s68, v38\n\t"
      "s_lshr_b32 s69, s44, 1\n\t"
      "s_lshl_b32 s69, s69, 3\n\t"
      "s_and_b32 s67, s44, 1\n\t"
      "s_add_u32 s69, s69, s67\n\t"
      "s_lshl_b32 s69, s69, 2\n\t"
      "s_add_u32 s69, s69, s40\n\t"
      "s_lshl_b32 s69, s69, 7\n\t"
      "s_add_u32 s69, s69, s68\n\t"
      "s_lshl_b32 s47, s43, 10\n\t"
      "s_add_u32 s47, s47, s69\n\t"
      "s_add_u32 s67, s43, 3\n\t"
      "v_sub_u32 v34, s67, v0\n\t"
      "v_and_b32 v34, 3, v34\n\t"
      "v_lshlrev_b32 v34, 10, v34\n\t"
      "v_add_u32 v34, s69, v34\n\t"
      "s_lshl_b32 s67, s41, 2\n\t"
      "s_add_u32 s66, s67, s40\n\t"
      "s_add_u32 s67, s67, s44\n\t"
      "s_mul_i32 s67, s67, 98304\n\t"
      "s_lshl_b32 s68, s40, 11\n\t"
      "s_add_u32 s67, s67, s68\n\t"
      "s_add_u32 s67, s67, 16777216\n\t"
      "s_mul_i32 s81, s43, 24576\n\t"
      "s_add_u32 s81, s81, s67\n\t"
      "s_add_u32 s69, s43, 1\n\t"
      "s_and_b32 s69, s69, 3\n\t"
      "s_mul_i32 s69, s69, 24576\n\t"
      "s_add_u32 s69, s69, s67\n\t"
      "s_add_u32 s32, s69, 0\n\t"
      "s_add_u32 s69, s43, 2\n\t"
      "s_and_b32 s69, s69, 3\n\t"
      "s_mul_i32 s69, s69, 24576\n\t"
      "s_add_u32 s69, s69, s67\n\t"
      "s_add_u32 s33, s69, 8192\n\t"
      "s_add_u32 s69, s43, 3\n\t"
      "s_and_b32 s69, s69, 3\n\t"
      "s_mul_i32 s69, s69, 24576\n\t"
      "s_add_u32 s69, s69, s67\n\t"
      "s_add_u32 s34, s69, 16384\n\t"
      "s_lshl_b32 s69, s44, 8\n\t"
      "s_add_u32 s80, s52, s69\n\t"
      "s_lshl_b32 s68, s40, 14\n\t"
      "s_add_u32 s73, s69, s68\n\t"
      "s_lshl_b32 s69, s43, 6\n\t"
      "s_add_u32 s80, s80, s69\n\t"
      "s_lshl_b32 s35, s66, 10\n\t"
      "s_add_u32 s35, s35, 196608\n\t"
      "s_lshl_b32 s68, s45, 11\n\t"
      "s_add_u32 s68, s68, 4194304\n\t"
      "s_mul_i32 s70, s66, 147456\n\t"
      "s_add_u32 s72, s70, 4194304\n\t"
      "s_add_u32 s70, s70, s68\n\t"
      "s_xor_b32 s66, s66, 1\n\t"
      "s_mul_i32 s71, s66, 147456\n\t"
      "s_add_u32 s71, s71, s68\n\t"
      "s_mov_b32 s39, 0x3fb8aa3b\n\t"
      "s_mov_b32 s38, 0xbfb8aa3b\n\t"
      "s_mov_b32 s50, 0x1ffff\n\t"
      "s_mov_b32 s51, 0x1ffff\n\t"
      "s_and_b32 s68, s42, 1\n\t"
      "s_add_u32 s83, s74, 2\n\t"
      "s_lshl_b32 s56, s83, 7\n\t"
      "s_lshl_b32 s69, s68, 6\n\t"
      "s_add_u32 s56, s56, s69\n\t"
      "s_mul_i32 s69, s40, 57344\n\t"
      "s_add_u32 s56, s56, s69\n\t"
      "s_lshl_b32 s84, s40, 10\n\t"
      "s_lshl_b32 s82, s82, 12\n\t"
      "s_lshl_b32 s83, s83, 12\n\t"
      "s_sub_u32 s68, s68, s45\n\t"
      "s_lshl_b32 s68, s68, 11\n\t"
      "s_add_u32 s83, s83, s68\n\t"
      "s_add_u32 s83, s83, s70\n\t"
      "v_lshrrev_b32 v48, 2, v0\n\t"
      "v_mul_u32_u24 v48, 3584, v48\n\t"
      "v_and_b32 v50, 3, v0\n\t"
      "v_lshl_add_u32 v10, v50, 4, v48\n\t"
      "v_add_u32 v12, 65536, v53\n\t"
      "s_add_u32 s69, s46, s42\n\t"
      "s_lshl_b32 s69, s69, 7\n\t"
      "s_add_u32 s69, s69, 133120\n\t"
      "v_lshlrev_b32 v11, 4, v49\n\t"
      "v_add_u32 v11, s69, v11\n\t"
      "s_lshl_b32 s69, s42, 2\n\t"
      "s_add_u32 s85, s35, s69\n\t"
      "v_and_b32 v48, 31, v0\n\t"
      "v_min_u32 v48, 16, v48\n\t"
      "v_lshlrev_b32 v48, 4, v48\n\t"
      "v_lshl_add_u32 v13, v49, 11, v48\n\t"
      "s_lshl_b32 s36, s42, 14\n\t"
      "s_lshl_b32 s69, s45, 13\n\t"
      "s_add_u32 s36, s36, s69\n\t"
      "s_add_u32 s36, s36, 786432\n\t"
      "s_add_u32 s37, s36, 4096\n\t"
      "s_lshl_b32 s88, s42, 12\n\t"
      "s_add_u32 s88, s88, 114688\n\t"
      "s_sleep 4\n\t"
      "s_add_u32 s60, s46, 1\n\t"
      "s_and_b32 s60, s60, 7\n\t"
      "s_lshl_b32 s61, s60, 7\n\t"
      "s_add_u32 m0, s58, 32768\n\t"
      "s_add_u32 s61, s61, s52\n\t"
      "buffer_load_dwordx4 v2, s[4:7], s61 offen lds\n\t"
      "buffer_load_dwordx4 v3, s[4:7], s61 offen offset:1024 lds\n\t"
      "buffer_load_dwordx4 v4, s[4:7], s61 offen offset:2048 lds\n\t"
      "buffer_load_dwordx4 v5, s[4:7], s61 offen offset:3072 lds\n\t"
      "s_add_u32 s60, s46, 1\n\t"
      "s_and_b32 s60, s60, 7\n\t"
      "s_mul_i32 s61, s60, 229376\n\t"
      "s_add_u32 m0, s57, 32768\n\t"
      "s_add_u32 s61, s61, s54\n\t"
      "buffer_load_dwordx4 v8, s[12:15], s61 offen lds\n\t"
      "buffer_load_dwordx4 v9, s[12:15], s61 offen offset:1024 lds\n\t"
      "s_add_u32 s60, s46, 1\n\t"
      "s_and_b32 s60, s60, 7\n\t"
      "s_mul_i32 s61, s60, 196608\n\t"
      "s_add_u32 m0, s57, 40960\n\t"
      "s_add_u32 s61, s61, s53\n\t"
      "buffer_load_dwordx4 v6, s[8:11], s61 offen lds\n\t"
      "buffer_load_dwordx4 v7, s[8:11], s61 offen offset:1024 lds\n\t"
      "s_waitcnt vmcnt(11)\n\t"
      "s_barrier\n\t"
      "ds_read_b128 a[0:3], v240 offset:0\n\t"
      "ds_read_b128 a[128:131], v244 offset:0\n\t"
      "ds_read_b64_tr_b16 v[64:65], v26 offset:0\n\t"
      "ds_read_b64_tr_b16 v[66:67], v26 offset:512\n\t"
      "ds_read_b64_tr_b16 v[68:69], v26 offset:8192\n\t"
      "ds_read_b64_tr_b16 v[70:71], v26 offset:8704\n\t"
      "ds_read_b128 a[4:7], v241 offset:0\n\t"
      "ds_read_b128 a[132:135], v245 offset:0\n\t"
      "ds_read_b64_tr_b16 v[72:73], v26 offset:2048\n\t"
      "ds_read_b64_tr_b16 v[74:75], v26 offset:2560\n\t"
      "ds_read_b64_tr_b16 v[76:77], v26 offset:10240\n\t"
      "ds_read_b64_tr_b16 v[78:79], v26 offset:10752\n\t"
      "ds_read_b128 a[8:11], v242 offset:0\n\t"
      "ds_read_b128 a[136:139], v246 offset:0\n\t"
      "ds_read_b64_tr_b16 v[80:81], v26 offset:4096\n\t"
      "ds_read_b64_tr_b16 v[82:83], v26 offset:4608\n\t"
      "s_waitcnt lgkmcnt(12)\n\t"
      "v_mfma_f32_32x32x16_bf16 v[160:175], v[64:67], a[0:3], 0\n\t"
      "s_add_u32 s60, s46, 2\n\t"
      "s_and_b32 s60, s60, 7\n\t"
      "s_lshl_b32 s61, s60, 7\n\t"
      "s_add_u32 m0, s58, 65536\n\t"
      "s_add_u32 s61, s61, s52\n\t"
      "buffer_load_dwordx4 v2, s[4:7], s61 offen lds\n\t"
      "v_mfma_f32_32x32x16_bf16 v[176:191], v[64:67], a[128:131], 0\n\t"
      "buffer_load_dwordx4 v3, s[4:7], s61 offen offset:1024 lds\n\t"
      "ds_read_b64_tr_b16 v[84:85], v26 offset:12288\n\t"
      "ds_read_b64_tr_b16 v[86:87], v26 offset:12800\n\t"
      "s_waitcnt lgkmcnt(12)\n\t"
      "v_mfma_f32_32x32x16_bf16 v[128:143], v[68:71], a[0:3], 0\n\t"
      "buffer_load_dwordx4 v4, s[4:7], s61 offen offset:2048 lds\n\t"
      "v_mfma_f32_32x32x16_bf16 v[144:159], v[68:71], a[128:131], 0\n\t"
      "buffer_load_dwordx4 v5, s[4:7], s61 offen offset:3072 lds\n\t"
      "ds_read_b128 a[12:15], v243 offset:0\n\t"
      "ds_read_b128 a[140:143], v247 offset:0\n\t"
      "ds_read_b64_tr_b16 v[88:89], v26 offset:6144\n\t"
      "ds_read_b64_tr_b16 v[90:91], v26 offset:6656\n\t"
      "s_waitcnt lgkmcnt(12)\n\t"
      "v_mfma_f32_32x32x16_bf16 v[160:175], v[72:75], a[4:7], v[160:175]\n\t"
      "s_add_u32 s60, s46, 2\n\t"
      "s_and_b32 s60, s60, 7\n\t"
      "s_mul_i32 s61, s60, 229376\n\t"
      "s_add_u32 m0, s57, 65536\n\t"
      "s_add_u32 s61, s61, s54\n\t"
      "buffer_load_dwordx4 v8, s[12:15], s61 offen lds\n\t"
      "v_mfma_f32_32x32x16_bf16 v[176:191], v[72:75], a[132:135], v[176:191]\n\t"
      "buffer_load_dwordx4 v9, s[12:15], s61 offen offset:1024 lds\n\t"
      "ds_read_b64_tr_b16 v[92:93], v26 offset:14336\n\t"
      "ds_read_b64_tr_b16 v[94:95], v26 offset:14848\n\t"
      "s_waitcnt lgkmcnt(12)\n\t"
      "v_mfma_f32_32x32x16_bf16 v[128:143], v[76:79], a[4:7], v[128:143]\n\t"
      "s_add_u32 s60, s46, 2\n\t"
      "s_and_b32 s60, s60, 7\n\t"
      "s_mul_i32 s61, s60, 196608\n\t"
      "s_add_u32 m0, s57, 73728\n\t"
      "s_add_u32 s61, s61, s53\n\t"
      "buffer_load_dwordx4 v6, s[8:11], s61 offen lds\n\t"
      "v_mfma_f32_32x32x16_bf16 v[144:159], v[76:79], a[132:135], v[144:159]\n\t"
      "buffer_load_dwordx4 v7, s[8:11], s61 offen offset:1024 lds\n\t"
      "s_waitcnt lgkmcnt(8)\n\t"
      "v_mfma_f32_32x32x16_bf16 v[160:175], v[80:83], a[8:11], v[160:175]\n\t"
      "s_add_u32 s60, s46, 3\n\t"
      "s_and_b32 s60, s60, 7\n\t"
      "s_lshl_b32 s61, s60, 7\n\t"
      "s_add_u32 m0, s58, 98304\n\t"
      "s_add_u32 s61, s61, s52\n\t"
      "buffer_load_dwordx4 v2, s[4:7], s61 offen lds\n\t"
      "v_mfma_f32_32x32x16_bf16 v[176:191], v[80:83], a[136:139], v[176:191]\n\t"
      "buffer_load_dwordx4 v3, s[4:7], s61 offen offset:1024 lds\n\t"
      "s_waitcnt vmcnt(10)\n\t"
      "s_waitcnt lgkmcnt(0)\n\t"
      "s_barrier\n\t"
      "ds_read_b128 a[16:19], v240 offset:32768\n\t"
      "ds_read_b128 a[144:147], v244 offset:32768\n\t"
      "ds_read_b64_tr_b16 v[96:97], v26 offset:32768\n\t"
      "ds_read_b64_tr_b16 v[98:99], v26 offset:33280\n\t"
      "ds_read_b64_tr_b16 v[100:101], v26 offset:40960\n\t"
      "ds_read_b64_tr_b16 v[102:103], v26 offset:41472\n\t"
      "v_mfma_f32_32x32x16_bf16 v[128:143], v[84:87], a[8:11], v[128:143]\n\t"
      "buffer_load_dwordx4 v4, s[4:7], s61 offen offset:2048 lds\n\t"
      "v_readfirstlane_b32 s48, v58\n\t"
      "v_readfirstlane_b32 s49, v59\n\t"
      "ds_read_b64_tr_b16 v[40:41], v57 offset:0\n\t"
      "ds_read_b64_tr_b16 v[42:43], v57 offset:256\n\t"
      "v_mfma_f32_32x32x16_bf16 v[144:159], v[84:87], a[136:139], v[144:159]\n\t"
      "buffer_load_dwordx4 v5, s[4:7], s61 offen offset:3072 lds\n\t"
      "ds_read_b64_tr_b16 v[44:45], v57 offset:512\n\t"
      "ds_read_b64_tr_b16 v[46:47], v57 offset:768\n\t"
      "s_add_u32 s48, s48, 1\n\t"
      "s_addc_u32 s49, s49, 0\n\t"
      "v_mov_b32 v62, s48\n\t"
      "ds_read_b128 a[20:23], v241 offset:32768\n\t"
      "ds_read_b128 a[148:151], v245 offset:32768\n\t"
      "ds_read_b64_tr_b16 v[104:105], v26 offset:34816\n\t"
      "ds_read_b64_tr_b16 v[106:107], v26 offset:35328\n\t"
      "v_mfma_f32_32x32x16_bf16 v[160:175], v[88:91], a[12:15], v[160:175]\n\t"
      "s_add_u32 s60, s46, 3\n\t"
      "s_and_b32 s60, s60, 7\n\t"
      "s_mul_i32 s61, s60, 229376\n\t"
      "s_add_u32 m0, s57, 98304\n\t"
      "s_add_u32 s61, s61, s54\n\t"
      "buffer_load_dwordx4 v8, s[12:15], s61 offen lds\n\t"
      "v_mov_b32 v63, s49\n\t"
      "s_waitcnt lgkmcnt(7)\n\t"
      "v_xor_b32 v40, v56, v40\n\t"
      "v_xor_b32 v41, v56, v41\n\t"
      "s_waitcnt lgkmcnt(6)\n\t"
      "v_xor_b32 v42, v56, v42\n\t"
      "v_mfma_f32_32x32x16_bf16 v[176:191], v[88:91], a[140:143], v[176:191]\n\t"
      "buffer_load_dwordx4 v9, s[12:15], s61 offen offset:1024 lds\n\t"
      "v_xor_b32 v43, v56, v43\n\t"
      "s_waitcnt lgkmcnt(5)\n\t"
      "v_xor_b32 v44, v56, v44\n\t"
      "v_xor_b32 v45, v56, v45\n\t"
      "s_waitcnt lgkmcnt(4)\n\t"
      "v_xor_b32 v46, v56, v46\n\t"
      "ds_read_b64_tr_b16 v[108:109], v26 offset:43008\n\t"
      "ds_read_b64_tr_b16 v[110:111], v26 offset:43520\n\t"
      "v_mfma_f32_32x32x16_bf16 v[128:143], v[92:95], a[12:15], v[128:143]\n\t"
      "s_add_u32 s60, s46, 3\n\t"
      "s_and_b32 s60, s60, 7\n\t"
      "s_mul_i32 s61, s60, 196608\n\t"
      "s_add_u32 m0, s57, 106496\n\t"
      "s_add_u32 s61, s61, s53\n\t"
      "buffer_load_dwordx4 v6, s[8:11], s61 offen lds\n\t"
      "v_xor_b32 v47, v56, v47\n\t"
      "v_mfma_f32_32x32x16_bf16 v[144:159], v[92:95], a[140:143], v[144:159]\n\t"
      "buffer_load_dwordx4 v7, s[8:11], s61 offen offset:1024 lds\n\t"
      "ds_read_b128 a[24:27], v242 offset:32768\n\t"
      "ds_read_b128 a[152:155], v246 offset:32768\n\t"
      "ds_read_b64_tr_b16 v[112:113], v26 offset:36864\n\t"
      "ds_read_b64_tr_b16 v[114:115], v26 offset:37376\n\t"
      "v_mfma_f32_32x32x16_bf16 v[160:175], v[96:99], a[16:19], v[160:175]\n\t"
      "v_mfma_f32_32x32x16_bf16 v[176:191], v[96:99], a[144:147], v[176:191]\n\t"
      "s_add_u32 s60, s42, 8\n\t"
      "s_lshl_b32 s60, s60, 13\n\t"
      "s_lshl_b32 s61, s41, 17\n\t"
      "s_add_u32 s60, s60, s61\n\t"
      "s_lshl_b32 s61, s40, 11\n\t"
      "s_add_u32 s60, s60, s61\n\t"
      "s_add_u32 s60, s60, 512\n\t"
      "v_and_b32 v36, 1, v0\n\t"
      "v_lshlrev_b32 v36, 3, v36\n\t"
      "v_lshrrev_b32 v37, 1, v0\n\t"
      "v_add_u32 v36, v36, v37\n\t"
      "v_lshlrev_b32 v36, 7, v36\n\t"
      "s_mov_b64 exec, 255\n\t"
      "buffer_load_dword v39, v36, s[4:7], s60 offen\n\t"
      "s_mov_b64 exec, 0xffff\n\t"
      "s_lshl_b32 s60, s41, 4\n\t"
      "s_lshr_b32 s61, s40, 1\n\t"
      "s_lshl_b32 s61, s61, 8\n\t"
      "s_add_u32 s60, s60, s61\n\t"
      "v_add_u32 v36, s60, v0\n\t"
      "s_and_b32 s61, s40, 1\n\t"
      "s_cmp_lg_u32 s61, 0\n\t"
      "s_cbranch_scc1 .Lskip_30_%=\n\t"
      "v_mul_u32_u24 v37, 3584, v36\n\t"
      "buffer_load_dword v39, v37, s[12:15], s55 offen\n\t"
      "s_nop 0\n\t"
      ".Lskip_30_%=:\n\t"
      "s_and_b32 s61, s40, 1\n\t"
      "s_cmp_lg_u32 s61, 1\n\t"
      "s_cbranch_scc1 .Lskip_31_%=\n\t"
      "v_mul_u32_u24 v37, 3584, v36\n\t"
      "buffer_load_dword v39, v37, s[12:15], s3 offen\n\t"
      "s_nop 0\n\t"
      ".Lskip_31_%=:\n\t"
      "s_mov_b64 exec, -1\n\t"
      "ds_read_b64_tr_b16 v[116:117], v26 offset:45056\n\t"
      "ds_read_b64_tr_b16 v[118:119], v26 offset:45568\n\t"
      "v_mfma_f32_32x32x16_bf16 v[128:143], v[100:103], a[16:19], v[128:143]\n\t"
      "v_mfma_f32_32x32x16_bf16 v[144:159], v[100:103], a[144:147], v[144:159]\n\t"
      "s_add_u32 s60, s46, 4\n\t"
      "s_and_b32 s60, s60, 7\n\t"
      "s_lshl_b32 s61, s60, 7\n\t"
      "s_add_u32 m0, s58, 0\n\t"
      "s_add_u32 s61, s61, s52\n\t"
      "buffer_load_dwordx4 v2, s[4:7], s61 offen lds\n\t"
      "ds_read_b128 a[28:31], v243 offset:32768\n\t"
      "ds_read_b128 a[156:159], v247 offset:32768\n\t"
      "ds_read_b64_tr_b16 v[120:121], v26 offset:38912\n\t"
      "ds_read_b64_tr_b16 v[122:123], v26 offset:39424\n\t"
      "s_waitcnt lgkmcnt(12)\n\t"
      "v_mfma_f32_32x32x16_bf16 v[160:175], v[104:107], a[20:23], v[160:175]\n\t"
      "v_mfma_f32_32x32x16_bf16 v[176:191], v[104:107], a[148:151], v[176:191]\n\t"
      "buffer_load_dwordx4 v3, s[4:7], s61 offen offset:1024 lds\n\t"
      "ds_read_b64_tr_b16 v[124:125], v26 offset:47104\n\t"
      "ds_read_b64_tr_b16 v[126:127], v26 offset:47616\n\t"
      "s_waitcnt lgkmcnt(12)\n\t"
      "v_mfma_f32_32x32x16_bf16 v[128:143], v[108:111], a[20:23], v[128:143]\n\t"
      "v_mfma_f32_32x32x16_bf16 v[144:159], v[108:111], a[148:151], v[144:159]\n\t"
      "buffer_load_dwordx4 v4, s[4:7], s61 offen offset:2048 lds\n\t"
      "s_waitcnt lgkmcnt(8)\n\t"
      "v_mfma_f32_32x32x16_bf16 v[160:175], v[112:115], a[24:27], v[160:175]\n\t"
      "v_mfma_f32_32x32x16_bf16 v[176:191], v[112:115], a[152:155], v[176:191]\n\t"
      "buffer_load_dwordx4 v5, s[4:7], s61 offen offset:3072 lds\n\t"
      "s_waitcnt vmcnt(13)\n\t"
      "s_waitcnt lgkmcnt(0)\n\t"
      "s_barrier\n\t"
      "ds_read_b128 a[32:35], v248 offset:0\n\t"
      "ds_read_b128 a[160:163], v252 offset:0\n\t"
      "ds_read_b64_tr_b16 v[64:65], v27 offset:0\n\t"
      "ds_read_b64_tr_b16 v[66:67], v27 offset:512\n\t"
      "ds_read_b64_tr_b16 v[68:69], v27 offset:8192\n\t"
      "ds_read_b64_tr_b16 v[70:71], v27 offset:8704\n\t"
      "v_mfma_f32_32x32x16_bf16 v[128:143], v[116:119], a[24:27], v[128:143]\n\t"
      "v_mfma_f32_32x32x16_bf16 v[144:159], v[116:119], a[152:155], v[144:159]\n\t"
      "s_add_u32 s60, s46, 4\n\t"
      "s_and_b32 s60, s60, 7\n\t"
      "s_mul_i32 s61, s60, 229376\n\t"
      "s_add_u32 m0, s57, 0\n\t"
      "s_add_u32 s61, s61, s54\n\t"
      "buffer_load_dwordx4 v8, s[12:15], s61 offen lds\n\t"
      "ds_read_b128 a[36:39], v249 offset:0\n\t"
      "ds_read_b128 a[164:167], v253 offset:0\n\t"
      "ds_read_b64_tr_b16 v[72:73], v27 offset:2048\n\t"
      "ds_read_b64_tr_b16 v[74:75], v27 offset:2560\n\t"
      "v_mfma_f32_32x32x16_bf16 v[160:175], v[120:123], a[28:31], v[160:175]\n\t"
      "v_mfma_f32_32x32x16_bf16 v[176:191], v[120:123], a[156:159], v[176:191]\n\t"
      "buffer_load_dwordx4 v9, s[12:15], s61 offen offset:1024 lds\n\t"
      "ds_read_b64_tr_b16 v[76:77], v27 offset:10240\n\t"
      "ds_read_b64_tr_b16 v[78:79], v27 offset:10752\n\t"
      "v_mfma_f32_32x32x16_bf16 v[128:143], v[124:127], a[28:31], v[128:143]\n\t"
      "v_mfma_f32_32x32x16_bf16 v[144:159], v[124:127], a[156:159], v[144:159]\n\t"
      "s_add_u32 s60, s46, 4\n\t"
      "s_and_b32 s60, s60, 7\n\t"
      "s_mul_i32 s61, s60, 196608\n\t"
      "s_add_u32 m0, s57, 8192\n\t"
      "s_add_u32 s61, s61, s53\n\t"
      "buffer_load_dwordx4 v6, s[8:11], s61 offen lds\n\t"
      "ds_read_b128 a[40:43], v250 offset:0\n\t"
      "ds_read_b128 a[168:171], v254 offset:0\n\t"
      "ds_read_b64_tr_b16 v[80:81], v27 offset:4096\n\t"
      "ds_read_b64_tr_b16 v[82:83], v27 offset:4608\n\t"
      "s_waitcnt lgkmcnt(12)\n\t"
      "v_mfma_f32_32x32x16_bf16 v[160:175], v[64:67], a[32:35], v[160:175]\n\t"
      "v_mfma_f32_32x32x16_bf16 v[176:191], v[64:67], a[160:163], v[176:191]\n\t"
      "buffer_load_dwordx4 v7, s[8:11], s61 offen offset:1024 lds\n\t"
      "ds_read_b64_tr_b16 v[84:85], v27 offset:12288\n\t"
      "ds_read_b64_tr_b16 v[86:87], v27 offset:12800\n\t"
      "s_waitcnt lgkmcnt(12)\n\t"
      "v_mfma_f32_32x32x16_bf16 v[128:143], v[68:71], a[32:35], v[128:143]\n\t"
      "v_mfma_f32_32x32x16_bf16 v[144:159], v[68:71], a[160:163], v[144:159]\n\t"
      "s_add_u32 s60, s46, 5\n\t"
      "s_and_b32 s60, s60, 7\n\t"
      "s_lshl_b32 s61, s60, 7\n\t"
      "s_add_u32 m0, s58, 32768\n\t"
      "s_add_u32 s61, s61, s52\n\t"
      "buffer_load_dwordx4 v2, s[4:7], s61 offen lds\n\t"
      "ds_read_b128 a[44:47], v251 offset:0\n\t"
      "ds_read_b128 a[172:175], v255 offset:0\n\t"
      "ds_read_b64_tr_b16 v[88:89], v27 offset:6144\n\t"
      "ds_read_b64_tr_b16 v[90:91], v27 offset:6656\n\t"
      "s_waitcnt lgkmcnt(12)\n\t"
      "v_mfma_f32_32x32x16_bf16 v[160:175], v[72:75], a[36:39], v[160:175]\n\t"
      "v_mfma_f32_32x32x16_bf16 v[176:191], v[72:75], a[164:167], v[176:191]\n\t"
      "buffer_load_dwordx4 v3, s[4:7], s61 offen offset:1024 lds\n\t"
      "ds_read_b64_tr_b16 v[92:93], v27 offset:14336\n\t"
      "ds_read_b64_tr_b16 v[94:95], v27 offset:14848\n\t"
      "s_waitcnt lgkmcnt(12)\n\t"
      "v_mfma_f32_32x32x16_bf16 v[128:143], v[76:79], a[36:39], v[128:143]\n\t"
      "v_mfma_f32_32x32x16_bf16 v[144:159], v[76:79], a[164:167], v[144:159]\n\t"
      "buffer_load_dwordx4 v4, s[4:7], s61 offen offset:2048 lds\n\t"
      "s_waitcnt lgkmcnt(8)\n\t"
      "v_mfma_f32_32x32x16_bf16 v[160:175], v[80:83], a[40:43], v[160:175]\n\t"
      "v_mfma_f32_32x32x16_bf16 v[176:191], v[80:83], a[168:171], v[176:191]\n\t"
      "buffer_load_dwordx4 v5, s[4:7], s61 offen offset:3072 lds\n\t"
      "s_waitcnt vmcnt(13)\n\t"
      "s_waitcnt lgkmcnt(0)\n\t"
      "s_barrier\n\t"
      "ds_read_b128 a[48:51], v248 offset:32768\n\t"
      "ds_read_b128 a[176:179], v252 offset:32768\n\t"
      "ds_read_b64_tr_b16 v[96:97], v27 offset:32768\n\t"
      "ds_read_b64_tr_b16 v[98:99], v27 offset:33280\n\t"
      "ds_read_b64_tr_b16 v[100:101], v27 offset:40960\n\t"
      "ds_read_b64_tr_b16 v[102:103], v27 offset:41472\n\t"
      "v_mfma_f32_32x32x16_bf16 v[128:143], v[84:87], a[40:43], v[128:143]\n\t"
      "v_mfma_f32_32x32x16_bf16 v[144:159], v[84:87], a[168:171], v[144:159]\n\t"
      "s_add_u32 s60, s46, 5\n\t"
      "s_and_b32 s60, s60, 7\n\t"
      "s_mul_i32 s61, s60, 229376\n\t"
      "s_add_u32 m0, s57, 32768\n\t"
      "s_add_u32 s61, s61, s54\n\t"
      "buffer_load_dwordx4 v8, s[12:15], s61 offen lds\n\t"
      "ds_read_b128 a[52:55], v249 offset:32768\n\t"
      "ds_read_b128 a[180:183], v253 offset:32768\n\t"
      "ds_read_b64_tr_b16 v[104:105], v27 offset:34816\n\t"
      "ds_read_b64_tr_b16 v[106:107], v27 offset:35328\n\t"
      "v_mfma_f32_32x32x16_bf16 v[160:175], v[88:91], a[44:47], v[160:175]\n\t"
      "v_mfma_f32_32x32x16_bf16 v[176:191], v[88:91], a[172:175], v[176:191]\n\t"
      "buffer_load_dwordx4 v9, s[12:15], s61 offen offset:1024 lds\n\t"
      "ds_read_b64_tr_b16 v[108:109], v27 offset:43008\n\t"
      "ds_read_b64_tr_b16 v[110:111], v27 offset:43520\n\t"
      "v_mfma_f32_32x32x16_bf16 v[128:143], v[92:95], a[44:47], v[128:143]\n\t"
      "v_mfma_f32_32x32x16_bf16 v[144:159], v[92:95], a[172:175], v[144:159]\n\t"
      "s_add_u32 s60, s46, 5\n\t"
      "s_and_b32 s60, s60, 7\n\t"
      "s_mul_i32 s61, s60, 196608\n\t"
      "s_add_u32 m0, s57, 40960\n\t"
      "s_add_u32 s61, s61, s53\n\t"
      "buffer_load_dwordx4 v6, s[8:11], s61 offen lds\n\t"
      "ds_read_b128 a[56:59], v250 offset:32768\n\t"
      "ds_read_b128 a[184:187], v254 offset:32768\n\t"
      "ds_read_b64_tr_b16 v[112:113], v27 offset:36864\n\t"
      "ds_read_b64_tr_b16 v[114:115], v27 offset:37376\n\t"
      "s_waitcnt lgkmcnt(12)\n\t"
      "v_mfma_f32_32x32x16_bf16 v[160:175], v[96:99], a[48:51], v[160:175]\n\t"
      "v_mfma_f32_32x32x16_bf16 v[176:191], v[96:99], a[176:179], v[176:191]\n\t"
      "buffer_load_dwordx4 v7, s[8:11], s61 offen offset:1024 lds\n\t"
      "ds_read_b64_tr_b16 v[116:117], v27 offset:45056\n\t"
      "ds_read_b64_tr_b16 v[118:119], v27 offset:45568\n\t"
      "s_waitcnt lgkmcnt(12)\n\t"
      "v_mfma_f32_32x32x16_bf16 v[128:143], v[100:103], a[48:51], v[128:143]\n\t"
      "v_mfma_f32_32x32x16_bf16 v[144:159], v[100:103], a[176:179], v[144:159]\n\t"
      "s_add_u32 s60, s46, 6\n\t"
      "s_and_b32 s60, s60, 7\n\t"
      "s_lshl_b32 s61, s60, 7\n\t"
      "s_add_u32 m0, s58, 65536\n\t"
      "s_add_u32 s61, s61, s52\n\t"
      "buffer_load_dwordx4 v2, s[4:7], s61 offen lds\n\t"
      "ds_read_b128 a[60:63], v251 offset:32768\n\t"
      "ds_read_b128 a[188:191], v255 offset:32768\n\t"
      "ds_read_b64_tr_b16 v[120:121], v27 offset:38912\n\t"
      "ds_read_b64_tr_b16 v[122:123], v27 offset:39424\n\t"
      "s_waitcnt lgkmcnt(12)\n\t"
      "v_mfma_f32_32x32x16_bf16 v[160:175], v[104:107], a[52:55], v[160:175]\n\t"
      "v_mfma_f32_32x32x16_bf16 v[176:191], v[104:107], a[180:183], v[176:191]\n\t"
      "buffer_load_dwordx4 v3, s[4:7], s61 offen offset:1024 lds\n\t"
      "ds_read_b64_tr_b16 v[124:125], v27 offset:47104\n\t"
      "ds_read_b64_tr_b16 v[126:127], v27 offset:47616\n\t"
      "s_waitcnt lgkmcnt(12)\n\t"
      "v_mfma_f32_32x32x16_bf16 v[128:143], v[108:111], a[52:55], v[128:143]\n\t"
      "v_mfma_f32_32x32x16_bf16 v[144:159], v[108:111], a[180:183], v[144:159]\n\t"
      "buffer_load_dwordx4 v4, s[4:7], s61 offen offset:2048 lds\n\t"
      "s_waitcnt lgkmcnt(8)\n\t"
      "v_mfma_f32_32x32x16_bf16 v[160:175], v[112:115], a[56:59], v[160:175]\n\t"
      "v_mfma_f32_32x32x16_bf16 v[176:191], v[112:115], a[184:187], v[176:191]\n\t"
      "buffer_load_dwordx4 v5, s[4:7], s61 offen offset:3072 lds\n\t"
      "s_waitcnt vmcnt(12)\n\t"
      "s_waitcnt lgkmcnt(0)\n\t"
      "s_barrier\n\t"
      "ds_read_b128 a[64:67], v240 offset:0\n\t"
      "ds_read_b128 a[192:195], v244 offset:0\n\t"
      "ds_read_b64_tr_b16 v[64:65], v26 offset:0\n\t"
      "ds_read_b64_tr_b16 v[66:67], v26 offset:512\n\t"
      "ds_read_b64_tr_b16 v[68:69], v26 offset:8192\n\t"
      "ds_read_b64_tr_b16 v[70:71], v26 offset:8704\n\t"
      "v_mfma_f32_32x32x16_bf16 v[128:143], v[116:119], a[56:59], v[128:143]\n\t"
      "v_mfma_f32_32x32x16_bf16 v[144:159], v[116:119], a[184:187], v[144:159]\n\t"
      "s_add_u32 s60, s46, 6\n\t"
      "s_and_b32 s60, s60, 7\n\t"
      "s_mul_i32 s61, s60, 229376\n\t"
      "s_add_u32 m0, s57, 65536\n\t"
      "s_add_u32 s61, s61, s54\n\t"
      "buffer_load_dwordx4 v8, s[12:15], s61 offen lds\n\t"
      "ds_read_b128 a[68:71], v241 offset:0\n\t"
      "ds_read_b128 a[196:199], v245 offset:0\n\t"
      "ds_read_b64_tr_b16 v[72:73], v26 offset:2048\n\t"
      "ds_read_b64_tr_b16 v[74:75], v26 offset:2560\n\t"
      "v_mfma_f32_32x32x16_bf16 v[160:175], v[120:123], a[60:63], v[160:175]\n\t"
      "v_mfma_f32_32x32x16_bf16 v[176:191], v[120:123], a[188:191], v[176:191]\n\t"
      "buffer_load_dwordx4 v9, s[12:15], s61 offen offset:1024 lds\n\t"
      "ds_read_b64_tr_b16 v[76:77], v26 offset:10240\n\t"
      "ds_read_b64_tr_b16 v[78:79], v26 offset:10752\n\t"
      "v_mfma_f32_32x32x16_bf16 v[128:143], v[124:127], a[60:63], v[128:143]\n\t"
      "v_mfma_f32_32x32x16_bf16 v[144:159], v[124:127], a[188:191], v[144:159]\n\t"
      "s_add_u32 s60, s46, 6\n\t"
      "s_and_b32 s60, s60, 7\n\t"
      "s_mul_i32 s61, s60, 196608\n\t"
      "s_add_u32 m0, s57, 73728\n\t"
      "s_add_u32 s61, s61, s53\n\t"
      "buffer_load_dwordx4 v6, s[8:11], s61 offen lds\n\t"
      "ds_read_b128 a[72:75], v242 offset:0\n\t"
      "ds_read_b128 a[200:203], v246 offset:0\n\t"
      "ds_read_b64_tr_b16 v[80:81], v26 offset:4096\n\t"
      "ds_read_b64_tr_b16 v[82:83], v26 offset:4608\n\t"
      "s_waitcnt lgkmcnt(12)\n\t"
      "v_mfma_f32_32x32x16_bf16 v[160:175], v[64:67], a[64:67], v[160:175]\n\t"
      "v_mfma_f32_32x32x16_bf16 v[176:191], v[64:67], a[192:195], v[176:191]\n\t"
      "buffer_load_dwordx4 v7, s[8:11], s61 offen offset:1024 lds\n\t"
      "ds_read_b64_tr_b16 v[84:85], v26 offset:12288\n\t"
      "ds_read_b64_tr_b16 v[86:87], v26 offset:12800\n\t"
      "s_waitcnt lgkmcnt(12)\n\t"
      "v_mfma_f32_32x32x16_bf16 v[128:143], v[68:71], a[64:67], v[128:143]\n\t"
      "v_mfma_f32_32x32x16_bf16 v[144:159], v[68:71], a[192:195], v[144:159]\n\t"
      "s_add_u32 s60, s46, 7\n\t"
      "s_and_b32 s60, s60, 7\n\t"
      "s_lshl_b32 s61, s60, 7\n\t"
      "s_add_u32 m0, s58, 98304\n\t"
      "s_add_u32 s61, s61, s52\n\t"
      "buffer_load_dwordx4 v2, s[4:7], s61 offen lds\n\t"
      "ds_read_b128 a[76:79], v243 offset:0\n\t"
      "ds_read_b128 a[204:207], v247 offset:0\n\t"
      "ds_read_b64_tr_b16 v[88:89], v26 offset:6144\n\t"
      "ds_read_b64_tr_b16 v[90:91], v26 offset:6656\n\t"
      "s_waitcnt lgkmcnt(12)\n\t"
      "v_mfma_f32_32x32x16_bf16 v[160:175], v[72:75], a[68:71], v[160:175]\n\t"
      "v_mfma_f32_32x32x16_bf16 v[176:191], v[72:75], a[196:199], v[176:191]\n\t"
      "buffer_load_dwordx4 v3, s[4:7], s61 offen offset:1024 lds\n\t"
      "ds_read_b64_tr_b16 v[92:93], v26 offset:14336\n\t"
      "ds_read_b64_tr_b16 v[94:95], v26 offset:14848\n\t"
      "s_waitcnt lgkmcnt(12)\n\t"
      "v_mfma_f32_32x32x16_bf16 v[128:143], v[76:79], a[68:71], v[128:143]\n\t"
      "v_mfma_f32_32x32x16_bf16 v[144:159], v[76:79], a[196:199], v[144:159]\n\t"
      "buffer_load_dwordx4 v4, s[4:7], s61 offen offset:2048 lds\n\t"
      "s_waitcnt lgkmcnt(8)\n\t"
      "v_mfma_f32_32x32x16_bf16 v[160:175], v[80:83], a[72:75], v[160:175]\n\t"
      "v_mfma_f32_32x32x16_bf16 v[176:191], v[80:83], a[200:203], v[176:191]\n\t"
      "buffer_load_dwordx4 v5, s[4:7], s61 offen offset:3072 lds\n\t"
      "s_waitcnt vmcnt(12)\n\t"
      "s_waitcnt lgkmcnt(0)\n\t"
      "s_barrier\n\t"
      "ds_read_b128 a[80:83], v240 offset:32768\n\t"
      "ds_read_b128 a[208:211], v244 offset:32768\n\t"
      "ds_read_b64_tr_b16 v[96:97], v26 offset:32768\n\t"
      "ds_read_b64_tr_b16 v[98:99], v26 offset:33280\n\t"
      "ds_read_b64_tr_b16 v[100:101], v26 offset:40960\n\t"
      "ds_read_b64_tr_b16 v[102:103], v26 offset:41472\n\t"
      "v_mfma_f32_32x32x16_bf16 v[128:143], v[84:87], a[72:75], v[128:143]\n\t"
      "v_mfma_f32_32x32x16_bf16 v[144:159], v[84:87], a[200:203], v[144:159]\n\t"
      "s_add_u32 s60, s46, 7\n\t"
      "s_and_b32 s60, s60, 7\n\t"
      "s_mul_i32 s61, s60, 229376\n\t"
      "s_add_u32 m0, s57, 98304\n\t"
      "s_add_u32 s61, s61, s54\n\t"
      "buffer_load_dwordx4 v8, s[12:15], s61 offen lds\n\t"
      "ds_read_b128 a[84:87], v241 offset:32768\n\t"
      "ds_read_b128 a[212:215], v245 offset:32768\n\t"
      "ds_read_b64_tr_b16 v[104:105], v26 offset:34816\n\t"
      "ds_read_b64_tr_b16 v[106:107], v26 offset:35328\n\t"
      "v_mfma_f32_32x32x16_bf16 v[160:175], v[88:91], a[76:79], v[160:175]\n\t"
      "v_mfma_f32_32x32x16_bf16 v[176:191], v[88:91], a[204:207], v[176:191]\n\t"
      "buffer_load_dwordx4 v9, s[12:15], s61 offen offset:1024 lds\n\t"
      "ds_read_b64_tr_b16 v[108:109], v26 offset:43008\n\t"
      "ds_read_b64_tr_b16 v[110:111], v26 offset:43520\n\t"
      "v_mfma_f32_32x32x16_bf16 v[128:143], v[92:95], a[76:79], v[128:143]\n\t"
      "v_mfma_f32_32x32x16_bf16 v[144:159], v[92:95], a[204:207], v[144:159]\n\t"
      "s_add_u32 s60, s46, 7\n\t"
      "s_and_b32 s60, s60, 7\n\t"
      "s_mul_i32 s61, s60, 196608\n\t"
      "s_add_u32 m0, s57, 106496\n\t"
      "s_add_u32 s61, s61, s53\n\t"
      "buffer_load_dwordx4 v6, s[8:11], s61 offen lds\n\t"
      "ds_read_b128 a[88:91], v242 offset:32768\n\t"
      "ds_read_b128 a[216:219], v246 offset:32768\n\t"
      "ds_read_b64_tr_b16 v[112:113], v26 offset:36864\n\t"
      "ds_read_b64_tr_b16 v[114:115], v26 offset:37376\n\t"
      "s_waitcnt lgkmcnt(12)\n\t"
      "v_mfma_f32_32x32x16_bf16 v[160:175], v[96:99], a[80:83], v[160:175]\n\t"
      "v_mfma_f32_32x32x16_bf16 v[176:191], v[96:99], a[208:211], v[176:191]\n\t"
      "buffer_load_dwordx4 v7, s[8:11], s61 offen offset:1024 lds\n\t"
      "ds_read_b64_tr_b16 v[116:117], v26 offset:45056\n\t"
      "ds_read_b64_tr_b16 v[118:119], v26 offset:45568\n\t"
      "s_waitcnt lgkmcnt(12)\n\t"
      "v_mfma_f32_32x32x16_bf16 v[128:143], v[100:103], a[80:83], v[128:143]\n\t"
      "v_mfma_f32_32x32x16_bf16 v[144:159], v[100:103], a[208:211], v[144:159]\n\t"
      "s_lshl_b32 s60, s2, 2\n\t"
      "s_add_u32 s60, s60, s40\n\t"
      "s_mul_i32 s61, s60, 1792\n\t"
      "s_lshl_b32 s60, s60, 9\n\t"
      "v_lshlrev_b32 v36, 7, v0\n\t"
      "s_mov_b64 exec, 0x3fff\n\t"
      "buffer_load_dword v39, v36, s[16:19], s61 offen\n\t"
      "s_mov_b64 exec, 0xf\n\t"
      "buffer_load_dword v39, v36, s[20:23], s60 offen\n\t"
      "s_mov_b64 exec, -1\n\t"
      "ds_read_b128 a[92:95], v243 offset:32768\n\t"
      "ds_read_b128 a[220:223], v247 offset:32768\n\t"
      "ds_read_b64_tr_b16 v[120:121], v26 offset:38912\n\t"
      "ds_read_b64_tr_b16 v[122:123], v26 offset:39424\n\t"
      "s_waitcnt lgkmcnt(12)\n\t"
      "v_mfma_f32_32x32x16_bf16 v[160:175], v[104:107], a[84:87], v[160:175]\n\t"
      "v_mfma_f32_32x32x16_bf16 v[176:191], v[104:107], a[212:215], v[176:191]\n\t"
      "s_lshl_b32 s60, s41, 4\n\t"
      "s_add_u32 s60, s60, s42\n\t"
      "s_add_u32 s60, s60, 8\n\t"
      "s_lshl_b32 s60, s60, 2\n\t"
      "s_add_u32 s60, s60, s40\n\t"
      "s_lshl_b32 s60, s60, 7\n\t"
      "s_add_u32 s60, s60, 3145728\n\t"
      "v_lshlrev_b32 v36, 17, v0\n\t"
      "v_add_u32 v36, s60, v36\n\t"
      "s_lshl_b32 s60, s41, 2\n\t"
      "s_add_u32 s60, s60, s40\n\t"
      "s_lshl_b32 s60, s60, 10\n\t"
      "s_add_u32 s60, s60, 195584\n\t"
      "v_lshlrev_b32 v37, 7, v0\n\t"
      "v_add_u32 v37, s60, v37\n\t"
      "v_cmp_lt_u32 vcc, 7, v0\n\t"
      "s_nop 1\n\t"
      "v_cndmask_b32 v36, v36, v37, vcc\n\t"
      "s_mov_b64 exec, 0xff3e\n\t"
      "buffer_load_dword v39, v36, s[24:27], 0 offen sc1\n\t"
      "s_mov_b64 exec, -1\n\t"
      "ds_read_b64_tr_b16 v[124:125], v26 offset:47104\n\t"
      "ds_read_b64_tr_b16 v[126:127], v26 offset:47616\n\t"
      "s_waitcnt lgkmcnt(12)\n\t"
      "v_mfma_f32_32x32x16_bf16 v[128:143], v[108:111], a[84:87], v[128:143]\n\t"
      "v_mfma_f32_32x32x16_bf16 v[144:159], v[108:111], a[212:215], v[144:159]\n\t"
      "s_add_u32 s60, s46, 0\n\t"
      "s_and_b32 s60, s60, 7\n\t"
      "s_mul_i32 s61, s60, 229376\n\t"
      "s_add_u32 m0, s57, 0\n\t"
      "s_add_u32 s61, s61, s55\n\t"
      "buffer_load_dwordx4 v8, s[12:15], s61 offen lds\n\t"
      "s_waitcnt lgkmcnt(8)\n\t"
      "v_mfma_f32_32x32x16_bf16 v[160:175], v[112:115], a[88:91], v[160:175]\n\t"
      "v_mfma_f32_32x32x16_bf16 v[176:191], v[112:115], a[216:219], v[176:191]\n\t"
      "buffer_load_dwordx4 v9, s[12:15], s61 offen offset:1024 lds\n\t"
      "s_waitcnt vmcnt(13)\n\t"
      "s_waitcnt lgkmcnt(0)\n\t"
      "s_barrier\n\t"
      "ds_read_b128 a[96:99], v248 offset:0\n\t"
      "ds_read_b128 a[224:227], v252 offset:0\n\t"
      "ds_read_b64_tr_b16 v[64:65], v27 offset:0\n\t"
      "ds_read_b64_tr_b16 v[66:67], v27 offset:512\n\t"
      "ds_read_b64_tr_b16 v[68:69], v27 offset:8192\n\t"
      "ds_read_b64_tr_b16 v[70:71], v27 offset:8704\n\t"
      "v_mfma_f32_32x32x16_bf16 v[128:143], v[116:119], a[88:91], v[128:143]\n\t"
      "v_mfma_f32_32x32x16_bf16 v[144:159], v[116:119], a[216:219], v[144:159]\n\t"
      "s_add_u32 s60, s46, 1\n\t"
      "s_and_b32 s60, s60, 7\n\t"
      "s_mul_i32 s61, s60, 229376\n\t"
      "s_add_u32 m0, s57, 8192\n\t"
      "s_add_u32 s61, s61, s55\n\t"
      "buffer_load_dwordx4 v8, s[12:15], s61 offen lds\n\t"
      "ds_read_b128 a[100:103], v249 offset:0\n\t"
      "ds_read_b128 a[228:231], v253 offset:0\n\t"
      "ds_read_b64_tr_b16 v[72:73], v27 offset:2048\n\t"
      "ds_read_b64_tr_b16 v[74:75], v27 offset:2560\n\t"
      "v_mfma_f32_32x32x16_bf16 v[160:175], v[120:123], a[92:95], v[160:175]\n\t"
      "v_mfma_f32_32x32x16_bf16 v[176:191], v[120:123], a[220:223], v[176:191]\n\t"
      "buffer_load_dwordx4 v9, s[12:15], s61 offen offset:1024 lds\n\t"
      "ds_read_b64_tr_b16 v[76:77], v27 offset:10240\n\t"
      "ds_read_b64_tr_b16 v[78:79], v27 offset:10752\n\t"
      "v_mfma_f32_32x32x16_bf16 v[128:143], v[124:127], a[92:95], v[128:143]\n\t"
      "v_mfma_f32_32x32x16_bf16 v[144:159], v[124:127], a[220:223], v[144:159]\n\t"
      "s_add_u32 s60, s46, 2\n\t"
      "s_and_b32 s60, s60, 7\n\t"
      "s_mul_i32 s61, s60, 229376\n\t"
      "s_add_u32 m0, s57, 16384\n\t"
      "s_add_u32 s61, s61, s55\n\t"
      "buffer_load_dwordx4 v8, s[12:15], s61 offen lds\n\t"
      "ds_read_b128 a[104:107], v250 offset:0\n\t"
      "ds_read_b128 a[232:235], v254 offset:0\n\t"
      "ds_read_b64_tr_b16 v[80:81], v27 offset:4096\n\t"
      "ds_read_b64_tr_b16 v[82:83], v27 offset:4608\n\t"
      "s_waitcnt lgkmcnt(12)\n\t"
      "v_mfma_f32_32x32x16_bf16 v[160:175], v[64:67], a[96:99], v[160:175]\n\t"
      "v_mfma_f32_32x32x16_bf16 v[176:191], v[64:67], a[224:227], v[176:191]\n\t"
      "buffer_load_dwordx4 v9, s[12:15], s61 offen offset:1024 lds\n\t"
      "ds_read_b64_tr_b16 v[84:85], v27 offset:12288\n\t"
      "ds_read_b64_tr_b16 v[86:87], v27 offset:12800\n\t"
      "s_waitcnt lgkmcnt(12)\n\t"
      "v_mfma_f32_32x32x16_bf16 v[128:143], v[68:71], a[96:99], v[128:143]\n\t"
      "v_mfma_f32_32x32x16_bf16 v[144:159], v[68:71], a[224:227], v[144:159]\n\t"
      "s_add_u32 s60, s46, 3\n\t"
      "s_and_b32 s60, s60, 7\n\t"
      "s_mul_i32 s61, s60, 229376\n\t"
      "s_add_u32 m0, s57, 24576\n\t"
      "s_add_u32 s61, s61, s55\n\t"
      "buffer_load_dwordx4 v8, s[12:15], s61 offen lds\n\t"
      "ds_read_b128 a[108:111], v251 offset:0\n\t"
      "ds_read_b128 a[236:239], v255 offset:0\n\t"
      "ds_read_b64_tr_b16 v[88:89], v27 offset:6144\n\t"
      "ds_read_b64_tr_b16 v[90:91], v27 offset:6656\n\t"
      "s_waitcnt lgkmcnt(12)\n\t"
      "v_mfma_f32_32x32x16_bf16 v[160:175], v[72:75], a[100:103], v[160:175]\n\t"
      "v_mfma_f32_32x32x16_bf16 v[176:191], v[72:75], a[228:231], v[176:191]\n\t"
      "buffer_load_dwordx4 v9, s[12:15], s61 offen offset:1024 lds\n\t"
      "ds_read_b64_tr_b16 v[92:93], v27 offset:14336\n\t"
      "ds_read_b64_tr_b16 v[94:95], v27 offset:14848\n\t"
      "s_waitcnt lgkmcnt(12)\n\t"
      "v_mfma_f32_32x32x16_bf16 v[128:143], v[76:79], a[100:103], v[128:143]\n\t"
      "v_mfma_f32_32x32x16_bf16 v[144:159], v[76:79], a[228:231], v[144:159]\n\t"
      "s_add_u32 s60, s46, 4\n\t"
      "s_and_b32 s60, s60, 7\n\t"
      "s_mul_i32 s61, s60, 229376\n\t"
      "s_add_u32 m0, s57, 32768\n\t"
      "s_add_u32 s61, s61, s55\n\t"
      "buffer_load_dwordx4 v8, s[12:15], s61 offen lds\n\t"
      "s_waitcnt lgkmcnt(8)\n\t"
      "v_mfma_f32_32x32x16_bf16 v[160:175], v[80:83], a[104:107], v[160:175]\n\t"
      "v_mfma_f32_32x32x16_bf16 v[176:191], v[80:83], a[232:235], v[176:191]\n\t"
      "buffer_load_dwordx4 v9, s[12:15], s61 offen offset:1024 lds\n\t"
      "s_waitcnt vmcnt(13)\n\t"
      "s_waitcnt lgkmcnt(0)\n\t"
      "s_barrier\n\t"
      "ds_read_b128 a[112:115], v248 offset:32768\n\t"
      "ds_read_b128 a[240:243], v252 offset:32768\n\t"
      "ds_read_b64_tr_b16 v[96:97], v27 offset:32768\n\t"
      "ds_read_b64_tr_b16 v[98:99], v27 offset:33280\n\t"
      "ds_read_b64_tr_b16 v[100:101], v27 offset:40960\n\t"
      "ds_read_b64_tr_b16 v[102:103], v27 offset:41472\n\t"
      "v_mfma_f32_32x32x16_bf16 v[128:143], v[84:87], a[104:107], v[128:143]\n\t"
      "v_mfma_f32_32x32x16_bf16 v[144:159], v[84:87], a[232:235], v[144:159]\n\t"
      "s_add_u32 s60, s46, 5\n\t"
      "s_and_b32 s60, s60, 7\n\t"
      "s_mul_i32 s61, s60, 229376\n\t"
      "s_add_u32 m0, s57, 40960\n\t"
      "s_add_u32 s61, s61, s55\n\t"
      "buffer_load_dwordx4 v8, s[12:15], s61 offen lds\n\t"
      "ds_read_b128 a[116:119], v249 offset:32768\n\t"
      "ds_read_b128 a[244:247], v253 offset:32768\n\t"
      "ds_read_b64_tr_b16 v[104:105], v27 offset:34816\n\t"
      "ds_read_b64_tr_b16 v[106:107], v27 offset:35328\n\t"
      "v_mfma_f32_32x32x16_bf16 v[160:175], v[88:91], a[108:111], v[160:175]\n\t"
      "v_mfma_f32_32x32x16_bf16 v[176:191], v[88:91], a[236:239], v[176:191]\n\t"
      "buffer_load_dwordx4 v9, s[12:15], s61 offen offset:1024 lds\n\t"
      "ds_read_b64_tr_b16 v[108:109], v27 offset:43008\n\t"
      "ds_read_b64_tr_b16 v[110:111], v27 offset:43520\n\t"
      "v_mfma_f32_32x32x16_bf16 v[128:143], v[92:95], a[108:111], v[128:143]\n\t"
      "v_mfma_f32_32x32x16_bf16 v[144:159], v[92:95], a[236:239], v[144:159]\n\t"
      "s_add_u32 s60, s46, 6\n\t"
      "s_and_b32 s60, s60, 7\n\t"
      "s_mul_i32 s61, s60, 229376\n\t"
      "s_add_u32 m0, s57, 49152\n\t"
      "s_add_u32 s61, s61, s55\n\t"
      "buffer_load_dwordx4 v8, s[12:15], s61 offen lds\n\t"
      "ds_read_b128 a[120:123], v250 offset:32768\n\t"
      "ds_read_b128 a[248:251], v254 offset:32768\n\t"
      "ds_read_b64_tr_b16 v[112:113], v27 offset:36864\n\t"
      "ds_read_b64_tr_b16 v[114:115], v27 offset:37376\n\t"
      "s_waitcnt lgkmcnt(12)\n\t"
      "v_mfma_f32_32x32x16_bf16 v[160:175], v[96:99], a[112:115], v[160:175]\n\t"
      "v_mfma_f32_32x32x16_bf16 v[176:191], v[96:99], a[240:243], v[176:191]\n\t"
      "buffer_load_dwordx4 v9, s[12:15], s61 offen offset:1024 lds\n\t"
      "ds_read_b128 a[124:127], v251 offset:32768\n\t"
      "ds_read_b128 a[252:255], v255 offset:32768\n\t"
      "ds_read_b64_tr_b16 v[116:117], v27 offset:38912\n\t"
      "ds_read_b64_tr_b16 v[118:119], v27 offset:39424\n\t"
      "s_waitcnt lgkmcnt(14)\n\t"
      "v_mfma_f32_32x32x16_bf16 v[128:143], v[100:103], a[112:115], v[128:143]\n\t"
      "v_mfma_f32_32x32x16_bf16 v[144:159], v[100:103], a[240:243], v[144:159]\n\t"
      "s_add_u32 s60, s46, 7\n\t"
      "s_and_b32 s60, s60, 7\n\t"
      "s_mul_i32 s61, s60, 229376\n\t"
      "s_add_u32 m0, s57, 57344\n\t"
      "s_add_u32 s61, s61, s55\n\t"
      "buffer_load_dwordx4 v8, s[12:15], s61 offen lds\n\t"
      "ds_read_b64_tr_b16 v[120:121], v27 offset:45056\n\t"
      "ds_read_b64_tr_b16 v[122:123], v27 offset:45568\n\t"
      "s_waitcnt lgkmcnt(12)\n\t"
      "v_mfma_f32_32x32x16_bf16 v[160:175], v[104:107], a[116:119], v[160:175]\n\t"
      "v_mfma_f32_32x32x16_bf16 v[176:191], v[104:107], a[244:247], v[176:191]\n\t"
      "buffer_load_dwordx4 v9, s[12:15], s61 offen offset:1024 lds\n\t"
      "ds_read_b64_tr_b16 v[124:125], v27 offset:47104\n\t"
      "ds_read_b64_tr_b16 v[126:127], v27 offset:47616\n\t"
      "ds_read_b128 v[240:243], v11 offset:0\n\t"
      "ds_read_b128 v[244:247], v11 offset:32\n\t"
      "ds_read_b128 v[248:251], v11 offset:64\n\t"
      "ds_read_b128 v[252:255], v11 offset:96\n\t"
      "s_waitcnt lgkmcnt(15)\n\t"
      "v_mfma_f32_32x32x16_bf16 v[128:143], v[108:111], a[116:119], v[128:143]\n\t"
      "v_mfma_f32_32x32x16_bf16 v[144:159], v[108:111], a[244:247], v[144:159]\n\t"
      "s_add_u32 s60, s46, 0\n\t"
      "s_and_b32 s60, s60, 7\n\t"
      "s_mul_i32 s61, s60, 229376\n\t"
      "s_add_u32 m0, s84, 65536\n\t"
      "s_add_u32 s61, s61, s56\n\t"
      "buffer_load_dwordx4 v10, s[12:15], s61 offen lds\n\t"
      "s_waitcnt lgkmcnt(12)\n\t"
      "v_mfma_f32_32x32x16_bf16 v[160:175], v[112:115], a[120:123], v[160:175]\n\t"
      "v_mfma_f32_32x32x16_bf16 v[176:191], v[112:115], a[248:251], v[176:191]\n\t"
      "s_add_u32 s60, s46, 1\n\t"
      "s_and_b32 s60, s60, 7\n\t"
      "s_mul_i32 s61, s60, 229376\n\t"
      "s_add_u32 m0, s84, 69632\n\t"
      "s_add_u32 s61, s61, s56\n\t"
      "buffer_load_dwordx4 v10, s[12:15], s61 offen lds\n\t"
      "s_waitcnt vmcnt(10)\n\t"
      "s_waitcnt lgkmcnt(0)\n\t"
      "s_barrier\n\t"
      "v_mov_b32 v2, v29\n\t"
      "v_add_u32 v3, 3072, v29\n\t"
      "v_add_u32 v4, 6144, v29\n\t"
      "v_add_u32 v5, 9216, v29\n\t"
      "ds_read_b64_tr_b16 v[64:65], v26 offset:0\n\t"
      "ds_read_b64_tr_b16 v[66:67], v26 offset:512\n\t"
      "ds_read_b64_tr_b16 v[68:69], v26 offset:2048\n\t"
      "ds_read_b64_tr_b16 v[70:71], v26 offset:2560\n\t"
      "v_mfma_f32_32x32x16_bf16 v[160:175], v[116:119], a[124:127], v[160:175]\n\t"
      "v_mfma_f32_32x32x16_bf16 v[176:191], v[116:119], a[252:255], v[176:191]\n\t"
      "s_add_u32 s60, s46, 2\n\t"
      "s_and_b32 s60, s60, 7\n\t"
      "s_mul_i32 s61, s60, 229376\n\t"
      "s_add_u32 m0, s84, 73728\n\t"
      "s_add_u32 s61, s61, s56\n\t"
      "buffer_load_dwordx4 v10, s[12:15], s61 offen lds\n\t"
      "ds_read_b64_tr_b16 v[72:73], v26 offset:4096\n\t"
      "ds_read_b64_tr_b16 v[74:75], v26 offset:4608\n\t"
      "v_mfma_f32_32x32x16_bf16 v[128:143], v[120:123], a[120:123], v[128:143]\n\t"
      "s_add_u32 s64, s82, s70\n\t"
      "s_add_u32 s65, s82, s71\n\t"
      "s_nop 0\n\t"
      "v_cvt_pk_bf16_f32 v48, v160, v161\n\t"
      "v_mfma_f32_32x32x16_bf16 v[144:159], v[120:123], a[248:251], v[144:159]\n\t"
      "s_add_u32 s60, s46, 3\n\t"
      "s_and_b32 s60, s60, 7\n\t"
      "s_mul_i32 s61, s60, 229376\n\t"
      "s_add_u32 m0, s84, 77824\n\t"
      "s_add_u32 s61, s61, s56\n\t"
      "buffer_load_dwordx4 v10, s[12:15], s61 offen lds\n\t"
      "v_cvt_pk_bf16_f32 v49, v162, v163\n\t"
      "v_cvt_pk_bf16_f32 v50, v164, v165\n\t"
      "v_cvt_pk_bf16_f32 v51, v166, v167\n\t"
      "v_cvt_pk_bf16_f32 v52, v168, v169\n\t"
      "ds_read_b64_tr_b16 v[76:77], v26 offset:6144\n\t"
      "ds_read_b64_tr_b16 v[78:79], v26 offset:6656\n\t"
      "v_mfma_f32_32x32x16_bf16 v[128:143], v[124:127], a[124:127], v[128:143]\n\t"
      "v_cvt_pk_bf16_f32 v53, v170, v171\n\t"
      "v_cvt_pk_bf16_f32 v54, v172, v173\n\t"
      "v_cvt_pk_bf16_f32 v55, v174, v175\n\t"
      "v_pk_max_i16 v48, v48, 0\n\t"
      "v_mfma_f32_32x32x16_bf16 v[144:159], v[124:127], a[252:255], v[144:159]\n\t"
      "s_add_u32 s60, s46, 4\n\t"
      "s_and_b32 s60, s60, 7\n\t"
      "s_mul_i32 s61, s60, 229376\n\t"
      "s_add_u32 m0, s84, 81920\n\t"
      "s_add_u32 s61, s61, s56\n\t"
      "buffer_load_dwordx4 v10, s[12:15], s61 offen lds\n\t"
      "v_pk_max_i16 v49, v49, 0\n\t"
      "v_pk_max_i16 v50, v50, 0\n\t"
      "v_pk_max_i16 v51, v51, 0\n\t"
      "v_pk_max_i16 v52, v52, 0\n\t"
      "ds_read_b64_tr_b16 v[80:81], v26 offset:8192\n\t"
      "ds_read_b64_tr_b16 v[82:83], v26 offset:8704\n\t"
      "s_waitcnt lgkmcnt(0)\n\t"
      "s_getpc_b64 s[66:67]\n\t"
      ".Lgt_%=:\n\t"
      "s_mul_i32 s68, s42, 36\n\t"
      "s_add_u32 s68, s68, .Lgt0_%=-.Lgt_%=\n\t"
      "s_add_u32 s66, s66, s68\n\t"
      "s_addc_u32 s67, s67, 0\n\t"
      "s_setpc_b64 s[66:67]\n\t"
      ".Lgt0_%=:\n\t"
      "v_mfma_f32_32x32x16_bf16 v[224:239], v[240:243], a[0:3], 0\n\t"
      "v_mfma_f32_32x32x16_bf16 v[224:239], v[244:247], a[4:7], v[224:239]\n\t"
      "v_mfma_f32_32x32x16_bf16 v[224:239], v[248:251], a[8:11], v[224:239]\n\t"
      "v_mfma_f32_32x32x16_bf16 v[224:239], v[252:255], a[12:15], v[224:239]\n\t"
      "s_branch .Lgt9_%=\n\t"
      "v_mfma_f32_32x32x16_bf16 v[224:239], v[240:243], a[16:19], 0\n\t"
      "v_mfma_f32_32x32x16_bf16 v[224:239], v[244:247], a[20:23], v[224:239]\n\t"
      "v_mfma_f32_32x32x16_bf16 v[224:239], v[248:251], a[24:27], v[224:239]\n\t"
      "v_mfma_f32_32x32x16_bf16 v[224:239], v[252:255], a[28:31], v[224:239]\n\t"
      "s_branch .Lgt9_%=\n\t"
      "v_mfma_f32_32x32x16_bf16 v[224:239], v[240:243], a[32:35], 0\n\t"
      "v_mfma_f32_32x32x16_bf16 v[224:239], v[244:247], a[36:39], v[224:239]\n\t"
      "v_mfma_f32_32x32x16_bf16 v[224:239], v[248:251], a[40:43], v[224:239]\n\t"
      "v_mfma_f32_32x32x16_bf16 v[224:239], v[252:255], a[44:47], v[224:239]\n\t"
      "s_branch .Lgt9_%=\n\t"
      "v_mfma_f32_32x32x16_bf16 v[224:239], v[240:243], a[48:51], 0\n\t"
      "v_mfma_f32_32x32x16_bf16 v[224:239], v[244:247], a[52:55], v[224:239]\n\t"
      "v_mfma_f32_32x32x16_bf16 v[224:239], v[248:251], a[56:59], v[224:239]\n\t"
      "v_mfma_f32_32x32x16_bf16 v[224:239], v[252:255], a[60:63], v[224:239]\n\t"
      "s_branch .Lgt9_%=\n\t"
      "v_mfma_f32_32x32x16_bf16 v[224:239], v[240:243], a[64:67], 0\n\t"
      "v_mfma_f32_32x32x16_bf16 v[224:239], v[244:247], a[68:71], v[224:239]\n\t"
      "v_mfma_f32_32x32x16_bf16 v[224:239], v[248:251], a[72:75], v[224:239]\n\t"
      "v_mfma_f32_32x32x16_bf16 v[224:239], v[252:255], a[76:79], v[224:239]\n\t"
      "s_branch .Lgt9_%=\n\t"
      "v_mfma_f32_32x32x16_bf16 v[224:239], v[240:243], a[80:83], 0\n\t"
      "v_mfma_f32_32x32x16_bf16 v[224:239], v[244:247], a[84:87], v[224:239]\n\t"
      "v_mfma_f32_32x32x16_bf16 v[224:239], v[248:251], a[88:91], v[224:239]\n\t"
      "v_mfma_f32_32x32x16_bf16 v[224:239], v[252:255], a[92:95], v[224:239]\n\t"
      "s_branch .Lgt9_%=\n\t"
      "v_mfma_f32_32x32x16_bf16 v[224:239], v[240:243], a[96:99], 0\n\t"
      "v_mfma_f32_32x32x16_bf16 v[224:239], v[244:247], a[100:103], v[224:239]\n\t"
      "v_mfma_f32_32x32x16_bf16 v[224:239], v[248:251], a[104:107], v[224:239]\n\t"
      "v_mfma_f32_32x32x16_bf16 v[224:239], v[252:255], a[108:111], v[224:239]\n\t"
      "s_branch .Lgt9_%=\n\t"
      "v_mfma_f32_32x32x16_bf16 v[224:239], v[240:243], a[112:115], 0\n\t"
      "v_mfma_f32_32x32x16_bf16 v[224:239], v[244:247], a[116:119], v[224:239]\n\t"
      "v_mfma_f32_32x32x16_bf16 v[224:239], v[248:251], a[120:123], v[224:239]\n\t"
      "v_mfma_f32_32x32x16_bf16 v[224:239], v[252:255], a[124:127], v[224:239]\n\t"
      "s_branch .Lgt9_%=\n\t"
      ".Lgt9_%=:\n\t"
      "s_nop 10\n\t"
      "v_mov_b32 v58, v224\n\t"
      "v_pk_max_i16 v53, v53, 0\n\t"
      "v_pk_max_i16 v54, v54, 0\n\t"
      "v_pk_max_i16 v55, v55, 0\n\t"
      "v_cvt_pk_bf16_f32 v192, v176, v177\n\t"
      "v_mfma_f32_32x32x16_bf16 v[208:223], v[64:67], a[0:3], 0\n\t"
      "s_add_u32 s60, s46, 5\n\t"
      "s_and_b32 s60, s60, 7\n\t"
      "s_mul_i32 s61, s60, 229376\n\t"
      "s_add_u32 m0, s84, 86016\n\t"
      "s_add_u32 s61, s61, s56\n\t"
      "buffer_load_dwordx4 v10, s[12:15], s61 offen lds\n\t"
      "v_cvt_pk_bf16_f32 v193, v178, v179\n\t"
      "v_cvt_pk_bf16_f32 v194, v180, v181\n\t"
      "v_cvt_pk_bf16_f32 v195, v182, v183\n\t"
      "v_cvt_pk_bf16_f32 v196, v184, v185\n\t"
      "v_mfma_f32_32x32x16_bf16 v[224:239], v[64:67], a[128:131], 0\n\t"
      "v_cvt_pk_bf16_f32 v197, v186, v187\n\t"
      "v_cvt_pk_bf16_f32 v198, v188, v189\n\t"
      "v_cvt_pk_bf16_f32 v199, v190, v191\n\t"
      "v_pk_max_i16 v192, v192, 0\n\t"
      "ds_read_b64_tr_b16 v[84:85], v26 offset:10240\n\t"
      "ds_read_b64_tr_b16 v[86:87], v26 offset:10752\n\t"
      "v_mfma_f32_32x32x16_bf16 v[208:223], v[68:71], a[4:7], v[208:223]\n\t"
      "s_add_u32 s60, s46, 6\n\t"
      "s_and_b32 s60, s60, 7\n\t"
      "s_mul_i32 s61, s60, 229376\n\t"
      "s_add_u32 m0, s84, 90112\n\t"
      "s_add_u32 s61, s61, s56\n\t"
      "buffer_load_dwordx4 v10, s[12:15], s61 offen lds\n\t"
      "v_pk_max_i16 v193, v193, 0\n\t"
      "v_pk_max_i16 v194, v194, 0\n\t"
      "v_pk_max_i16 v195, v195, 0\n\t"
      "v_pk_max_i16 v196, v196, 0\n\t"
      "v_mfma_f32_32x32x16_bf16 v[224:239], v[68:71], a[132:135], v[224:239]\n\t"
      "v_pk_max_i16 v197, v197, 0\n\t"
      "v_pk_max_i16 v198, v198, 0\n\t"
      "v_pk_max_i16 v199, v199, 0\n\t"
      "ds_read_b64_tr_b16 v[88:89], v26 offset:12288\n\t"
      "ds_read_b64_tr_b16 v[90:91], v26 offset:12800\n\t"
      "v_mfma_f32_32x32x16_bf16 v[208:223], v[72:75], a[8:11], v[208:223]\n\t"
      "s_add_u32 s60, s46, 7\n\t"
      "s_and_b32 s60, s60, 7\n\t"
      "s_mul_i32 s61, s60, 229376\n\t"
      "s_add_u32 m0, s84, 94208\n\t"
      "s_add_u32 s61, s61, s56\n\t"
      "buffer_load_dwordx4 v10, s[12:15], s61 offen lds\n\t"
      "buffer_store_dwordx4 v[48:51], v1, s[24:27], s64 offen sc1\n\t"
      "buffer_store_dwordx4 v[52:55], v1, s[24:27], s64 offen offset:1024 sc1\n\t"
      "buffer_store_dwordx4 v[192:195], v1, s[24:27], s65 offen sc1\n\t"
      "buffer_store_dwordx4 v[196:199], v1, s[24:27], s65 offen offset:1024 sc1\n\t"
      "s_mov_b32 exec_hi, 0\n\t"
      "buffer_store_dword v58, v200, s[24:27], s85 offen sc1\n\t"
      "s_mov_b64 exec, -1\n\t"
      "v_cvt_pk_bf16_f32 v240, v128, v129\n\t"
      "v_cvt_pk_bf16_f32 v241, v130, v131\n\t"
      "v_mfma_f32_32x32x16_bf16 v[224:239], v[72:75], a[136:139], v[224:239]\n\t"
      "v_cvt_pk_bf16_f32 v242, v132, v133\n\t"
      "v_cvt_pk_bf16_f32 v243, v134, v135\n\t"
      "v_cvt_pk_bf16_f32 v244, v136, v137\n\t"
      "v_cvt_pk_bf16_f32 v245, v138, v139\n\t"
      "ds_read_b64_tr_b16 v[92:93], v26 offset:14336\n\t"
      "ds_read_b64_tr_b16 v[94:95], v26 offset:14848\n\t"
      "v_mfma_f32_32x32x16_bf16 v[208:223], v[76:79], a[12:15], v[208:223]\n\t"
      "s_add_u32 s60, s74, 0\n\t"
      "s_lshl_b32 s60, s60, 16\n\t"
      "s_add_u32 m0, s58, 81920\n\t"
      "s_add_u32 s60, s60, s73\n\t"
      "buffer_load_dwordx4 v2, s[16:19], s60 offen lds\n\t"
      "v_cvt_pk_bf16_f32 v246, v140, v141\n\t"
      "v_cvt_pk_bf16_f32 v247, v142, v143\n\t"
      "v_cvt_pk_bf16_f32 v248, v144, v145\n\t"
      "v_cvt_pk_bf16_f32 v249, v146, v147\n\t"
      "v_mfma_f32_32x32x16_bf16 v[224:239], v[76:79], a[140:143], v[224:239]\n\t"
      "v_cvt_pk_bf16_f32 v250, v148, v149\n\t"
      "v_cvt_pk_bf16_f32 v251, v150, v151\n\t"
      "v_cvt_pk_bf16_f32 v252, v152, v153\n\t"
      "v_cvt_pk_bf16_f32 v253, v154, v155\n\t"
      "ds_read_b64_tr_b16 v[96:97], v26 offset:16384\n\t"
      "ds_read_b64_tr_b16 v[98:99], v26 offset:16896\n\t"
      "v_mfma_f32_32x32x16_bf16 v[208:223], v[80:83], a[16:19], v[208:223]\n\t"
      "v_cvt_pk_bf16_f32 v254, v156, v157\n\t"
      "v_cvt_pk_bf16_f32 v255, v158, v159\n\t"
      "v_mfma_f32_32x32x16_bf16 v[176:191], v[40:43], v[240:243], 0\n\t"
      "v_mfma_f32_32x32x16_bf16 v[224:239], v[80:83], a[144:147], v[224:239]\n\t"
      "v_mfma_f32_32x32x16_bf16 v[128:143], v[44:47], v[244:247], 0\n\t"
      "s_nop 9\n\t"
      "v_max3_f32 v56, v176, v177, v178\n\t"
      "v_max3_f32 v56, v56, v179, v180\n\t"
      "ds_read_b64_tr_b16 v[100:101], v26 offset:18432\n\t"
      "ds_read_b64_tr_b16 v[102:103], v26 offset:18944\n\t"
      "s_waitcnt lgkmcnt(8)\n\t"
      "v_mfma_f32_32x32x16_bf16 v[208:223], v[84:87], a[20:23], v[208:223]\n\t"
      "buffer_load_dwordx4 v3, s[16:19], s60 offen offset:1024 lds\n\t"
      "v_max3_f32 v56, v56, v181, v182\n\t"
      "v_max3_f32 v56, v56, v183, v184\n\t"
      "v_max3_f32 v56, v56, v185, v186\n\t"
      "v_mfma_f32_32x32x16_bf16 v[224:239], v[84:87], a[148:151], v[224:239]\n\t"
      "v_max3_f32 v56, v56, v187, v188\n\t"
      "v_max3_f32 v56, v56, v189, v190\n\t"
      "v_max_f32 v56, v56, v191\n\t"
      "ds_read_b64_tr_b16 v[104:105], v26 offset:20480\n\t"
      "ds_read_b64_tr_b16 v[106:107], v26 offset:20992\n\t"
      "s_waitcnt lgkmcnt(8)\n\t"
      "v_mfma_f32_32x32x16_bf16 v[208:223], v[88:91], a[24:27], v[208:223]\n\t"
      "buffer_load_dwordx4 v4, s[16:19], s60 offen offset:2048 lds\n\t"
      "v_mul_f32 v56, s38, v56\n\t"
      "v_fma_f32 v176, v176, s39, v56\n\t"
      "v_fma_f32 v177, v177, s39, v56\n\t"
      "v_mfma_f32_32x32x16_bf16 v[224:239], v[88:91], a[152:155], v[224:239]\n\t"
      "v_fma_f32 v178, v178, s39, v56\n\t"
      "v_fma_f32 v179, v179, s39, v56\n\t"
      "v_fma_f32 v180, v180, s39, v56\n\t"
      "ds_read_b64_tr_b16 v[108:109], v26 offset:22528\n\t"
      "ds_read_b64_tr_b16 v[110:111], v26 offset:23040\n\t"
      "s_waitcnt lgkmcnt(8)\n\t"
      "v_mfma_f32_32x32x16_bf16 v[208:223], v[92:95], a[28:31], v[208:223]\n\t"
      "buffer_load_dwordx4 v5, s[16:19], s60 offen offset:3072 lds\n\t"
      "v_fma_f32 v181, v181, s39, v56\n\t"
      "v_fma_f32 v182, v182, s39, v56\n\t"
      "v_fma_f32 v183, v183, s39, v56\n\t"
      "v_mfma_f32_32x32x16_bf16 v[224:239], v[92:95], a[156:159], v[224:239]\n\t"
      "v_fma_f32 v184, v184, s39, v56\n\t"
      "v_fma_f32 v185, v185, s39, v56\n\t"
      "v_fma_f32 v186, v186, s39, v56\n\t"
      "ds_read_b64_tr_b16 v[112:113], v26 offset:24576\n\t"
      "ds_read_b64_tr_b16 v[114:115], v26 offset:25088\n\t"
      "s_waitcnt lgkmcnt(8)\n\t"
      "v_mfma_f32_32x32x16_bf16 v[208:223], v[96:99], a[32:35], v[208:223]\n\t"
      "v_fma_f32 v187, v187, s39, v56\n\t"
      "v_fma_f32 v188, v188, s39, v56\n\t"
      "v_fma_f32 v189, v189, s39, v56\n\t"
      "v_mfma_f32_32x32x16_bf16 v[224:239], v[96:99], a[160:163], v[224:239]\n\t"
      "v_fma_f32 v190, v190, s39, v56\n\t"
      "v_fma_f32 v191, v191, s39, v56\n\t"
      "v_exp_f32 v176, v176\n\t"
      "ds_read_b64_tr_b16 v[116:117], v26 offset:26624\n\t"
      "ds_read_b64_tr_b16 v[118:119], v26 offset:27136\n\t"
      "s_waitcnt lgkmcnt(8)\n\t"
      "v_mfma_f32_32x32x16_bf16 v[208:223], v[100:103], a[36:39], v[208:223]\n\t"
      "s_add_u32 s60, s74, 3\n\t"
      "s_lshl_b32 s60, s60, 16\n\t"
      "s_add_u32 m0, s58, 98304\n\t"
      "s_add_u32 s60, s60, s73\n\t"
      "buffer_load_dwordx4 v2, s[16:19], s60 offen lds\n\t"
      "v_exp_f32 v177, v177\n\t"
      "v_exp_f32 v178, v178\n\t"
      "v_exp_f32 v179, v179\n\t"
      "v_mfma_f32_32x32x16_bf16 v[224:239], v[100:103], a[164:167], v[224:239]\n\t"
      "v_add_f32 v56, v176, v177\n\t"
      "v_exp_f32 v180, v180\n\t"
      "v_add_f32 v56, v56, v178\n\t"
      "ds_read_b64_tr_b16 v[120:121], v26 offset:28672\n\t"
      "ds_read_b64_tr_b16 v[122:123], v26 offset:29184\n\t"
      "s_waitcnt lgkmcnt(8)\n\t"
      "v_mfma_f32_32x32x16_bf16 v[208:223], v[104:107], a[40:43], v[208:223]\n\t"
      "buffer_load_dwordx4 v3, s[16:19], s60 offen offset:1024 lds\n\t"
      "v_exp_f32 v181, v181\n\t"
      "v_add_f32 v56, v56, v179\n\t"
      "v_exp_f32 v182, v182\n\t"
      "v_mfma_f32_32x32x16_bf16 v[224:239], v[104:107], a[168:171], v[224:239]\n\t"
      "v_add_f32 v56, v56, v180\n\t"
      "v_exp_f32 v183, v183\n\t"
      "v_add_f32 v56, v56, v181\n\t"
      "ds_read_b64_tr_b16 v[124:125], v26 offset:30720\n\t"
      "ds_read_b64_tr_b16 v[126:127], v26 offset:31232\n\t"
      "s_waitcnt lgkmcnt(8)\n\t"
      "v_mfma_f32_32x32x16_bf16 v[208:223], v[108:111], a[44:47], v[208:223]\n\t"
      "buffer_load_dwordx4 v4, s[16:19], s60 offen offset:2048 lds\n\t"
      "v_exp_f32 v184, v184\n\t"
      "v_add_f32 v56, v56, v182\n\t"
      "v_exp_f32 v185, v185\n\t"
      "v_mfma_f32_32x32x16_bf16 v[224:239], v[108:111], a[172:175], v[224:239]\n\t"
      "v_add_f32 v56, v56, v183\n\t"
      "v_exp_f32 v186, v186\n\t"
      "v_add_f32 v56, v56, v184\n\t"
      "s_waitcnt lgkmcnt(6)\n\t"
      "v_mfma_f32_32x32x16_bf16 v[208:223], v[112:115], a[48:51], v[208:223]\n\t"
      "v_exp_f32 v187, v187\n\t"
      "v_add_f32 v56, v56, v185\n\t"
      "v_exp_f32 v188, v188\n\t"
      "v_mfma_f32_32x32x16_bf16 v[224:239], v[112:115], a[176:179], v[224:239]\n\t"
      "v_add_f32 v56, v56, v186\n\t"
      "v_exp_f32 v189, v189\n\t"
      "v_add_f32 v56, v56, v187\n\t"
      "s_waitcnt vmcnt(20)\n\t"
      "s_waitcnt lgkmcnt(0)\n\t"
      "s_barrier\n\t"
      "ds_read_b64_tr_b16 v[64:65], v26 offset:32768\n\t"
      "ds_read_b64_tr_b16 v[66:67], v26 offset:33280\n\t"
      "ds_read_b64_tr_b16 v[68:69], v26 offset:34816\n\t"
      "ds_read_b64_tr_b16 v[70:71], v26 offset:35328\n\t"
      "v_mfma_f32_32x32x16_bf16 v[208:223], v[116:119], a[52:55], v[208:223]\n\t"
      "buffer_load_dwordx4 v5, s[16:19], s60 offen offset:3072 lds\n\t"
      "v_exp_f32 v190, v190\n\t"
      "v_add_f32 v56, v56, v188\n\t"
      "v_exp_f32 v191, v191\n\t"
      "v_mfma_f32_32x32x16_bf16 v[224:239], v[116:119], a[180:183], v[224:239]\n\t"
      "v_add_f32 v56, v56, v189\n\t"
      "v_add_f32 v56, v56, v190\n\t"
      "v_add_f32 v56, v56, v191\n\t"
      "ds_read_b64_tr_b16 v[72:73], v26 offset:36864\n\t"
      "ds_read_b64_tr_b16 v[74:75], v26 offset:37376\n\t"
      "v_mfma_f32_32x32x16_bf16 v[208:223], v[120:123], a[56:59], v[208:223]\n\t"
      "s_add_u32 s60, s74, 1\n\t"
      "s_lshl_b32 s60, s60, 16\n\t"
      "s_add_u32 m0, s58, 114688\n\t"
      "s_add_u32 s60, s60, s73\n\t"
      "buffer_load_dwordx4 v2, s[16:19], s60 offen lds\n\t"
      "v_rcp_f32 v56, v56\n\t"
      "s_nop 0\n\t"
      "v_mul_f32 v176, v176, v56\n\t"
      "v_mul_f32 v177, v177, v56\n\t"
      "v_mfma_f32_32x32x16_bf16 v[224:239], v[120:123], a[184:187], v[224:239]\n\t"
      "v_mul_f32 v178, v178, v56\n\t"
      "v_mul_f32 v179, v179, v56\n\t"
      "v_mul_f32 v180, v180, v56\n\t"
      "ds_read_b64_tr_b16 v[76:77], v26 offset:38912\n\t"
      "ds_read_b64_tr_b16 v[78:79], v26 offset:39424\n\t"
      "v_mfma_f32_32x32x16_bf16 v[208:223], v[124:127], a[60:63], v[208:223]\n\t"
      "s_waitcnt vmcnt(9)\n\t"
      "s_add_u32 s78, s47, 131072\n\t"
      "s_mov_b64 exec, 1\n\t"
      "buffer_store_dwordx2 v[62:63], off, s[24:27], s78 sc1\n\t"
      "s_mov_b64 exec, -1\n\t"
      "v_mul_f32 v181, v181, v56\n\t"
      "v_mul_f32 v182, v182, v56\n\t"
      "v_mul_f32 v183, v183, v56\n\t"
      "v_mfma_f32_32x32x16_bf16 v[224:239], v[124:127], a[188:191], v[224:239]\n\t"
      "v_mul_f32 v184, v184, v56\n\t"
      "v_mul_f32 v185, v185, v56\n\t"
      "v_mul_f32 v186, v186, v56\n\t"
      "ds_read_b64_tr_b16 v[80:81], v26 offset:40960\n\t"
      "ds_read_b64_tr_b16 v[82:83], v26 offset:41472\n\t"
      "s_waitcnt lgkmcnt(8)\n\t"
      "v_mfma_f32_32x32x16_bf16 v[208:223], v[64:67], a[64:67], v[208:223]\n\t"
      "v_mul_f32 v187, v187, v56\n\t"
      "v_mul_f32 v188, v188, v56\n\t"
      "v_mul_f32 v189, v189, v56\n\t"
      "v_mfma_f32_32x32x16_bf16 v[224:239], v[64:67], a[192:195], v[224:239]\n\t"
      "v_mul_f32 v190, v190, v56\n\t"
      "v_mul_f32 v191, v191, v56\n\t"
      "v_cvt_pk_bf16_f32 v144, v176, v177\n\t"
      "ds_read_b64_tr_b16 v[84:85], v26 offset:43008\n\t"
      "ds_read_b64_tr_b16 v[86:87], v26 offset:43520\n\t"
      "s_waitcnt lgkmcnt(8)\n\t"
      "v_mfma_f32_32x32x16_bf16 v[208:223], v[68:71], a[68:71], v[208:223]\n\t"
      "buffer_load_dwordx4 v3, s[16:19], s60 offen offset:1024 lds\n\t"
      "v_cvt_pk_bf16_f32 v145, v178, v179\n\t"
      "v_cvt_pk_bf16_f32 v146, v180, v181\n\t"
      "v_cvt_pk_bf16_f32 v147, v182, v183\n\t"
      "v_mfma_f32_32x32x16_bf16 v[224:239], v[68:71], a[196:199], v[224:239]\n\t"
      "v_cvt_pk_bf16_f32 v148, v184, v185\n\t"
      "v_cvt_pk_bf16_f32 v149, v186, v187\n\t"
      "v_cvt_pk_bf16_f32 v150, v188, v189\n\t"
      "ds_read_b64_tr_b16 v[88:89], v26 offset:45056\n\t"
      "ds_read_b64_tr_b16 v[90:91], v26 offset:45568\n\t"
      "s_waitcnt lgkmcnt(8)\n\t"
      "v_mfma_f32_32x32x16_bf16 v[208:223], v[72:75], a[72:75], v[208:223]\n\t"
      "buffer_load_dwordx4 v4, s[16:19], s60 offen offset:2048 lds\n\t"
      "v_cvt_pk_bf16_f32 v151, v190, v191\n\t"
      "v_mfma_f32_32x32x16_bf16 v[224:239], v[72:75], a[200:203], v[224:239]\n\t"
      "ds_read_b64_tr_b16 v[92:93], v26 offset:47104\n\t"
      "ds_read_b64_tr_b16 v[94:95], v26 offset:47616\n\t"
      "s_waitcnt lgkmcnt(8)\n\t"
      "v_mfma_f32_32x32x16_bf16 v[208:223], v[76:79], a[76:79], v[208:223]\n\t"
      "buffer_load_dwordx4 v5, s[16:19], s60 offen offset:3072 lds\n\t"
      "v_mfma_f32_32x32x16_bf16 v[224:239], v[76:79], a[204:207], v[224:239]\n\t"
      "ds_read_b64_tr_b16 v[96:97], v26 offset:49152\n\t"
      "ds_read_b64_tr_b16 v[98:99], v26 offset:49664\n\t"
      "s_waitcnt lgkmcnt(8)\n\t"
      "v_mfma_f32_32x32x16_bf16 v[208:223], v[80:83], a[80:83], v[208:223]\n\t"
      "s_add_u32 s60, s74, 4\n\t"
      "s_lshl_b32 s60, s60, 16\n\t"
      "s_add_u32 m0, s58, 131072\n\t"
      "s_add_u32 s60, s60, s73\n\t"
      "buffer_load_dwordx4 v2, s[16:19], s60 offen lds\n\t"
      "v_mfma_f32_32x32x16_bf16 v[224:239], v[80:83], a[208:211], v[224:239]\n\t"
      "ds_read_b64_tr_b16 v[100:101], v26 offset:51200\n\t"
      "ds_read_b64_tr_b16 v[102:103], v26 offset:51712\n\t"
      "s_waitcnt lgkmcnt(8)\n\t"
      "v_mfma_f32_32x32x16_bf16 v[208:223], v[84:87], a[84:87], v[208:223]\n\t"
      "buffer_load_dwordx4 v3, s[16:19], s60 offen offset:1024 lds\n\t"
      "v_mfma_f32_32x32x16_bf16 v[224:239], v[84:87], a[212:215], v[224:239]\n\t"
      "ds_read_b64_tr_b16 v[104:105], v26 offset:53248\n\t"
      "ds_read_b64_tr_b16 v[106:107], v26 offset:53760\n\t"
      "s_waitcnt lgkmcnt(8)\n\t"
      "v_mfma_f32_32x32x16_bf16 v[208:223], v[88:91], a[88:91], v[208:223]\n\t"
      "buffer_load_dwordx4 v4, s[16:19], s60 offen offset:2048 lds\n\t"
      "v_mfma_f32_32x32x16_bf16 v[224:239], v[88:91], a[216:219], v[224:239]\n\t"
      "ds_read_b64_tr_b16 v[108:109], v26 offset:55296\n\t"
      "ds_read_b64_tr_b16 v[110:111], v26 offset:55808\n\t"
      "s_waitcnt lgkmcnt(8)\n\t"
      "v_mfma_f32_32x32x16_bf16 v[208:223], v[92:95], a[92:95], v[208:223]\n\t"
      "buffer_load_dwordx4 v5, s[16:19], s60 offen offset:3072 lds\n\t"
      "v_mfma_f32_32x32x16_bf16 v[224:239], v[92:95], a[220:223], v[224:239]\n\t"
      "ds_read_b64_tr_b16 v[112:113], v26 offset:57344\n\t"
      "ds_read_b64_tr_b16 v[114:115], v26 offset:57856\n\t"
      "s_waitcnt lgkmcnt(8)\n\t"
      "v_mfma_f32_32x32x16_bf16 v[208:223], v[96:99], a[96:99], v[208:223]\n\t"
      "v_mfma_f32_32x32x16_bf16 v[224:239], v[96:99], a[224:227], v[224:239]\n\t"
      "ds_read_b64_tr_b16 v[116:117], v26 offset:59392\n\t"
      "ds_read_b64_tr_b16 v[118:119], v26 offset:59904\n\t"
      "s_waitcnt lgkmcnt(8)\n\t"
      "v_mfma_f32_32x32x16_bf16 v[208:223], v[100:103], a[100:103], v[208:223]\n\t"
      "v_mfma_f32_32x32x16_bf16 v[224:239], v[100:103], a[228:231], v[224:239]\n\t"
      "ds_read_b64_tr_b16 v[120:121], v26 offset:61440\n\t"
      "ds_read_b64_tr_b16 v[122:123], v26 offset:61952\n\t"
      "s_waitcnt lgkmcnt(8)\n\t"
      "v_mfma_f32_32x32x16_bf16 v[208:223], v[104:107], a[104:107], v[208:223]\n\t"
      "v_mfma_f32_32x32x16_bf16 v[224:239], v[104:107], a[232:235], v[224:239]\n\t"
      "ds_read_b64_tr_b16 v[124:125], v26 offset:63488\n\t"
      "ds_read_b64_tr_b16 v[126:127], v26 offset:64000\n\t"
      "s_waitcnt lgkmcnt(8)\n\t"
      "v_mfma_f32_32x32x16_bf16 v[208:223], v[108:111], a[108:111], v[208:223]\n\t"
      "v_mfma_f32_32x32x16_bf16 v[224:239], v[108:111], a[236:239], v[224:239]\n\t"
      "s_waitcnt lgkmcnt(6)\n\t"
      "v_mfma_f32_32x32x16_bf16 v[208:223], v[112:115], a[112:115], v[208:223]\n\t"
      "v_mfma_f32_32x32x16_bf16 v[224:239], v[112:115], a[240:243], v[224:239]\n\t"
      "s_waitcnt lgkmcnt(0)\n\t"
      "s_barrier\n\t"
      "ds_read_b64_tr_b16 v[64:65], v12 offset:0\n\t"
      "ds_read_b64_tr_b16 v[66:67], v12 offset:256\n\t"
      "ds_read_b64_tr_b16 v[68:69], v12 offset:1024\n\t"
      "ds_read_b64_tr_b16 v[70:71], v12 offset:1280\n\t"
      "v_mfma_f32_32x32x16_bf16 v[208:223], v[116:119], a[116:119], v[208:223]\n\t"
      "v_mfma_f32_32x32x16_bf16 v[224:239], v[116:119], a[244:247], v[224:239]\n\t"
      "ds_read_b64_tr_b16 v[72:73], v12 offset:2048\n\t"
      "ds_read_b64_tr_b16 v[74:75], v12 offset:2304\n\t"
      "v_mfma_f32_32x32x16_bf16 v[208:223], v[120:123], a[120:123], v[208:223]\n\t"
      "v_mfma_f32_32x32x16_bf16 v[224:239], v[120:123], a[248:251], v[224:239]\n\t"
      "ds_read_b64_tr_b16 v[76:77], v12 offset:3072\n\t"
      "ds_read_b64_tr_b16 v[78:79], v12 offset:3328\n\t"
      "ds_read_b64_tr_b16 v[80:81], v12 offset:4096\n\t"
      "ds_read_b64_tr_b16 v[82:83], v12 offset:4352\n\t"
      "ds_read_b64_tr_b16 v[84:85], v12 offset:5120\n\t"
      "ds_read_b64_tr_b16 v[86:87], v12 offset:5376\n\t"
      "v_mfma_f32_32x32x16_bf16 v[208:223], v[124:127], a[124:127], v[208:223]\n\t"
      "v_mfma_f32_32x32x16_bf16 v[224:239], v[124:127], a[252:255], v[224:239]\n\t"
      "ds_read_b64_tr_b16 v[88:89], v12 offset:6144\n\t"
      "ds_read_b64_tr_b16 v[90:91], v12 offset:6400\n\t"
      "s_waitcnt lgkmcnt(12)\n\t"
      "v_mfma_f32_32x32x16_bf16 v[160:175], v[64:67], a[0:3], 0\n\t"
      "ds_read_b64_tr_b16 v[92:93], v12 offset:7168\n\t"
      "ds_read_b64_tr_b16 v[94:95], v12 offset:7424\n\t"
      "s_waitcnt lgkmcnt(12)\n\t"
      "v_mfma_f32_32x32x16_bf16 v[160:175], v[68:71], a[4:7], v[160:175]\n\t"
      "s_add_u32 s64, s82, s70\n\t"
      "s_add_u32 s65, s82, s71\n\t"
      "ds_read_b64_tr_b16 v[96:97], v12 offset:8192\n\t"
      "ds_read_b64_tr_b16 v[98:99], v12 offset:8448\n\t"
      "s_waitcnt lgkmcnt(12)\n\t"
      "v_mfma_f32_32x32x16_bf16 v[160:175], v[72:75], a[8:11], v[160:175]\n\t"
      "s_add_u32 s64, s64, 4096\n\t"
      "s_add_u32 s65, s65, 4096\n\t"
      "ds_read_b64_tr_b16 v[100:101], v12 offset:9216\n\t"
      "ds_read_b64_tr_b16 v[102:103], v12 offset:9472\n\t"
      "s_waitcnt lgkmcnt(12)\n\t"
      "v_mfma_f32_32x32x16_bf16 v[160:175], v[76:79], a[12:15], v[160:175]\n\t"
      "s_mov_b32 s78, 131072\n\t"
      "s_mov_b32 exec_lo, 16776975\n\t"
      "s_mov_b32 exec_hi, 0\n\t"
      "buffer_load_dwordx2 v[36:37], v38, s[24:27], s78 offen sc1\n\t"
      "s_mov_b64 exec, -1\n\t"
      "v_cvt_pk_bf16_f32 v48, v208, v209\n\t"
      "v_cvt_pk_bf16_f32 v49, v210, v211\n\t"
      "ds_read_b64_tr_b16 v[104:105], v12 offset:10240\n\t"
      "ds_read_b64_tr_b16 v[106:107], v12 offset:10496\n\t"
      "s_waitcnt lgkmcnt(12)\n\t"
      "v_mfma_f32_32x32x16_bf16 v[160:175], v[80:83], a[16:19], v[160:175]\n\t"
      "v_cvt_pk_bf16_f32 v50, v212, v213\n\t"
      "v_cvt_pk_bf16_f32 v51, v214, v215\n\t"
      "ds_read_b64_tr_b16 v[108:109], v12 offset:11264\n\t"
      "ds_read_b64_tr_b16 v[110:111], v12 offset:11520\n\t"
      "s_waitcnt lgkmcnt(12)\n\t"
      "v_mfma_f32_32x32x16_bf16 v[160:175], v[84:87], a[20:23], v[160:175]\n\t"
      "v_cvt_pk_bf16_f32 v52, v216, v217\n\t"
      "v_cvt_pk_bf16_f32 v53, v218, v219\n\t"
      "ds_read_b64_tr_b16 v[112:113], v12 offset:12288\n\t"
      "ds_read_b64_tr_b16 v[114:115], v12 offset:12544\n\t"
      "s_waitcnt lgkmcnt(12)\n\t"
      "v_mfma_f32_32x32x16_bf16 v[160:175], v[88:91], a[24:27], v[160:175]\n\t"
      "v_cvt_pk_bf16_f32 v54, v220, v221\n\t"
      "v_cvt_pk_bf16_f32 v55, v222, v223\n\t"
      "ds_read_b64_tr_b16 v[116:117], v12 offset:13312\n\t"
      "ds_read_b64_tr_b16 v[118:119], v12 offset:13568\n\t"
      "s_waitcnt lgkmcnt(12)\n\t"
      "v_mfma_f32_32x32x16_bf16 v[160:175], v[92:95], a[28:31], v[160:175]\n\t"
      "v_pk_max_i16 v48, v48, 0\n\t"
      "v_pk_max_i16 v49, v49, 0\n\t"
      "ds_read_b64_tr_b16 v[120:121], v12 offset:14336\n\t"
      "ds_read_b64_tr_b16 v[122:123], v12 offset:14592\n\t"
      "s_waitcnt lgkmcnt(12)\n\t"
      "v_mfma_f32_32x32x16_bf16 v[160:175], v[96:99], a[32:35], v[160:175]\n\t"
      "v_pk_max_i16 v50, v50, 0\n\t"
      "v_pk_max_i16 v51, v51, 0\n\t"
      "ds_read_b64_tr_b16 v[124:125], v12 offset:15360\n\t"
      "ds_read_b64_tr_b16 v[126:127], v12 offset:15616\n\t"
      "s_waitcnt lgkmcnt(12)\n\t"
      "v_mfma_f32_32x32x16_bf16 v[160:175], v[100:103], a[36:39], v[160:175]\n\t"
      "v_pk_max_i16 v52, v52, 0\n\t"
      "v_pk_max_i16 v53, v53, 0\n\t"
      "ds_read_b64_tr_b16 v[64:65], v12 offset:16384\n\t"
      "ds_read_b64_tr_b16 v[66:67], v12 offset:16640\n\t"
      "s_waitcnt lgkmcnt(12)\n\t"
      "v_mfma_f32_32x32x16_bf16 v[160:175], v[104:107], a[40:43], v[160:175]\n\t"
      "v_pk_max_i16 v54, v54, 0\n\t"
      "v_pk_max_i16 v55, v55, 0\n\t"
      "ds_read_b64_tr_b16 v[68:69], v12 offset:17408\n\t"
      "ds_read_b64_tr_b16 v[70:71], v12 offset:17664\n\t"
      "s_waitcnt lgkmcnt(12)\n\t"
      "v_mfma_f32_32x32x16_bf16 v[160:175], v[108:111], a[44:47], v[160:175]\n\t"
      "v_cvt_pk_bf16_f32 v192, v224, v225\n\t"
      "v_cvt_pk_bf16_f32 v193, v226, v227\n\t"
      "ds_read_b64_tr_b16 v[72:73], v12 offset:18432\n\t"
      "ds_read_b64_tr_b16 v[74:75], v12 offset:18688\n\t"
      "s_waitcnt lgkmcnt(12)\n\t"
      "v_mfma_f32_32x32x16_bf16 v[160:175], v[112:115], a[48:51], v[160:175]\n\t"
      "v_cvt_pk_bf16_f32 v194, v228, v229\n\t"
      "v_cvt_pk_bf16_f32 v195, v230, v231\n\t"
      "ds_read_b64_tr_b16 v[76:77], v12 offset:19456\n\t"
      "ds_read_b64_tr_b16 v[78:79], v12 offset:19712\n\t"
      "s_waitcnt lgkmcnt(12)\n\t"
      "v_mfma_f32_32x32x16_bf16 v[160:175], v[116:119], a[52:55], v[160:175]\n\t"
      "s_waitcnt vmcnt(0)\n\t"
      "v_cmp_eq_u64 vcc, s[48:49], v[36:37]\n\t"
      "s_nop 0\n\t"
      "s_and_b32 s78, vcc_lo, 16776975\n\t"
      "s_cmp_eq_u32 s78, 16776975\n\t"
      "s_cbranch_scc1 .Lskip_32_%=\n\t"
      "s_mov_b32 s79, 32768\n\t"
      ".Lspin_33_%=:\n\t"
      "s_sleep 1\n\t"
      "s_mov_b32 s78, 131072\n\t"
      "s_mov_b32 exec_lo, 16776975\n\t"
      "s_mov_b32 exec_hi, 0\n\t"
      "buffer_load_dwordx2 v[36:37], v38, s[24:27], s78 offen sc1\n\t"
      "s_mov_b64 exec, -1\n\t"
      "s_waitcnt vmcnt(0)\n\t"
      "v_cmp_eq_u64 vcc, s[48:49], v[36:37]\n\t"
      "s_nop 0\n\t"
      "s_and_b32 s78, vcc_lo, 16776975\n\t"
      "s_cmp_eq_u32 s78, 16776975\n\t"
      "s_cbranch_scc1 .Lskip_32_%=\n\t"
      "s_sub_u32 s79, s79, 1\n\t"
      "s_cmp_lg_u32 s79, 0\n\t"
      "s_cbranch_scc1 .Lspin_33_%=\n\t"
      ".Lskip_32_%=:\n\t"
      "s_add_u32 s62, s74, 0\n\t"
      "s_lshl_b32 s62, s62, 12\n\t"
      "s_add_u32 s62, s62, s72\n\t"
      "buffer_load_dwordx4 a[128:131], v1, s[24:27], s62 offen sc1\n\t"
      "v_cvt_pk_bf16_f32 v196, v232, v233\n\t"
      "v_cvt_pk_bf16_f32 v197, v234, v235\n\t"
      "ds_read_b64_tr_b16 v[80:81], v12 offset:20480\n\t"
      "ds_read_b64_tr_b16 v[82:83], v12 offset:20736\n\t"
      "s_waitcnt lgkmcnt(12)\n\t"
      "v_mfma_f32_32x32x16_bf16 v[160:175], v[120:123], a[56:59], v[160:175]\n\t"
      "v_cvt_pk_bf16_f32 v198, v236, v237\n\t"
      "v_cvt_pk_bf16_f32 v199, v238, v239\n\t"
      "ds_read_b64_tr_b16 v[84:85], v12 offset:21504\n\t"
      "ds_read_b64_tr_b16 v[86:87], v12 offset:21760\n\t"
      "s_waitcnt lgkmcnt(12)\n\t"
      "v_mfma_f32_32x32x16_bf16 v[160:175], v[124:127], a[60:63], v[160:175]\n\t"
      "buffer_load_dwordx4 a[132:135], v1, s[24:27], s62 offen offset:1024 sc1\n\t"
      "v_pk_max_i16 v192, v192, 0\n\t"
      "v_pk_max_i16 v193, v193, 0\n\t"
      "ds_read_b64_tr_b16 v[88:89], v12 offset:22528\n\t"
      "ds_read_b64_tr_b16 v[90:91], v12 offset:22784\n\t"
      "s_waitcnt lgkmcnt(12)\n\t"
      "v_mfma_f32_32x32x16_bf16 v[160:175], v[64:67], a[64:67], v[160:175]\n\t"
      "v_pk_max_i16 v194, v194, 0\n\t"
      "v_pk_max_i16 v195, v195, 0\n\t"
      "ds_read_b64_tr_b16 v[92:93], v12 offset:23552\n\t"
      "ds_read_b64_tr_b16 v[94:95], v12 offset:23808\n\t"
      "s_waitcnt lgkmcnt(12)\n\t"
      "v_mfma_f32_32x32x16_bf16 v[160:175], v[68:71], a[68:71], v[160:175]\n\t"
      "v_pk_max_i16 v196, v196, 0\n\t"
      "v_pk_max_i16 v197, v197, 0\n\t"
      "ds_read_b64_tr_b16 v[96:97], v12 offset:24576\n\t"
      "ds_read_b64_tr_b16 v[98:99], v12 offset:24832\n\t"
      "s_waitcnt lgkmcnt(12)\n\t"
      "v_mfma_f32_32x32x16_bf16 v[160:175], v[72:75], a[72:75], v[160:175]\n\t"
      "v_pk_max_i16 v198, v198, 0\n\t"
      "v_pk_max_i16 v199, v199, 0\n\t"
      "ds_read_b64_tr_b16 v[100:101], v12 offset:25600\n\t"
      "ds_read_b64_tr_b16 v[102:103], v12 offset:25856\n\t"
      "s_waitcnt lgkmcnt(12)\n\t"
      "v_mfma_f32_32x32x16_bf16 v[160:175], v[76:79], a[76:79], v[160:175]\n\t"
      "buffer_load_dwordx4 a[136:139], v1, s[24:27], s62 offen offset:2048 sc1\n\t"
      "buffer_store_dwordx4 v[48:51], v1, s[24:27], s64 offen sc1\n\t"
      "buffer_store_dwordx4 v[52:55], v1, s[24:27], s64 offen offset:1024 sc1\n\t"
      "buffer_store_dwordx4 v[192:195], v1, s[24:27], s65 offen sc1\n\t"
      "buffer_store_dwordx4 v[196:199], v1, s[24:27], s65 offen offset:1024 sc1\n\t"
      "ds_read_b64_tr_b16 v[104:105], v12 offset:26624\n\t"
      "ds_read_b64_tr_b16 v[106:107], v12 offset:26880\n\t"
      "s_waitcnt lgkmcnt(12)\n\t"
      "v_mfma_f32_32x32x16_bf16 v[160:175], v[80:83], a[80:83], v[160:175]\n\t"
      "ds_read_b64_tr_b16 v[108:109], v12 offset:27648\n\t"
      "ds_read_b64_tr_b16 v[110:111], v12 offset:27904\n\t"
      "s_waitcnt lgkmcnt(12)\n\t"
      "v_mfma_f32_32x32x16_bf16 v[160:175], v[84:87], a[84:87], v[160:175]\n\t"
      "buffer_load_dwordx4 a[140:143], v1, s[24:27], s62 offen offset:3072 sc1\n\t"
      "ds_read_b64_tr_b16 v[112:113], v12 offset:28672\n\t"
      "ds_read_b64_tr_b16 v[114:115], v12 offset:28928\n\t"
      "s_waitcnt lgkmcnt(12)\n\t"
      "v_mfma_f32_32x32x16_bf16 v[160:175], v[88:91], a[88:91], v[160:175]\n\t"
      "ds_read_b64_tr_b16 v[116:117], v12 offset:29696\n\t"
      "ds_read_b64_tr_b16 v[118:119], v12 offset:29952\n\t"
      "s_waitcnt lgkmcnt(12)\n\t"
      "v_mfma_f32_32x32x16_bf16 v[160:175], v[92:95], a[92:95], v[160:175]\n\t"
      "s_add_u32 s62, s74, 3\n\t"
      "s_lshl_b32 s62, s62, 12\n\t"
      "s_add_u32 s62, s62, s72\n\t"
      "buffer_load_dwordx4 a[144:147], v1, s[24:27], s62 offen sc1\n\t"
      "ds_read_b64_tr_b16 v[120:121], v12 offset:30720\n\t"
      "ds_read_b64_tr_b16 v[122:123], v12 offset:30976\n\t"
      "s_waitcnt lgkmcnt(12)\n\t"
      "v_mfma_f32_32x32x16_bf16 v[160:175], v[96:99], a[96:99], v[160:175]\n\t"
      "ds_read_b64_tr_b16 v[124:125], v12 offset:31744\n\t"
      "ds_read_b64_tr_b16 v[126:127], v12 offset:32000\n\t"
      "s_waitcnt lgkmcnt(0)\n\t"
      "s_barrier\n\t"
      "v_mfma_f32_32x32x16_bf16 v[160:175], v[100:103], a[100:103], v[160:175]\n\t"
      "v_mfma_f32_32x32x16_bf16 v[160:175], v[104:107], a[104:107], v[160:175]\n\t"
      "v_mfma_f32_32x32x16_bf16 v[160:175], v[108:111], a[108:111], v[160:175]\n\t"
      "buffer_load_dwordx4 a[148:151], v1, s[24:27], s62 offen offset:1024 sc1\n\t"
      "v_mfma_f32_32x32x16_bf16 v[160:175], v[112:115], a[112:115], v[160:175]\n\t"
      "v_mfma_f32_32x32x16_bf16 v[160:175], v[116:119], a[116:119], v[160:175]\n\t"
      "buffer_load_dwordx4 a[152:155], v1, s[24:27], s62 offen offset:2048 sc1\n\t"
      "v_mfma_f32_32x32x16_bf16 v[160:175], v[120:123], a[120:123], v[160:175]\n\t"
      "v_mfma_f32_32x32x16_bf16 v[160:175], v[124:127], a[124:127], v[160:175]\n\t"
      "buffer_load_dwordx4 a[156:159], v1, s[24:27], s62 offen offset:3072 sc1\n\t"
      "buffer_load_dwordx4 v[6:9], v200, s[24:27], s35 offen sc1\n\t"
      "s_xor_b32 s63, s35, 1024\n\t"
      "buffer_load_dwordx4 v[204:207], v200, s[24:27], s63 offen sc1\n\t"
      "v_mfma_f32_32x32x16_bf16 v[208:223], v[40:43], v[248:251], 0\n\t"
      "v_mfma_f32_32x32x16_bf16 v[224:239], v[44:47], v[252:255], 0\n\t"
      "s_nop 5\n\t"
      "v_cvt_pk_bf16_f32 v48, v160, v161\n\t"
      "v_cvt_pk_bf16_f32 v49, v162, v163\n\t"
      "v_cvt_pk_bf16_f32 v50, v164, v165\n\t"
      "v_cvt_pk_bf16_f32 v51, v166, v167\n\t"
      "v_cvt_pk_bf16_f32 v52, v168, v169\n\t"
      "v_cvt_pk_bf16_f32 v53, v170, v171\n\t"
      "v_cvt_pk_bf16_f32 v54, v172, v173\n\t"
      "v_cvt_pk_bf16_f32 v55, v174, v175\n\t"
      "v_pk_max_i16 v48, v48, 0\n\t"
      "v_pk_max_i16 v49, v49, 0\n\t"
      "v_pk_max_i16 v50, v50, 0\n\t"
      "v_pk_max_i16 v51, v51, 0\n\t"
      "v_pk_max_i16 v52, v52, 0\n\t"
      "v_pk_max_i16 v53, v53, 0\n\t"
      "v_pk_max_i16 v54, v54, 0\n\t"
      "v_pk_max_i16 v55, v55, 0\n\t"
      "s_waitcnt vmcnt(7)\n\t"
      "s_add_u32 s78, s47, 262144\n\t"
      "s_mov_b64 exec, 1\n\t"
      "buffer_store_dwordx2 v[62:63], off, s[24:27], s78 sc1\n\t"
      "s_mov_b64 exec, -1\n\t"
      "buffer_store_dwordx4 v[48:51], v1, s[24:27], s83 offen sc1\n\t"
      "buffer_store_dwordx4 v[52:55], v1, s[24:27], s83 offen offset:1024 sc1\n\t"
      "v_max3_f32 v56, v128, v129, v130\n\t"
      "v_max3_f32 v56, v56, v131, v132\n\t"
      "v_max3_f32 v56, v56, v133, v134\n\t"
      "v_max3_f32 v56, v56, v135, v136\n\t"
      "v_max3_f32 v56, v56, v137, v138\n\t"
      "v_max3_f32 v56, v56, v139, v140\n\t"
      "v_max3_f32 v56, v56, v141, v142\n\t"
      "v_max_f32 v56, v56, v143\n\t"
      "v_mul_f32 v56, s38, v56\n\t"
      "v_pk_fma_f32 v[128:129], v[128:129], s[38:39], v[56:57] op_sel:[0,1,0] op_sel_hi:[1,1,0]\n\t"
      "s_add_u32 s60, s74, 2\n\t"
      "s_lshl_b32 s60, s60, 16\n\t"
      "s_add_u32 m0, s58, 16384\n\t"
      "s_add_u32 s60, s60, s73\n\t"
      "buffer_load_dwordx4 v2, s[16:19], s60 offen lds\n\t"
      "v_pk_fma_f32 v[130:131], v[130:131], s[38:39], v[56:57] op_sel:[0,1,0] op_sel_hi:[1,1,0]\n\t"
      "v_pk_fma_f32 v[132:133], v[132:133], s[38:39], v[56:57] op_sel:[0,1,0] op_sel_hi:[1,1,0]\n\t"
      "v_pk_fma_f32 v[134:135], v[134:135], s[38:39], v[56:57] op_sel:[0,1,0] op_sel_hi:[1,1,0]\n\t"
      "v_pk_fma_f32 v[136:137], v[136:137], s[38:39], v[56:57] op_sel:[0,1,0] op_sel_hi:[1,1,0]\n\t"
      "v_pk_fma_f32 v[138:139], v[138:139], s[38:39], v[56:57] op_sel:[0,1,0] op_sel_hi:[1,1,0]\n\t"
      "v_pk_fma_f32 v[140:141], v[140:141], s[38:39], v[56:57] op_sel:[0,1,0] op_sel_hi:[1,1,0]\n\t"
      "v_pk_fma_f32 v[142:143], v[142:143], s[38:39], v[56:57] op_sel:[0,1,0] op_sel_hi:[1,1,0]\n\t"
      "v_exp_f32 v128, v128\n\t"
      "v_exp_f32 v129, v129\n\t"
      "v_exp_f32 v130, v130\n\t"
      "buffer_load_dwordx4 v3, s[16:19], s60 offen offset:1024 lds\n\t"
      "v_exp_f32 v131, v131\n\t"
      "v_exp_f32 v132, v132\n\t"
      "v_pk_add_f32 v[56:57], v[128:129], v[130:131]\n\t"
      "v_exp_f32 v133, v133\n\t"
      "v_exp_f32 v134, v134\n\t"
      "v_pk_add_f32 v[56:57], v[56:57], v[132:133]\n\t"
      "v_exp_f32 v135, v135\n\t"
      "v_exp_f32 v136, v136\n\t"
      "v_pk_add_f32 v[56:57], v[56:57], v[134:135]\n\t"
      "v_exp_f32 v137, v137\n\t"
      "buffer_load_dwordx4 v4, s[16:19], s60 offen offset:2048 lds\n\t"
      "v_exp_f32 v138, v138\n\t"
      "v_pk_add_f32 v[56:57], v[56:57], v[136:137]\n\t"
      "v_exp_f32 v139, v139\n\t"
      "v_exp_f32 v140, v140\n\t"
      "v_pk_add_f32 v[56:57], v[56:57], v[138:139]\n\t"
      "v_exp_f32 v141, v141\n\t"
      "v_exp_f32 v142, v142\n\t"
      "v_pk_add_f32 v[56:57], v[56:57], v[140:141]\n\t"
      "v_exp_f32 v143, v143\n\t"
      "s_nop 0\n\t"
      "v_pk_add_f32 v[56:57], v[56:57], v[142:143]\n\t"
      "buffer_load_dwordx4 v5, s[16:19], s60 offen offset:3072 lds\n\t"
      "v_add_f32 v56, v56, v57\n\t"
      "v_rcp_f32 v56, v56\n\t"
      "s_nop 0\n\t"
      "v_pk_mul_f32 v[128:129], v[128:129], v[56:57] op_sel_hi:[1,0]\n\t"
      "v_pk_mul_f32 v[130:131], v[130:131], v[56:57] op_sel_hi:[1,0]\n\t"
      "v_pk_mul_f32 v[132:133], v[132:133], v[56:57] op_sel_hi:[1,0]\n\t"
      "v_pk_mul_f32 v[134:135], v[134:135], v[56:57] op_sel_hi:[1,0]\n\t"
      "v_pk_mul_f32 v[136:137], v[136:137], v[56:57] op_sel_hi:[1,0]\n\t"
      "v_pk_mul_f32 v[138:139], v[138:139], v[56:57] op_sel_hi:[1,0]\n\t"
      "v_pk_mul_f32 v[140:141], v[140:141], v[56:57] op_sel_hi:[1,0]\n\t"
      "v_pk_mul_f32 v[142:143], v[142:143], v[56:57] op_sel_hi:[1,0]\n\t"
      "s_add_u32 s60, s75, 0\n\t"
      "s_lshl_b32 s60, s60, 16\n\t"
      "s_add_u32 m0, s58, 32768\n\t"
      "s_add_u32 s60, s60, s73\n\t"
      "buffer_load_dwordx4 v2, s[16:19], s60 offen lds\n\t"
      "v_cvt_pk_bf16_f32 v152, v128, v129\n\t"
      "v_cvt_pk_bf16_f32 v153, v130, v131\n\t"
      "v_cvt_pk_bf16_f32 v154, v132, v133\n\t"
      "v_cvt_pk_bf16_f32 v155, v134, v135\n\t"
      "v_cvt_pk_bf16_f32 v156, v136, v137\n\t"
      "v_cvt_pk_bf16_f32 v157, v138, v139\n\t"
      "v_cvt_pk_bf16_f32 v158, v140, v141\n\t"
      "v_cvt_pk_bf16_f32 v159, v142, v143\n\t"
      "buffer_load_dwordx4 v3, s[16:19], s60 offen offset:1024 lds\n\t"
      "s_waitcnt vmcnt(10)\n\t"
      "v_add_f32 v6, v6, v7\n\t"
      "v_add_f32 v8, v8, v9\n\t"
      "v_add_f32 v6, v6, v8\n\t"
      "v_mov_b32 v7, v6\n\t"
      "s_nop 1\n\t"
      "v_permlane32_swap_b32 v6, v7\n\t"
      "v_add_f32 v6, v6, v7\n\t"
      "v_mul_f32 v6, s38, v6\n\t"
      "v_exp_f32 v6, v6\n\t"
      "s_nop 0\n\t"
      "v_add_f32 v6, 1.0, v6\n\t"
      "v_rcp_f32 v60, v6\n\t"
      "buffer_load_dwordx4 v4, s[16:19], s60 offen offset:2048 lds\n\t"
      "s_waitcnt vmcnt(10)\n\t"
      "v_add_f32 v204, v204, v205\n\t"
      "v_add_f32 v206, v206, v207\n\t"
      "v_add_f32 v204, v204, v206\n\t"
      "v_mov_b32 v205, v204\n\t"
      "s_nop 1\n\t"
      "v_permlane32_swap_b32 v204, v205\n\t"
      "v_add_f32 v204, v204, v205\n\t"
      "v_mul_f32 v204, s38, v204\n\t"
      "v_exp_f32 v204, v204\n\t"
      "s_nop 0\n\t"
      "v_add_f32 v204, 1.0, v204\n\t"
      "v_rcp_f32 v207, v204\n\t"
      "buffer_load_dwordx4 v5, s[16:19], s60 offen offset:3072 lds\n\t"
      "v_max3_f32 v56, v208, v209, v210\n\t"
      "v_max3_f32 v56, v56, v211, v212\n\t"
      "v_max3_f32 v56, v56, v213, v214\n\t"
      "v_max3_f32 v56, v56, v215, v216\n\t"
      "v_max3_f32 v56, v56, v217, v218\n\t"
      "v_max3_f32 v56, v56, v219, v220\n\t"
      "v_max3_f32 v56, v56, v221, v222\n\t"
      "v_max_f32 v56, v56, v223\n\t"
      "v_mul_f32 v56, s38, v56\n\t"
      "v_pk_fma_f32 v[208:209], v[208:209], s[38:39], v[56:57] op_sel:[0,1,0] op_sel_hi:[1,1,0]\n\t"
      "s_add_u32 s60, s75, 1\n\t"
      "s_lshl_b32 s60, s60, 16\n\t"
      "s_add_u32 m0, s58, 49152\n\t"
      "s_add_u32 s60, s60, s73\n\t"
      "buffer_load_dwordx4 v2, s[16:19], s60 offen lds\n\t"
      "v_pk_fma_f32 v[210:211], v[210:211], s[38:39], v[56:57] op_sel:[0,1,0] op_sel_hi:[1,1,0]\n\t"
      "v_pk_fma_f32 v[212:213], v[212:213], s[38:39], v[56:57] op_sel:[0,1,0] op_sel_hi:[1,1,0]\n\t"
      "v_pk_fma_f32 v[214:215], v[214:215], s[38:39], v[56:57] op_sel:[0,1,0] op_sel_hi:[1,1,0]\n\t"
      "v_pk_fma_f32 v[216:217], v[216:217], s[38:39], v[56:57] op_sel:[0,1,0] op_sel_hi:[1,1,0]\n\t"
      "v_pk_fma_f32 v[218:219], v[218:219], s[38:39], v[56:57] op_sel:[0,1,0] op_sel_hi:[1,1,0]\n\t"
      "v_pk_fma_f32 v[220:221], v[220:221], s[38:39], v[56:57] op_sel:[0,1,0] op_sel_hi:[1,1,0]\n\t"
      "v_pk_fma_f32 v[222:223], v[222:223], s[38:39], v[56:57] op_sel:[0,1,0] op_sel_hi:[1,1,0]\n\t"
      "v_exp_f32 v208, v208\n\t"
      "v_exp_f32 v209, v209\n\t"
      "v_exp_f32 v210, v210\n\t"
      "buffer_load_dwordx4 v3, s[16:19], s60 offen offset:1024 lds\n\t"
      "v_exp_f32 v211, v211\n\t"
      "v_exp_f32 v212, v212\n\t"
      "v_pk_add_f32 v[56:57], v[208:209], v[210:211]\n\t"
      "v_exp_f32 v213, v213\n\t"
      "v_exp_f32 v214, v214\n\t"
      "v_pk_add_f32 v[56:57], v[56:57], v[212:213]\n\t"
      "v_exp_f32 v215, v215\n\t"
      "v_exp_f32 v216, v216\n\t"
      "v_pk_add_f32 v[56:57], v[56:57], v[214:215]\n\t"
      "v_exp_f32 v217, v217\n\t"
      "buffer_load_dwordx4 v4, s[16:19], s60 offen offset:2048 lds\n\t"
      "v_exp_f32 v218, v218\n\t"
      "v_pk_add_f32 v[56:57], v[56:57], v[216:217]\n\t"
      "v_exp_f32 v219, v219\n\t"
      "v_exp_f32 v220, v220\n\t"
      "v_pk_add_f32 v[56:57], v[56:57], v[218:219]\n\t"
      "v_exp_f32 v221, v221\n\t"
      "v_exp_f32 v222, v222\n\t"
      "v_pk_add_f32 v[56:57], v[56:57], v[220:221]\n\t"
      "v_exp_f32 v223, v223\n\t"
      "s_nop 0\n\t"
      "v_pk_add_f32 v[56:57], v[56:57], v[222:223]\n\t"
      "buffer_load_dwordx4 v5, s[16:19], s60 offen offset:3072 lds\n\t"
      "v_add_f32 v56, v56, v57\n\t"
      "v_rcp_f32 v56, v56\n\t"
      "s_nop 0\n\t"
      "v_pk_mul_f32 v[208:209], v[208:209], v[56:57] op_sel_hi:[1,0]\n\t"
      "v_pk_mul_f32 v[210:211], v[210:211], v[56:57] op_sel_hi:[1,0]\n\t"
      "v_pk_mul_f32 v[212:213], v[212:213], v[56:57] op_sel_hi:[1,0]\n\t"
      "v_pk_mul_f32 v[214:215], v[214:215], v[56:57] op_sel_hi:[1,0]\n\t"
      "v_pk_mul_f32 v[216:217], v[216:217], v[56:57] op_sel_hi:[1,0]\n\t"
      "v_pk_mul_f32 v[218:219], v[218:219], v[56:57] op_sel_hi:[1,0]\n\t"
      "v_pk_mul_f32 v[220:221], v[220:221], v[56:57] op_sel_hi:[1,0]\n\t"
      "v_pk_mul_f32 v[222:223], v[222:223], v[56:57] op_sel_hi:[1,0]\n\t"
      "v_cvt_pk_bf16_f32 v128, v208, v209\n\t"
      "v_cvt_pk_bf16_f32 v129, v210, v211\n\t"
      "v_cvt_pk_bf16_f32 v130, v212, v213\n\t"
      "v_cvt_pk_bf16_f32 v131, v214, v215\n\t"
      "v_cvt_pk_bf16_f32 v132, v216, v217\n\t"
      "v_cvt_pk_bf16_f32 v133, v218, v219\n\t"
      "v_cvt_pk_bf16_f32 v134, v220, v221\n\t"
      "v_cvt_pk_bf16_f32 v135, v222, v223\n\t"
      "s_waitcnt vmcnt(12)\n\t"
      "s_add_u32 s78, s47, 393216\n\t"
      "s_mov_b64 exec, 1\n\t"
      "buffer_store_dwordx2 v[62:63], off, s[24:27], s78 sc1\n\t"
      "s_mov_b64 exec, -1\n\t"
      "buffer_load_dwordx4 v[240:243], v13, s[24:27], s36 offen sc1\n\t"
      "buffer_load_dwordx4 v[96:99], v13, s[24:27], s36 offen offset:512 sc1\n\t"
      "buffer_load_dwordx4 v[244:247], v13, s[24:27], s36 offen offset:1024 sc1\n\t"
      "buffer_load_dwordx4 v[100:103], v13, s[24:27], s36 offen offset:1536 sc1\n\t"
      "buffer_load_dwordx4 v[248:251], v13, s[24:27], s37 offen sc1\n\t"
      "buffer_load_dwordx4 v[104:107], v13, s[24:27], s37 offen offset:512 sc1\n\t"
      "buffer_load_dwordx4 v[252:255], v13, s[24:27], s37 offen offset:1024 sc1\n\t"
      "buffer_load_dwordx4 v[108:111], v13, s[24:27], s37 offen offset:1536 sc1\n\t"
      "s_mov_b32 s78, 262144\n\t"
      "s_mov_b32 exec_lo, 15\n\t"
      "s_mov_b32 exec_hi, 0\n\t"
      "buffer_load_dwordx2 v[36:37], v38, s[24:27], s78 offen sc1\n\t"
      "s_mov_b64 exec, -1\n\t"
      "v_max3_f32 v56, v224, v225, v226\n\t"
      "v_max3_f32 v56, v56, v227, v228\n\t"
      "v_max3_f32 v56, v56, v229, v230\n\t"
      "v_max3_f32 v56, v56, v231, v232\n\t"
      "v_max3_f32 v56, v56, v233, v234\n\t"
      "v_max3_f32 v56, v56, v235, v236\n\t"
      "v_max3_f32 v56, v56, v237, v238\n\t"
      "v_max_f32 v56, v56, v239\n\t"
      "v_mul_f32 v56, s38, v56\n\t"
      "v_pk_fma_f32 v[224:225], v[224:225], s[38:39], v[56:57] op_sel:[0,1,0] op_sel_hi:[1,1,0]\n\t"
      "v_pk_fma_f32 v[226:227], v[226:227], s[38:39], v[56:57] op_sel:[0,1,0] op_sel_hi:[1,1,0]\n\t"
      "v_pk_fma_f32 v[228:229], v[228:229], s[38:39], v[56:57] op_sel:[0,1,0] op_sel_hi:[1,1,0]\n\t"
      "v_pk_fma_f32 v[230:231], v[230:231], s[38:39], v[56:57] op_sel:[0,1,0] op_sel_hi:[1,1,0]\n\t"
      "v_pk_fma_f32 v[232:233], v[232:233], s[38:39], v[56:57] op_sel:[0,1,0] op_sel_hi:[1,1,0]\n\t"
      "v_pk_fma_f32 v[234:235], v[234:235], s[38:39], v[56:57] op_sel:[0,1,0] op_sel_hi:[1,1,0]\n\t"
      "v_pk_fma_f32 v[236:237], v[236:237], s[38:39], v[56:57] op_sel:[0,1,0] op_sel_hi:[1,1,0]\n\t"
      "v_pk_fma_f32 v[238:239], v[238:239], s[38:39], v[56:57] op_sel:[0,1,0] op_sel_hi:[1,1,0]\n\t"
      "v_exp_f32 v224, v224\n\t"
      "v_exp_f32 v225, v225\n\t"
      "v_exp_f32 v226, v226\n\t"
      "v_exp_f32 v227, v227\n\t"
      "v_exp_f32 v228, v228\n\t"
      "v_pk_add_f32 v[56:57], v[224:225], v[226:227]\n\t"
      "v_exp_f32 v229, v229\n\t"
      "v_exp_f32 v230, v230\n\t"
      "v_pk_add_f32 v[56:57], v[56:57], v[228:229]\n\t"
      "v_exp_f32 v231, v231\n\t"
      "v_exp_f32 v232, v232\n\t"
      "v_pk_add_f32 v[56:57], v[56:57], v[230:231]\n\t"
      "v_exp_f32 v233, v233\n\t"
      "v_exp_f32 v234, v234\n\t"
      "v_pk_add_f32 v[56:57], v[56:57], v[232:233]\n\t"
      "v_exp_f32 v235, v235\n\t"
      "v_exp_f32 v236, v236\n\t"
      "v_pk_add_f32 v[56:57], v[56:57], v[234:235]\n\t"
      "v_exp_f32 v237, v237\n\t"
      "v_exp_f32 v238, v238\n\t"
      "v_pk_add_f32 v[56:57], v[56:57], v[236:237]\n\t"
      "v_exp_f32 v239, v239\n\t"
      "s_nop 0\n\t"
      "v_pk_add_f32 v[56:57], v[56:57], v[238:239]\n\t"
      "v_add_f32 v56, v56, v57\n\t"
      "v_rcp_f32 v56, v56\n\t"
      "s_nop 0\n\t"
      "v_pk_mul_f32 v[224:225], v[224:225], v[56:57] op_sel_hi:[1,0]\n\t"
      "v_pk_mul_f32 v[226:227], v[226:227], v[56:57] op_sel_hi:[1,0]\n\t"
      "v_pk_mul_f32 v[228:229], v[228:229], v[56:57] op_sel_hi:[1,0]\n\t"
      "v_pk_mul_f32 v[230:231], v[230:231], v[56:57] op_sel_hi:[1,0]\n\t"
      "v_pk_mul_f32 v[232:233], v[232:233], v[56:57] op_sel_hi:[1,0]\n\t"
      "v_pk_mul_f32 v[234:235], v[234:235], v[56:57] op_sel_hi:[1,0]\n\t"
      "v_pk_mul_f32 v[236:237], v[236:237], v[56:57] op_sel_hi:[1,0]\n\t"
      "v_pk_mul_f32 v[238:239], v[238:239], v[56:57] op_sel_hi:[1,0]\n\t"
      "v_cvt_pk_bf16_f32 v136, v224, v225\n\t"
      "v_cvt_pk_bf16_f32 v137, v226, v227\n\t"
      "v_cvt_pk_bf16_f32 v138, v228, v229\n\t"
      "v_cvt_pk_bf16_f32 v139, v230, v231\n\t"
      "v_cvt_pk_bf16_f32 v140, v232, v233\n\t"
      "v_cvt_pk_bf16_f32 v141, v234, v235\n\t"
      "v_cvt_pk_bf16_f32 v142, v236, v237\n\t"
      "v_cvt_pk_bf16_f32 v143, v238, v239\n\t"
      "s_mov_b32 s78, 393216\n\t"
      "s_mov_b32 exec_lo, 15\n\t"
      "s_mov_b32 exec_hi, 0\n\t"
      "buffer_load_dwordx2 v[202:203], v38, s[24:27], s78 offen sc1\n\t"
      "s_mov_b64 exec, -1\n\t"
      "s_waitcnt vmcnt(9)\n\t"
      "v_cmp_eq_u64 vcc, s[48:49], v[242:243]\n\t"
      "s_waitcnt vmcnt(7)\n\t"
      "v_cmp_eq_u64_e64 s[76:77], s[48:49], v[246:247]\n\t"
      "s_nop 0\n\t"
      "s_and_b64 vcc, vcc, s[76:77]\n\t"
      "s_waitcnt vmcnt(5)\n\t"
      "v_cmp_eq_u64_e64 s[76:77], s[48:49], v[250:251]\n\t"
      "s_nop 0\n\t"
      "s_and_b64 vcc, vcc, s[76:77]\n\t"
      "s_waitcnt vmcnt(3)\n\t"
      "v_cmp_eq_u64_e64 s[76:77], s[48:49], v[254:255]\n\t"
      "s_nop 0\n\t"
      "s_and_b64 vcc, vcc, s[76:77]\n\t"
      "v_cmp_eq_u64_e64 s[76:77], s[48:49], v[98:99]\n\t"
      "s_nop 0\n\t"
      "s_and_b64 vcc, vcc, s[76:77]\n\t"
      "v_cmp_eq_u64_e64 s[76:77], s[48:49], v[102:103]\n\t"
      "s_nop 0\n\t"
      "s_and_b64 vcc, vcc, s[76:77]\n\t"
      "v_cmp_eq_u64_e64 s[76:77], s[48:49], v[106:107]\n\t"
      "s_nop 0\n\t"
      "s_and_b64 vcc, vcc, s[76:77]\n\t"
      "s_waitcnt vmcnt(2)\n\t"
      "v_cmp_eq_u64_e64 s[76:77], s[48:49], v[110:111]\n\t"
      "s_nop 0\n\t"
      "s_and_b64 vcc, vcc, s[76:77]\n\t"
      "s_cmp_eq_u64 vcc, exec\n\t"
      "s_cbranch_scc1 .Lskip_34_%=\n\t"
      "s_mov_b32 s79, 32768\n\t"
      ".Lspin_35_%=:\n\t"
      "s_sleep 2\n\t"
      "buffer_load_dwordx4 v[240:243], v13, s[24:27], s36 offen sc1\n\t"
      "buffer_load_dwordx4 v[96:99], v13, s[24:27], s36 offen offset:512 sc1\n\t"
      "buffer_load_dwordx4 v[244:247], v13, s[24:27], s36 offen offset:1024 sc1\n\t"
      "buffer_load_dwordx4 v[100:103], v13, s[24:27], s36 offen offset:1536 sc1\n\t"
      "buffer_load_dwordx4 v[248:251], v13, s[24:27], s37 offen sc1\n\t"
      "buffer_load_dwordx4 v[104:107], v13, s[24:27], s37 offen offset:512 sc1\n\t"
      "buffer_load_dwordx4 v[252:255], v13, s[24:27], s37 offen offset:1024 sc1\n\t"
      "buffer_load_dwordx4 v[108:111], v13, s[24:27], s37 offen offset:1536 sc1\n\t"
      "s_waitcnt vmcnt(0)\n\t"
      "v_cmp_eq_u64 vcc, s[48:49], v[242:243]\n\t"
      "v_cmp_eq_u64_e64 s[76:77], s[48:49], v[246:247]\n\t"
      "s_nop 0\n\t"
      "s_and_b64 vcc, vcc, s[76:77]\n\t"
      "v_cmp_eq_u64_e64 s[76:77], s[48:49], v[250:251]\n\t"
      "s_nop 0\n\t"
      "s_and_b64 vcc, vcc, s[76:77]\n\t"
      "v_cmp_eq_u64_e64 s[76:77], s[48:49], v[254:255]\n\t"
      "s_nop 0\n\t"
      "s_and_b64 vcc, vcc, s[76:77]\n\t"
      "v_cmp_eq_u64_e64 s[76:77], s[48:49], v[98:99]\n\t"
      "s_nop 0\n\t"
      "s_and_b64 vcc, vcc, s[76:77]\n\t"
      "v_cmp_eq_u64_e64 s[76:77], s[48:49], v[102:103]\n\t"
      "s_nop 0\n\t"
      "s_and_b64 vcc, vcc, s[76:77]\n\t"
      "v_cmp_eq_u64_e64 s[76:77], s[48:49], v[106:107]\n\t"
      "s_nop 0\n\t"
      "s_and_b64 vcc, vcc, s[76:77]\n\t"
      "v_cmp_eq_u64_e64 s[76:77], s[48:49], v[110:111]\n\t"
      "s_nop 0\n\t"
      "s_and_b64 vcc, vcc, s[76:77]\n\t"
      "s_cmp_eq_u64 vcc, exec\n\t"
      "s_cbranch_scc1 .Lskip_34_%=\n\t"
      "s_sub_u32 s79, s79, 1\n\t"
      "s_cmp_lg_u32 s79, 0\n\t"
      "s_cbranch_scc1 .Lspin_35_%=\n\t"
      ".Lskip_34_%=:\n\t"
      "v_mov_b32 v242, v96\n\t"
      "v_mov_b32 v243, v97\n\t"
      "v_mov_b32 v246, v100\n\t"
      "v_mov_b32 v247, v101\n\t"
      "v_mov_b32 v250, v104\n\t"
      "v_mov_b32 v251, v105\n\t"
      "v_mov_b32 v254, v108\n\t"
      "v_mov_b32 v255, v109\n\t"
      "v_mfma_f32_32x32x16_bf16 v[160:175], v[240:243], v[144:147], 0\n\t"
      "v_mfma_f32_32x32x16_bf16 v[176:191], v[248:251], v[152:155], 0\n\t"
      "v_mfma_f32_32x32x16_bf16 v[64:79], v[240:243], v[128:131], 0\n\t"
      "v_mfma_f32_32x32x16_bf16 v[80:95], v[248:251], v[136:139], 0\n\t"
      "v_mfma_f32_32x32x16_bf16 v[160:175], v[244:247], v[148:151], v[160:175]\n\t"
      "v_mfma_f32_32x32x16_bf16 v[176:191], v[252:255], v[156:159], v[176:191]\n\t"
      "v_mfma_f32_32x32x16_bf16 v[64:79], v[244:247], v[132:135], v[64:79]\n\t"
      "v_mfma_f32_32x32x16_bf16 v[80:95], v[252:255], v[140:143], v[80:95]\n\t"
      "s_add_u32 s64, s88, s70\n\t"
      "s_add_u32 s65, s88, s71\n\t"
      "s_nop 6\n\t"
      "v_add_f32 v56, 0x358637bd, v168\n\t"
      "v_rcp_f32 v56, v56\n\t"
      "v_add_f32 v57, 0x358637bd, v184\n\t"
      "v_rcp_f32 v57, v57\n\t"
      "v_add_f32 v58, 0x358637bd, v72\n\t"
      "v_rcp_f32 v58, v58\n\t"
      "v_add_f32 v59, 0x358637bd, v88\n\t"
      "v_rcp_f32 v59, v59\n\t"
      "v_mul_f32 v56, v56, v60\n\t"
      "v_mul_f32 v160, v160, v56\n\t"
      "v_mul_f32 v161, v161, v56\n\t"
      "v_mul_f32 v162, v162, v56\n\t"
      "v_mul_f32 v163, v163, v56\n\t"
      "v_mul_f32 v164, v164, v56\n\t"
      "v_mul_f32 v165, v165, v56\n\t"
      "v_mul_f32 v166, v166, v56\n\t"
      "v_mul_f32 v167, v167, v56\n\t"
      "v_cvt_pk_bf16_f32 v48, v160, v161\n\t"
      "v_cvt_pk_bf16_f32 v49, v162, v163\n\t"
      "v_cvt_pk_bf16_f32 v50, v164, v165\n\t"
      "v_cvt_pk_bf16_f32 v51, v166, v167\n\t"
      "v_mul_f32 v57, v57, v60\n\t"
      "v_mul_f32 v176, v176, v57\n\t"
      "v_mul_f32 v177, v177, v57\n\t"
      "v_mul_f32 v178, v178, v57\n\t"
      "v_mul_f32 v179, v179, v57\n\t"
      "v_mul_f32 v180, v180, v57\n\t"
      "v_mul_f32 v181, v181, v57\n\t"
      "v_mul_f32 v182, v182, v57\n\t"
      "v_mul_f32 v183, v183, v57\n\t"
      "v_cvt_pk_bf16_f32 v52, v176, v177\n\t"
      "v_cvt_pk_bf16_f32 v53, v178, v179\n\t"
      "v_cvt_pk_bf16_f32 v54, v180, v181\n\t"
      "v_cvt_pk_bf16_f32 v55, v182, v183\n\t"
      "v_mul_f32 v58, v58, v207\n\t"
      "v_mul_f32 v64, v64, v58\n\t"
      "v_mul_f32 v65, v65, v58\n\t"
      "v_mul_f32 v66, v66, v58\n\t"
      "v_mul_f32 v67, v67, v58\n\t"
      "v_mul_f32 v68, v68, v58\n\t"
      "v_mul_f32 v69, v69, v58\n\t"
      "v_mul_f32 v70, v70, v58\n\t"
      "v_mul_f32 v71, v71, v58\n\t"
      "v_cvt_pk_bf16_f32 v192, v64, v65\n\t"
      "v_cvt_pk_bf16_f32 v193, v66, v67\n\t"
      "v_cvt_pk_bf16_f32 v194, v68, v69\n\t"
      "v_cvt_pk_bf16_f32 v195, v70, v71\n\t"
      "v_mul_f32 v59, v59, v207\n\t"
      "v_mul_f32 v80, v80, v59\n\t"
      "v_mul_f32 v81, v81, v59\n\t"
      "v_mul_f32 v82, v82, v59\n\t"
      "v_mul_f32 v83, v83, v59\n\t"
      "v_mul_f32 v84, v84, v59\n\t"
      "v_mul_f32 v85, v85, v59\n\t"
      "v_mul_f32 v86, v86, v59\n\t"
      "v_mul_f32 v87, v87, v59\n\t"
      "v_cvt_pk_bf16_f32 v196, v80, v81\n\t"
      "v_cvt_pk_bf16_f32 v197, v82, v83\n\t"
      "v_cvt_pk_bf16_f32 v198, v84, v85\n\t"
      "v_cvt_pk_bf16_f32 v199, v86, v87\n\t"
      "buffer_store_dwordx4 v[48:51], v1, s[24:27], s64 offen sc1\n\t"
      "buffer_store_dwordx4 v[52:55], v1, s[24:27], s64 offen offset:1024 sc1\n\t"
      "buffer_store_dwordx4 v[192:195], v1, s[24:27], s65 offen sc1\n\t"
      "buffer_store_dwordx4 v[196:199], v1, s[24:27], s65 offen offset:1024 sc1\n\t"
      "v_sub_f32 v60, 1.0, v60\n\t"
      "ds_read_b64_tr_b16 v[64:65], v18 offset:32768\n\t"
      "ds_read_b64_tr_b16 v[66:67], v18 offset:34816\n\t"
      "ds_read_b64_tr_b16 v[68:69], v19 offset:32768\n\t"
      "ds_read_b64_tr_b16 v[70:71], v19 offset:34816\n\t"
      "ds_read_b64_tr_b16 v[72:73], v20 offset:32768\n\t"
      "ds_read_b64_tr_b16 v[74:75], v20 offset:34816\n\t"
      "ds_read_b64_tr_b16 v[76:77], v21 offset:32768\n\t"
      "ds_read_b64_tr_b16 v[78:79], v21 offset:34816\n\t"
      "ds_read_b64_tr_b16 v[80:81], v18 offset:36864\n\t"
      "ds_read_b64_tr_b16 v[82:83], v18 offset:38912\n\t"
      "ds_read_b64_tr_b16 v[84:85], v19 offset:36864\n\t"
      "ds_read_b64_tr_b16 v[86:87], v19 offset:38912\n\t"
      "ds_read_b64_tr_b16 v[88:89], v20 offset:36864\n\t"
      "ds_read_b64_tr_b16 v[90:91], v20 offset:38912\n\t"
      "s_waitcnt lgkmcnt(12)\n\t"
      "v_mfma_f32_32x32x16_bf16 v[128:143], v[64:67], a[128:131], 0\n\t"
      "s_waitcnt vmcnt(5)\n\t"
      "v_cmp_eq_u64 vcc, s[48:49], v[36:37]\n\t"
      "s_nop 0\n\t"
      "s_and_b32 s78, vcc_lo, 15\n\t"
      "s_cmp_eq_u32 s78, 15\n\t"
      "s_cbranch_scc1 .Lskip_36_%=\n\t"
      "s_mov_b32 s79, 32768\n\t"
      ".Lspin_37_%=:\n\t"
      "s_sleep 1\n\t"
      "s_mov_b32 s78, 262144\n\t"
      "s_mov_b32 exec_lo, 15\n\t"
      "s_mov_b32 exec_hi, 0\n\t"
      "buffer_load_dwordx2 v[36:37], v38, s[24:27], s78 offen sc1\n\t"
      "s_mov_b64 exec, -1\n\t"
      "s_waitcnt vmcnt(0)\n\t"
      "v_cmp_eq_u64 vcc, s[48:49], v[36:37]\n\t"
      "s_nop 0\n\t"
      "s_and_b32 s78, vcc_lo, 15\n\t"
      "s_cmp_eq_u32 s78, 15\n\t"
      "s_cbranch_scc1 .Lskip_36_%=\n\t"
      "s_sub_u32 s79, s79, 1\n\t"
      "s_cmp_lg_u32 s79, 0\n\t"
      "s_cbranch_scc1 .Lspin_37_%=\n\t"
      ".Lskip_36_%=:\n\t"
      "s_add_u32 s62, s74, 1\n\t"
      "s_lshl_b32 s62, s62, 12\n\t"
      "s_add_u32 s62, s62, s72\n\t"
      "buffer_load_dwordx4 a[160:163], v1, s[24:27], s62 offen sc1\n\t"
      "ds_read_b64_tr_b16 v[92:93], v21 offset:36864\n\t"
      "ds_read_b64_tr_b16 v[94:95], v21 offset:38912\n\t"
      "s_waitcnt lgkmcnt(12)\n\t"
      "v_mfma_f32_32x32x16_bf16 v[144:159], v[68:71], a[128:131], 0\n\t"
      "ds_read_b64_tr_b16 v[96:97], v18 offset:40960\n\t"
      "ds_read_b64_tr_b16 v[98:99], v18 offset:43008\n\t"
      "s_waitcnt lgkmcnt(12)\n\t"
      "v_mfma_f32_32x32x16_bf16 v[160:175], v[72:75], a[128:131], 0\n\t"
      "buffer_load_dwordx4 a[164:167], v1, s[24:27], s62 offen offset:1024 sc1\n\t"
      "ds_read_b64_tr_b16 v[100:101], v19 offset:40960\n\t"
      "ds_read_b64_tr_b16 v[102:103], v19 offset:43008\n\t"
      "s_waitcnt lgkmcnt(12)\n\t"
      "v_mfma_f32_32x32x16_bf16 v[176:191], v[76:79], a[128:131], 0\n\t"
      "ds_read_b64_tr_b16 v[104:105], v20 offset:40960\n\t"
      "ds_read_b64_tr_b16 v[106:107], v20 offset:43008\n\t"
      "s_waitcnt lgkmcnt(12)\n\t"
      "v_mfma_f32_32x32x16_bf16 v[128:143], v[80:83], a[132:135], v[128:143]\n\t"
      "buffer_load_dwordx4 a[168:171], v1, s[24:27], s62 offen offset:2048 sc1\n\t"
      "ds_read_b64_tr_b16 v[108:109], v21 offset:40960\n\t"
      "ds_read_b64_tr_b16 v[110:111], v21 offset:43008\n\t"
      "s_waitcnt lgkmcnt(12)\n\t"
      "v_mfma_f32_32x32x16_bf16 v[144:159], v[84:87], a[132:135], v[144:159]\n\t"
      "ds_read_b64_tr_b16 v[112:113], v18 offset:45056\n\t"
      "ds_read_b64_tr_b16 v[114:115], v18 offset:47104\n\t"
      "s_waitcnt lgkmcnt(12)\n\t"
      "v_mfma_f32_32x32x16_bf16 v[160:175], v[88:91], a[132:135], v[160:175]\n\t"
      "buffer_load_dwordx4 a[172:175], v1, s[24:27], s62 offen offset:3072 sc1\n\t"
      "ds_read_b64_tr_b16 v[116:117], v19 offset:45056\n\t"
      "ds_read_b64_tr_b16 v[118:119], v19 offset:47104\n\t"
      "s_waitcnt lgkmcnt(12)\n\t"
      "v_mfma_f32_32x32x16_bf16 v[176:191], v[92:95], a[132:135], v[176:191]\n\t"
      "ds_read_b64_tr_b16 v[120:121], v20 offset:45056\n\t"
      "ds_read_b64_tr_b16 v[122:123], v20 offset:47104\n\t"
      "s_waitcnt lgkmcnt(12)\n\t"
      "v_mfma_f32_32x32x16_bf16 v[128:143], v[96:99], a[136:139], v[128:143]\n\t"
      "s_add_u32 s62, s74, 4\n\t"
      "s_lshl_b32 s62, s62, 12\n\t"
      "s_add_u32 s62, s62, s72\n\t"
      "buffer_load_dwordx4 a[176:179], v1, s[24:27], s62 offen sc1\n\t"
      "ds_read_b64_tr_b16 v[124:125], v21 offset:45056\n\t"
      "ds_read_b64_tr_b16 v[126:127], v21 offset:47104\n\t"
      "s_waitcnt lgkmcnt(12)\n\t"
      "v_mfma_f32_32x32x16_bf16 v[144:159], v[100:103], a[136:139], v[144:159]\n\t"
      "s_waitcnt vmcnt(5)\n\t"
      "s_add_u32 s78, s47, 524288\n\t"
      "s_mov_b64 exec, 1\n\t"
      "buffer_store_dwordx2 v[62:63], off, s[24:27], s78 sc1\n\t"
      "s_mov_b64 exec, -1\n\t"
      "ds_read_b64_tr_b16 v[64:65], v18 offset:49152\n\t"
      "ds_read_b64_tr_b16 v[66:67], v18 offset:51200\n\t"
      "s_waitcnt lgkmcnt(12)\n\t"
      "v_mfma_f32_32x32x16_bf16 v[160:175], v[104:107], a[136:139], v[160:175]\n\t"
      "buffer_load_dwordx4 a[180:183], v1, s[24:27], s62 offen offset:1024 sc1\n\t"
      "ds_read_b64_tr_b16 v[68:69], v19 offset:49152\n\t"
      "ds_read_b64_tr_b16 v[70:71], v19 offset:51200\n\t"
      "s_waitcnt lgkmcnt(12)\n\t"
      "v_mfma_f32_32x32x16_bf16 v[176:191], v[108:111], a[136:139], v[176:191]\n\t"
      "ds_read_b64_tr_b16 v[72:73], v20 offset:49152\n\t"
      "ds_read_b64_tr_b16 v[74:75], v20 offset:51200\n\t"
      "s_waitcnt lgkmcnt(12)\n\t"
      "v_mfma_f32_32x32x16_bf16 v[128:143], v[112:115], a[140:143], v[128:143]\n\t"
      "buffer_load_dwordx4 a[184:187], v1, s[24:27], s62 offen offset:2048 sc1\n\t"
      "ds_read_b64_tr_b16 v[76:77], v21 offset:49152\n\t"
      "ds_read_b64_tr_b16 v[78:79], v21 offset:51200\n\t"
      "s_waitcnt lgkmcnt(12)\n\t"
      "v_mfma_f32_32x32x16_bf16 v[144:159], v[116:119], a[140:143], v[144:159]\n\t"
      "ds_read_b64_tr_b16 v[80:81], v18 offset:53248\n\t"
      "ds_read_b64_tr_b16 v[82:83], v18 offset:55296\n\t"
      "s_waitcnt lgkmcnt(12)\n\t"
      "v_mfma_f32_32x32x16_bf16 v[160:175], v[120:123], a[140:143], v[160:175]\n\t"
      "buffer_load_dwordx4 a[188:191], v1, s[24:27], s62 offen offset:3072 sc1\n\t"
      "ds_read_b64_tr_b16 v[84:85], v19 offset:53248\n\t"
      "ds_read_b64_tr_b16 v[86:87], v19 offset:55296\n\t"
      "s_waitcnt lgkmcnt(12)\n\t"
      "v_mfma_f32_32x32x16_bf16 v[176:191], v[124:127], a[140:143], v[176:191]\n\t"
      "ds_read_b64_tr_b16 v[88:89], v20 offset:53248\n\t"
      "ds_read_b64_tr_b16 v[90:91], v20 offset:55296\n\t"
      "s_waitcnt lgkmcnt(12)\n\t"
      "v_mfma_f32_32x32x16_bf16 v[128:143], v[64:67], a[144:147], v[128:143]\n\t"
      "s_mov_b32 s78, 393216\n\t"
      "s_mov_b32 exec_lo, 240\n\t"
      "s_mov_b32 exec_hi, 0\n\t"
      "buffer_load_dwordx2 v[36:37], v38, s[24:27], s78 offen sc1\n\t"
      "s_mov_b64 exec, -1\n\t"
      "ds_read_b64_tr_b16 v[92:93], v21 offset:53248\n\t"
      "ds_read_b64_tr_b16 v[94:95], v21 offset:55296\n\t"
      "s_waitcnt lgkmcnt(12)\n\t"
      "v_mfma_f32_32x32x16_bf16 v[144:159], v[68:71], a[144:147], v[144:159]\n\t"
      "ds_read_b64_tr_b16 v[96:97], v18 offset:57344\n\t"
      "ds_read_b64_tr_b16 v[98:99], v18 offset:59392\n\t"
      "s_waitcnt lgkmcnt(12)\n\t"
      "v_mfma_f32_32x32x16_bf16 v[160:175], v[72:75], a[144:147], v[160:175]\n\t"
      "v_cmp_eq_u64 vcc, s[48:49], v[202:203]\n\t"
      "s_nop 0\n\t"
      "s_and_b32 s78, vcc_lo, 15\n\t"
      "s_cmp_eq_u32 s78, 15\n\t"
      "s_cbranch_scc1 .Lskip_38_%=\n\t"
      "s_mov_b32 s79, 32768\n\t"
      ".Lspin_39_%=:\n\t"
      "s_sleep 1\n\t"
      "s_mov_b32 s78, 393216\n\t"
      "s_mov_b32 exec_lo, 15\n\t"
      "s_mov_b32 exec_hi, 0\n\t"
      "buffer_load_dwordx2 v[202:203], v38, s[24:27], s78 offen sc1\n\t"
      "s_mov_b64 exec, -1\n\t"
      "s_waitcnt vmcnt(0)\n\t"
      "v_cmp_eq_u64 vcc, s[48:49], v[202:203]\n\t"
      "s_nop 0\n\t"
      "s_and_b32 s78, vcc_lo, 15\n\t"
      "s_cmp_eq_u32 s78, 15\n\t"
      "s_cbranch_scc1 .Lskip_38_%=\n\t"
      "s_sub_u32 s79, s79, 1\n\t"
      "s_cmp_lg_u32 s79, 0\n\t"
      "s_cbranch_scc1 .Lspin_39_%=\n\t"
      ".Lskip_38_%=:\n\t"
      "s_add_u32 s62, s74, 2\n\t"
      "s_lshl_b32 s62, s62, 12\n\t"
      "s_add_u32 s62, s62, s72\n\t"
      "buffer_load_dwordx4 a[192:195], v1, s[24:27], s62 offen sc1\n\t"
      "ds_read_b64_tr_b16 v[100:101], v19 offset:57344\n\t"
      "ds_read_b64_tr_b16 v[102:103], v19 offset:59392\n\t"
      "s_waitcnt lgkmcnt(12)\n\t"
      "v_mfma_f32_32x32x16_bf16 v[176:191], v[76:79], a[144:147], v[176:191]\n\t"
      "ds_read_b64_tr_b16 v[104:105], v20 offset:57344\n\t"
      "ds_read_b64_tr_b16 v[106:107], v20 offset:59392\n\t"
      "s_waitcnt lgkmcnt(12)\n\t"
      "v_mfma_f32_32x32x16_bf16 v[128:143], v[80:83], a[148:151], v[128:143]\n\t"
      "buffer_load_dwordx4 a[196:199], v1, s[24:27], s62 offen offset:1024 sc1\n\t"
      "ds_read_b64_tr_b16 v[108:109], v21 offset:57344\n\t"
      "ds_read_b64_tr_b16 v[110:111], v21 offset:59392\n\t"
      "s_waitcnt lgkmcnt(12)\n\t"
      "v_mfma_f32_32x32x16_bf16 v[144:159], v[84:87], a[148:151], v[144:159]\n\t"
      "ds_read_b64_tr_b16 v[112:113], v18 offset:61440\n\t"
      "ds_read_b64_tr_b16 v[114:115], v18 offset:63488\n\t"
      "s_waitcnt lgkmcnt(12)\n\t"
      "v_mfma_f32_32x32x16_bf16 v[160:175], v[88:91], a[148:151], v[160:175]\n\t"
      "buffer_load_dwordx4 a[200:203], v1, s[24:27], s62 offen offset:2048 sc1\n\t"
      "ds_read_b64_tr_b16 v[116:117], v19 offset:61440\n\t"
      "ds_read_b64_tr_b16 v[118:119], v19 offset:63488\n\t"
      "s_waitcnt lgkmcnt(12)\n\t"
      "v_mfma_f32_32x32x16_bf16 v[176:191], v[92:95], a[148:151], v[176:191]\n\t"
      "ds_read_b64_tr_b16 v[120:121], v20 offset:61440\n\t"
      "ds_read_b64_tr_b16 v[122:123], v20 offset:63488\n\t"
      "s_waitcnt lgkmcnt(12)\n\t"
      "v_mfma_f32_32x32x16_bf16 v[128:143], v[96:99], a[152:155], v[128:143]\n\t"
      "buffer_load_dwordx4 a[204:207], v1, s[24:27], s62 offen offset:3072 sc1\n\t"
      "ds_read_b64_tr_b16 v[124:125], v21 offset:61440\n\t"
      "ds_read_b64_tr_b16 v[126:127], v21 offset:63488\n\t"
      "s_waitcnt lgkmcnt(12)\n\t"
      "v_mfma_f32_32x32x16_bf16 v[144:159], v[100:103], a[152:155], v[144:159]\n\t"
      "v_and_b32 v56, 1, v0\n\t"
      "v_lshlrev_b32 v56, 4, v56\n\t"
      "v_mov_b32 v57, 0x3f80\n\t"
      "ds_read_b64_tr_b16 v[64:65], v23 offset:0\n\t"
      "ds_read_b64_tr_b16 v[66:67], v23 offset:2048\n\t"
      "s_waitcnt lgkmcnt(12)\n\t"
      "v_mfma_f32_32x32x16_bf16 v[160:175], v[104:107], a[152:155], v[160:175]\n\t"
      "s_waitcnt vmcnt(4)\n\t"
      "v_cmp_eq_u64 vcc, s[48:49], v[36:37]\n\t"
      "s_nop 0\n\t"
      "s_and_b32 s78, vcc_lo, 240\n\t"
      "s_cmp_eq_u32 s78, 240\n\t"
      "s_cbranch_scc1 .Lskip_40_%=\n\t"
      "s_mov_b32 s79, 32768\n\t"
      ".Lspin_41_%=:\n\t"
      "s_sleep 1\n\t"
      "s_mov_b32 s78, 393216\n\t"
      "s_mov_b32 exec_lo, 240\n\t"
      "s_mov_b32 exec_hi, 0\n\t"
      "buffer_load_dwordx2 v[36:37], v38, s[24:27], s78 offen sc1\n\t"
      "s_mov_b64 exec, -1\n\t"
      "s_waitcnt vmcnt(0)\n\t"
      "v_cmp_eq_u64 vcc, s[48:49], v[36:37]\n\t"
      "s_nop 0\n\t"
      "s_and_b32 s78, vcc_lo, 240\n\t"
      "s_cmp_eq_u32 s78, 240\n\t"
      "s_cbranch_scc1 .Lskip_40_%=\n\t"
      "s_sub_u32 s79, s79, 1\n\t"
      "s_cmp_lg_u32 s79, 0\n\t"
      "s_cbranch_scc1 .Lspin_41_%=\n\t"
      ".Lskip_40_%=:\n\t"
      "s_add_u32 s62, s75, 0\n\t"
      "s_lshl_b32 s62, s62, 12\n\t"
      "s_add_u32 s62, s62, s72\n\t"
      "buffer_load_dwordx4 a[208:211], v1, s[24:27], s62 offen sc1\n\t"
      "v_lshlrev_b32 v56, v56, v57\n\t"
      "v_bfe_u32 v57, v0, 1, 1\n\t"
      "v_bfe_u32 v58, v0, 3, 2\n\t"
      "ds_read_b64_tr_b16 v[68:69], v24 offset:0\n\t"
      "ds_read_b64_tr_b16 v[70:71], v24 offset:2048\n\t"
      "s_waitcnt lgkmcnt(12)\n\t"
      "v_mfma_f32_32x32x16_bf16 v[176:191], v[108:111], a[152:155], v[176:191]\n\t"
      "v_lshl_add_u32 v57, v58, 1, v57\n\t"
      "v_bfe_u32 v58, v0, 2, 1\n\t"
      "v_lshrrev_b32 v59, 5, v0\n\t"
      "ds_read_b64_tr_b16 v[72:73], v25 offset:0\n\t"
      "ds_read_b64_tr_b16 v[74:75], v25 offset:2048\n\t"
      "s_waitcnt lgkmcnt(12)\n\t"
      "v_mfma_f32_32x32x16_bf16 v[128:143], v[112:115], a[156:159], v[128:143]\n\t"
      "buffer_load_dwordx4 a[212:215], v1, s[24:27], s62 offen offset:1024 sc1\n\t"
      "v_xor_b32 v58, v58, v59\n\t"
      "v_lshl_add_u32 v57, v58, 3, v57\n\t"
      "v_cmp_eq_u32 vcc, 0, v57\n\t"
      "s_nop 1\n\t"
      "v_cndmask_b32 v40, 0, v56, vcc\n\t"
      "ds_read_b64_tr_b16 v[76:77], v23 offset:4096\n\t"
      "ds_read_b64_tr_b16 v[78:79], v23 offset:6144\n\t"
      "s_waitcnt lgkmcnt(12)\n\t"
      "v_mfma_f32_32x32x16_bf16 v[144:159], v[116:119], a[156:159], v[144:159]\n\t"
      "v_cmp_eq_u32 vcc, 1, v57\n\t"
      "s_nop 1\n\t"
      "v_cndmask_b32 v41, 0, v56, vcc\n\t"
      "v_cmp_eq_u32 vcc, 2, v57\n\t"
      "s_nop 1\n\t"
      "v_cndmask_b32 v42, 0, v56, vcc\n\t"
      "v_cmp_eq_u32 vcc, 3, v57\n\t"
      "s_nop 1\n\t"
      "v_cndmask_b32 v43, 0, v56, vcc\n\t"
      "ds_read_b64_tr_b16 v[80:81], v24 offset:4096\n\t"
      "ds_read_b64_tr_b16 v[82:83], v24 offset:6144\n\t"
      "s_waitcnt lgkmcnt(12)\n\t"
      "v_mfma_f32_32x32x16_bf16 v[160:175], v[120:123], a[156:159], v[160:175]\n\t"
      "buffer_load_dwordx4 a[216:219], v1, s[24:27], s62 offen offset:2048 sc1\n\t"
      "v_cmp_eq_u32 vcc, 4, v57\n\t"
      "s_nop 1\n\t"
      "v_cndmask_b32 v44, 0, v56, vcc\n\t"
      "v_cmp_eq_u32 vcc, 5, v57\n\t"
      "s_nop 1\n\t"
      "v_cndmask_b32 v45, 0, v56, vcc\n\t"
      "v_cmp_eq_u32 vcc, 6, v57\n\t"
      "s_nop 1\n\t"
      "v_cndmask_b32 v46, 0, v56, vcc\n\t"
      "ds_read_b64_tr_b16 v[84:85], v25 offset:4096\n\t"
      "ds_read_b64_tr_b16 v[86:87], v25 offset:6144\n\t"
      "s_waitcnt lgkmcnt(12)\n\t"
      "v_mfma_f32_32x32x16_bf16 v[176:191], v[124:127], a[156:159], v[176:191]\n\t"
      "v_cmp_eq_u32 vcc, 7, v57\n\t"
      "s_nop 1\n\t"
      "v_cndmask_b32 v47, 0, v56, vcc\n\t"
      "ds_read_b64_tr_b16 v[88:89], v23 offset:8192\n\t"
      "ds_read_b64_tr_b16 v[90:91], v23 offset:10240\n\t"
      "s_waitcnt lgkmcnt(12)\n\t"
      "v_mfma_f32_32x32x16_bf16 v[144:159], v[64:67], a[160:163], v[144:159]\n\t"
      "buffer_load_dwordx4 a[220:223], v1, s[24:27], s62 offen offset:3072 sc1\n\t"
      "ds_read_b64_tr_b16 v[92:93], v24 offset:8192\n\t"
      "ds_read_b64_tr_b16 v[94:95], v24 offset:10240\n\t"
      "s_waitcnt lgkmcnt(12)\n\t"
      "v_mfma_f32_32x32x16_bf16 v[160:175], v[68:71], a[160:163], v[160:175]\n\t"
      "ds_read_b64_tr_b16 v[96:97], v25 offset:8192\n\t"
      "ds_read_b64_tr_b16 v[98:99], v25 offset:10240\n\t"
      "s_waitcnt lgkmcnt(12)\n\t"
      "v_mfma_f32_32x32x16_bf16 v[176:191], v[72:75], a[160:163], v[176:191]\n\t"
      "s_add_u32 s62, s75, 1\n\t"
      "s_lshl_b32 s62, s62, 12\n\t"
      "s_add_u32 s62, s62, s72\n\t"
      "buffer_load_dwordx4 a[224:227], v1, s[24:27], s62 offen sc1\n\t"
      "ds_read_b64_tr_b16 v[100:101], v23 offset:12288\n\t"
      "ds_read_b64_tr_b16 v[102:103], v23 offset:14336\n\t"
      "s_waitcnt lgkmcnt(12)\n\t"
      "v_mfma_f32_32x32x16_bf16 v[144:159], v[76:79], a[164:167], v[144:159]\n\t"
      "ds_read_b64_tr_b16 v[104:105], v24 offset:12288\n\t"
      "ds_read_b64_tr_b16 v[106:107], v24 offset:14336\n\t"
      "s_waitcnt lgkmcnt(12)\n\t"
      "v_mfma_f32_32x32x16_bf16 v[160:175], v[80:83], a[164:167], v[160:175]\n\t"
      "buffer_load_dwordx4 a[228:231], v1, s[24:27], s62 offen offset:1024 sc1\n\t"
      "ds_read_b64_tr_b16 v[108:109], v25 offset:12288\n\t"
      "ds_read_b64_tr_b16 v[110:111], v25 offset:14336\n\t"
      "s_waitcnt lgkmcnt(12)\n\t"
      "v_mfma_f32_32x32x16_bf16 v[176:191], v[84:87], a[164:167], v[176:191]\n\t"
      "ds_read_b64_tr_b16 v[112:113], v23 offset:16384\n\t"
      "ds_read_b64_tr_b16 v[114:115], v23 offset:18432\n\t"
      "s_waitcnt lgkmcnt(12)\n\t"
      "v_mfma_f32_32x32x16_bf16 v[144:159], v[88:91], a[168:171], v[144:159]\n\t"
      "buffer_load_dwordx4 a[232:235], v1, s[24:27], s62 offen offset:2048 sc1\n\t"
      "ds_read_b64_tr_b16 v[116:117], v24 offset:16384\n\t"
      "ds_read_b64_tr_b16 v[118:119], v24 offset:18432\n\t"
      "s_waitcnt lgkmcnt(12)\n\t"
      "v_mfma_f32_32x32x16_bf16 v[160:175], v[92:95], a[168:171], v[160:175]\n\t"
      "ds_read_b64_tr_b16 v[120:121], v25 offset:16384\n\t"
      "ds_read_b64_tr_b16 v[122:123], v25 offset:18432\n\t"
      "s_waitcnt lgkmcnt(12)\n\t"
      "v_mfma_f32_32x32x16_bf16 v[176:191], v[96:99], a[168:171], v[176:191]\n\t"
      "buffer_load_dwordx4 a[236:239], v1, s[24:27], s62 offen offset:3072 sc1\n\t"
      "ds_read_b64_tr_b16 v[124:125], v23 offset:20480\n\t"
      "ds_read_b64_tr_b16 v[126:127], v23 offset:22528\n\t"
      "s_waitcnt lgkmcnt(12)\n\t"
      "v_mfma_f32_32x32x16_bf16 v[144:159], v[100:103], a[172:175], v[144:159]\n\t"
      "ds_read_b64_tr_b16 v[64:65], v24 offset:20480\n\t"
      "ds_read_b64_tr_b16 v[66:67], v24 offset:22528\n\t"
      "s_waitcnt lgkmcnt(12)\n\t"
      "v_mfma_f32_32x32x16_bf16 v[160:175], v[104:107], a[172:175], v[160:175]\n\t"
      "s_add_u32 s60, s75, 0\n\t"
      "s_lshl_b32 s60, s60, 16\n\t"
      "s_add_u32 m0, s58, 4294950912\n\t"
      "s_add_u32 s60, s60, s73\n\t"
      "buffer_load_dwordx4 v2, s[20:23], s60 offen lds\n\t"
      "ds_read_b64_tr_b16 v[68:69], v25 offset:20480\n\t"
      "ds_read_b64_tr_b16 v[70:71], v25 offset:22528\n\t"
      "s_waitcnt lgkmcnt(12)\n\t"
      "v_mfma_f32_32x32x16_bf16 v[176:191], v[108:111], a[172:175], v[176:191]\n\t"
      "ds_read_b64_tr_b16 v[72:73], v23 offset:24576\n\t"
      "ds_read_b64_tr_b16 v[74:75], v23 offset:26624\n\t"
      "s_waitcnt lgkmcnt(12)\n\t"
      "v_mfma_f32_32x32x16_bf16 v[144:159], v[112:115], a[176:179], v[144:159]\n\t"
      "buffer_load_dwordx4 v3, s[20:23], s60 offen offset:1024 lds\n\t"
      "ds_read_b64_tr_b16 v[76:77], v24 offset:24576\n\t"
      "ds_read_b64_tr_b16 v[78:79], v24 offset:26624\n\t"
      "s_waitcnt lgkmcnt(12)\n\t"
      "v_mfma_f32_32x32x16_bf16 v[160:175], v[116:119], a[176:179], v[160:175]\n\t"
      "ds_read_b64_tr_b16 v[80:81], v25 offset:24576\n\t"
      "ds_read_b64_tr_b16 v[82:83], v25 offset:26624\n\t"
      "s_waitcnt lgkmcnt(12)\n\t"
      "v_mfma_f32_32x32x16_bf16 v[176:191], v[120:123], a[176:179], v[176:191]\n\t"
      "buffer_load_dwordx4 v4, s[20:23], s60 offen offset:2048 lds\n\t"
      "ds_read_b64_tr_b16 v[84:85], v23 offset:28672\n\t"
      "ds_read_b64_tr_b16 v[86:87], v23 offset:30720\n\t"
      "s_waitcnt lgkmcnt(12)\n\t"
      "v_mfma_f32_32x32x16_bf16 v[144:159], v[124:127], a[180:183], v[144:159]\n\t"
      "ds_read_b64_tr_b16 v[88:89], v24 offset:28672\n\t"
      "ds_read_b64_tr_b16 v[90:91], v24 offset:30720\n\t"
      "s_waitcnt lgkmcnt(12)\n\t"
      "v_mfma_f32_32x32x16_bf16 v[160:175], v[64:67], a[180:183], v[160:175]\n\t"
      "buffer_load_dwordx4 v5, s[20:23], s60 offen offset:3072 lds\n\t"
      "ds_read_b64_tr_b16 v[92:93], v25 offset:28672\n\t"
      "ds_read_b64_tr_b16 v[94:95], v25 offset:30720\n\t"
      "s_waitcnt lgkmcnt(12)\n\t"
      "v_mfma_f32_32x32x16_bf16 v[176:191], v[68:71], a[180:183], v[176:191]\n\t"
      "s_waitcnt lgkmcnt(10)\n\t"
      "v_mfma_f32_32x32x16_bf16 v[144:159], v[72:75], a[184:187], v[144:159]\n\t"
      "s_add_u32 s60, s75, 1\n\t"
      "s_lshl_b32 s60, s60, 16\n\t"
      "s_add_u32 m0, s58, 0\n\t"
      "s_add_u32 s60, s60, s73\n\t"
      "buffer_load_dwordx4 v2, s[20:23], s60 offen lds\n\t"
      "s_waitcnt lgkmcnt(0)\n\t"
      "s_barrier\n\t"
      "ds_read_b64_tr_b16 v[96:97], v15 offset:32768\n\t"
      "ds_read_b64_tr_b16 v[98:99], v15 offset:34816\n\t"
      "ds_read_b64_tr_b16 v[100:101], v16 offset:32768\n\t"
      "ds_read_b64_tr_b16 v[102:103], v16 offset:34816\n\t"
      "v_mfma_f32_32x32x16_bf16 v[160:175], v[76:79], a[184:187], v[160:175]\n\t"
      "ds_read_b64_tr_b16 v[104:105], v17 offset:32768\n\t"
      "ds_read_b64_tr_b16 v[106:107], v17 offset:34816\n\t"
      "v_mfma_f32_32x32x16_bf16 v[176:191], v[80:83], a[184:187], v[176:191]\n\t"
      "buffer_load_dwordx4 v3, s[20:23], s60 offen offset:1024 lds\n\t"
      "ds_read_b64_tr_b16 v[108:109], v15 offset:36864\n\t"
      "ds_read_b64_tr_b16 v[110:111], v15 offset:38912\n\t"
      "v_mfma_f32_32x32x16_bf16 v[144:159], v[84:87], a[188:191], v[144:159]\n\t"
      "ds_read_b64_tr_b16 v[112:113], v16 offset:36864\n\t"
      "ds_read_b64_tr_b16 v[114:115], v16 offset:38912\n\t"
      "v_mfma_f32_32x32x16_bf16 v[160:175], v[88:91], a[188:191], v[160:175]\n\t"
      "buffer_load_dwordx4 v4, s[20:23], s60 offen offset:2048 lds\n\t"
      "ds_read_b64_tr_b16 v[116:117], v17 offset:36864\n\t"
      "ds_read_b64_tr_b16 v[118:119], v17 offset:38912\n\t"
      "v_mfma_f32_32x32x16_bf16 v[176:191], v[92:95], a[188:191], v[176:191]\n\t"
      "ds_read_b64_tr_b16 v[120:121], v15 offset:40960\n\t"
      "ds_read_b64_tr_b16 v[122:123], v15 offset:43008\n\t"
      "s_waitcnt vmcnt(18) lgkmcnt(12)\n\t"
      "v_mfma_f32_32x32x16_bf16 v[144:159], v[96:99], a[192:195], v[144:159]\n\t"
      "buffer_load_dwordx4 v5, s[20:23], s60 offen offset:3072 lds\n\t"
      "ds_read_b64_tr_b16 v[124:125], v16 offset:40960\n\t"
      "ds_read_b64_tr_b16 v[126:127], v16 offset:43008\n\t"
      "s_waitcnt lgkmcnt(12)\n\t"
      "v_mfma_f32_32x32x16_bf16 v[160:175], v[100:103], a[192:195], v[160:175]\n\t"
      "ds_read_b64_tr_b16 v[64:65], v17 offset:40960\n\t"
      "ds_read_b64_tr_b16 v[66:67], v17 offset:43008\n\t"
      "s_waitcnt lgkmcnt(12)\n\t"
      "v_mfma_f32_32x32x16_bf16 v[176:191], v[104:107], a[192:195], v[176:191]\n\t"
      "s_mov_b32 s78, 524288\n\t"
      "s_mov_b32 exec_lo, 15\n\t"
      "s_mov_b32 exec_hi, 0\n\t"
      "buffer_load_dwordx2 v[36:37], v38, s[24:27], s78 offen sc1\n\t"
      "s_mov_b64 exec, -1\n\t"
      "ds_read_b64_tr_b16 v[68:69], v15 offset:45056\n\t"
      "ds_read_b64_tr_b16 v[70:71], v15 offset:47104\n\t"
      "s_waitcnt vmcnt(19) lgkmcnt(12)\n\t"
      "v_mfma_f32_32x32x16_bf16 v[144:159], v[108:111], a[196:199], v[144:159]\n\t"
      "ds_read_b64_tr_b16 v[72:73], v16 offset:45056\n\t"
      "ds_read_b64_tr_b16 v[74:75], v16 offset:47104\n\t"
      "s_waitcnt lgkmcnt(12)\n\t"
      "v_mfma_f32_32x32x16_bf16 v[160:175], v[112:115], a[196:199], v[160:175]\n\t"
      "ds_read_b64_tr_b16 v[76:77], v17 offset:45056\n\t"
      "ds_read_b64_tr_b16 v[78:79], v17 offset:47104\n\t"
      "s_waitcnt lgkmcnt(12)\n\t"
      "v_mfma_f32_32x32x16_bf16 v[176:191], v[116:119], a[196:199], v[176:191]\n\t"
      "ds_read_b64_tr_b16 v[80:81], v15 offset:49152\n\t"
      "ds_read_b64_tr_b16 v[82:83], v15 offset:51200\n\t"
      "s_waitcnt vmcnt(18) lgkmcnt(12)\n\t"
      "v_mfma_f32_32x32x16_bf16 v[144:159], v[120:123], a[200:203], v[144:159]\n\t"
      "ds_read_b64_tr_b16 v[84:85], v15 offset:53248\n\t"
      "ds_read_b64_tr_b16 v[86:87], v15 offset:55296\n\t"
      "s_waitcnt lgkmcnt(12)\n\t"
      "v_mfma_f32_32x32x16_bf16 v[160:175], v[124:127], a[200:203], v[160:175]\n\t"
      "ds_read_b64_tr_b16 v[88:89], v15 offset:57344\n\t"
      "ds_read_b64_tr_b16 v[90:91], v15 offset:59392\n\t"
      "s_waitcnt lgkmcnt(12)\n\t"
      "v_mfma_f32_32x32x16_bf16 v[176:191], v[64:67], a[200:203], v[176:191]\n\t"
      "ds_read_b64_tr_b16 v[92:93], v15 offset:61440\n\t"
      "ds_read_b64_tr_b16 v[94:95], v15 offset:63488\n\t"
      "s_waitcnt vmcnt(17) lgkmcnt(12)\n\t"
      "v_mfma_f32_32x32x16_bf16 v[144:159], v[68:71], a[204:207], v[144:159]\n\t"
      "s_waitcnt vmcnt(0)\n\t"
      "v_cmp_eq_u64 vcc, s[48:49], v[36:37]\n\t"
      "s_nop 0\n\t"
      "s_and_b32 s78, vcc_lo, 15\n\t"
      "s_cmp_eq_u32 s78, 15\n\t"
      "s_cbranch_scc1 .Lskip_42_%=\n\t"
      "s_mov_b32 s79, 32768\n\t"
      ".Lspin_43_%=:\n\t"
      "s_sleep 1\n\t"
      "s_mov_b32 s78, 524288\n\t"
      "s_mov_b32 exec_lo, 15\n\t"
      "s_mov_b32 exec_hi, 0\n\t"
      "buffer_load_dwordx2 v[36:37], v38, s[24:27], s78 offen sc1\n\t"
      "s_mov_b64 exec, -1\n\t"
      "s_waitcnt vmcnt(0)\n\t"
      "v_cmp_eq_u64 vcc, s[48:49], v[36:37]\n\t"
      "s_nop 0\n\t"
      "s_and_b32 s78, vcc_lo, 15\n\t"
      "s_cmp_eq_u32 s78, 15\n\t"
      "s_cbranch_scc1 .Lskip_42_%=\n\t"
      "s_sub_u32 s79, s79, 1\n\t"
      "s_cmp_lg_u32 s79, 0\n\t"
      "s_cbranch_scc1 .Lspin_43_%=\n\t"
      ".Lskip_42_%=:\n\t"
      "s_add_u32 s62, s75, 28\n\t"
      "s_lshl_b32 s62, s62, 12\n\t"
      "s_add_u32 s62, s62, s72\n\t"
      "buffer_load_dwordx4 a[240:243], v1, s[24:27], s62 offen sc1\n\t"
      "ds_read_b64_tr_b16 v[96:97], v19 offset:0\n\t"
      "ds_read_b64_tr_b16 v[98:99], v19 offset:2048\n\t"
      "s_waitcnt lgkmcnt(12)\n\t"
      "v_mfma_f32_32x32x16_bf16 v[160:175], v[72:75], a[204:207], v[160:175]\n\t"
      "ds_read_b64_tr_b16 v[100:101], v19 offset:4096\n\t"
      "ds_read_b64_tr_b16 v[102:103], v19 offset:6144\n\t"
      "s_waitcnt lgkmcnt(12)\n\t"
      "v_mfma_f32_32x32x16_bf16 v[176:191], v[76:79], a[204:207], v[176:191]\n\t"
      "buffer_load_dwordx4 a[244:247], v1, s[24:27], s62 offen offset:1024 sc1\n\t"
      "ds_read_b64_tr_b16 v[104:105], v19 offset:8192\n\t"
      "ds_read_b64_tr_b16 v[106:107], v19 offset:10240\n\t"
      "s_waitcnt lgkmcnt(12)\n\t"
      "v_mfma_f32_32x32x16_bf16 v[144:159], v[80:83], a[208:211], v[144:159]\n\t"
      "ds_read_b64_tr_b16 v[108:109], v19 offset:12288\n\t"
      "ds_read_b64_tr_b16 v[110:111], v19 offset:14336\n\t"
      "s_waitcnt lgkmcnt(12)\n\t"
      "v_mfma_f32_32x32x16_bf16 v[144:159], v[84:87], a[212:215], v[144:159]\n\t"
      "buffer_load_dwordx4 a[248:251], v1, s[24:27], s62 offen offset:2048 sc1\n\t"
      "ds_read_b64_tr_b16 v[112:113], v16 offset:49152\n\t"
      "ds_read_b64_tr_b16 v[114:115], v16 offset:51200\n\t"
      "s_waitcnt lgkmcnt(12)\n\t"
      "v_mfma_f32_32x32x16_bf16 v[144:159], v[88:91], a[216:219], v[144:159]\n\t"
      "ds_read_b64_tr_b16 v[116:117], v16 offset:53248\n\t"
      "ds_read_b64_tr_b16 v[118:119], v16 offset:55296\n\t"
      "s_waitcnt lgkmcnt(12)\n\t"
      "v_mfma_f32_32x32x16_bf16 v[144:159], v[92:95], a[220:223], v[144:159]\n\t"
      "buffer_load_dwordx4 a[252:255], v1, s[24:27], s62 offen offset:3072 sc1\n\t"
      "ds_read_b64_tr_b16 v[120:121], v16 offset:57344\n\t"
      "ds_read_b64_tr_b16 v[122:123], v16 offset:59392\n\t"
      "s_waitcnt lgkmcnt(12)\n\t"
      "v_mfma_f32_32x32x16_bf16 v[144:159], v[96:99], a[224:227], v[144:159]\n\t"
      "ds_read_b64_tr_b16 v[124:125], v16 offset:61440\n\t"
      "ds_read_b64_tr_b16 v[126:127], v16 offset:63488\n\t"
      "s_waitcnt lgkmcnt(12)\n\t"
      "v_mfma_f32_32x32x16_bf16 v[144:159], v[100:103], a[228:231], v[144:159]\n\t"
      "s_add_u32 s62, s75, 29\n\t"
      "s_lshl_b32 s62, s62, 12\n\t"
      "s_add_u32 s62, s62, s72\n\t"
      "buffer_load_dwordx4 a[0:3], v1, s[24:27], s62 offen sc1\n\t"
      "ds_read_b64_tr_b16 v[64:65], v20 offset:0\n\t"
      "ds_read_b64_tr_b16 v[66:67], v20 offset:2048\n\t"
      "s_waitcnt lgkmcnt(12)\n\t"
      "v_mfma_f32_32x32x16_bf16 v[144:159], v[104:107], a[232:235], v[144:159]\n\t"
      "ds_read_b64_tr_b16 v[68:69], v20 offset:4096\n\t"
      "ds_read_b64_tr_b16 v[70:71], v20 offset:6144\n\t"
      "s_waitcnt lgkmcnt(12)\n\t"
      "v_mfma_f32_32x32x16_bf16 v[144:159], v[108:111], a[236:239], v[144:159]\n\t"
      "buffer_load_dwordx4 a[4:7], v1, s[24:27], s62 offen offset:1024 sc1\n\t"
      "ds_read_b64_tr_b16 v[72:73], v20 offset:8192\n\t"
      "ds_read_b64_tr_b16 v[74:75], v20 offset:10240\n\t"
      "s_waitcnt lgkmcnt(12)\n\t"
      "v_mfma_f32_32x32x16_bf16 v[160:175], v[112:115], a[208:211], v[160:175]\n\t"
      "s_nop 0\n\t"
      "s_nop 0\n\t"
      "s_nop 0\n\t"
      "ds_read_b64_tr_b16 v[76:77], v20 offset:12288\n\t"
      "ds_read_b64_tr_b16 v[78:79], v20 offset:14336\n\t"
      "s_waitcnt lgkmcnt(12)\n\t"
      "v_mfma_f32_32x32x16_bf16 v[160:175], v[116:119], a[212:215], v[160:175]\n\t"
      "buffer_load_dwordx4 a[8:11], v1, s[24:27], s62 offen offset:2048 sc1\n\t"
      "v_mul_f32 v144, v144, v60\n\t"
      "v_mul_f32 v145, v145, v60\n\t"
      "v_mul_f32 v146, v146, v60\n\t"
      "ds_read_b64_tr_b16 v[80:81], v17 offset:49152\n\t"
      "ds_read_b64_tr_b16 v[82:83], v17 offset:51200\n\t"
      "s_waitcnt lgkmcnt(12)\n\t"
      "v_mfma_f32_32x32x16_bf16 v[160:175], v[120:123], a[216:219], v[160:175]\n\t"
      "v_mul_f32 v147, v147, v60\n\t"
      "v_mul_f32 v148, v148, v60\n\t"
      "v_mul_f32 v149, v149, v60\n\t"
      "ds_read_b64_tr_b16 v[84:85], v17 offset:53248\n\t"
      "ds_read_b64_tr_b16 v[86:87], v17 offset:55296\n\t"
      "s_waitcnt lgkmcnt(12)\n\t"
      "v_mfma_f32_32x32x16_bf16 v[160:175], v[124:127], a[220:223], v[160:175]\n\t"
      "buffer_load_dwordx4 a[12:15], v1, s[24:27], s62 offen offset:3072 sc1\n\t"
      "v_mul_f32 v150, v150, v60\n\t"
      "v_mul_f32 v151, v151, v60\n\t"
      "v_mul_f32 v152, v152, v60\n\t"
      "ds_read_b64_tr_b16 v[88:89], v17 offset:57344\n\t"
      "ds_read_b64_tr_b16 v[90:91], v17 offset:59392\n\t"
      "s_waitcnt lgkmcnt(12)\n\t"
      "v_mfma_f32_32x32x16_bf16 v[160:175], v[64:67], a[224:227], v[160:175]\n\t"
      "v_mul_f32 v153, v153, v60\n\t"
      "v_mul_f32 v154, v154, v60\n\t"
      "ds_read_b64_tr_b16 v[92:93], v17 offset:61440\n\t"
      "ds_read_b64_tr_b16 v[94:95], v17 offset:63488\n\t"
      "s_waitcnt lgkmcnt(12)\n\t"
      "v_mfma_f32_32x32x16_bf16 v[160:175], v[68:71], a[228:231], v[160:175]\n\t"
      "v_mul_f32 v155, v155, v60\n\t"
      "v_mul_f32 v156, v156, v60\n\t"
      "v_mul_f32 v157, v157, v60\n\t"
      "ds_read_b64_tr_b16 v[96:97], v21 offset:0\n\t"
      "ds_read_b64_tr_b16 v[98:99], v21 offset:2048\n\t"
      "s_waitcnt lgkmcnt(12)\n\t"
      "v_mfma_f32_32x32x16_bf16 v[160:175], v[72:75], a[232:235], v[160:175]\n\t"
      "v_mul_f32 v158, v158, v60\n\t"
      "v_mul_f32 v159, v159, v60\n\t"
      "ds_read_b64_tr_b16 v[100:101], v21 offset:4096\n\t"
      "ds_read_b64_tr_b16 v[102:103], v21 offset:6144\n\t"
      "s_waitcnt lgkmcnt(12)\n\t"
      "v_mfma_f32_32x32x16_bf16 v[160:175], v[76:79], a[236:239], v[160:175]\n\t"
      "ds_read_b64_tr_b16 v[104:105], v21 offset:8192\n\t"
      "ds_read_b64_tr_b16 v[106:107], v21 offset:10240\n\t"
      "s_waitcnt lgkmcnt(12)\n\t"
      "v_mfma_f32_32x32x16_bf16 v[176:191], v[80:83], a[208:211], v[176:191]\n\t"
      "s_nop 0\n\t"
      "s_nop 0\n\t"
      "s_nop 0\n\t"
      "ds_read_b64_tr_b16 v[108:109], v21 offset:12288\n\t"
      "ds_read_b64_tr_b16 v[110:111], v21 offset:14336\n\t"
      "s_waitcnt lgkmcnt(12)\n\t"
      "v_mfma_f32_32x32x16_bf16 v[176:191], v[84:87], a[212:215], v[176:191]\n\t"
      "s_nop 0\n\t"
      "v_mul_f32 v160, v160, v60\n\t"
      "v_mul_f32 v161, v161, v60\n\t"
      "v_mul_f32 v162, v162, v60\n\t"
      "s_waitcnt lgkmcnt(10)\n\t"
      "v_mfma_f32_32x32x16_bf16 v[176:191], v[88:91], a[216:219], v[176:191]\n\t"
      "v_mul_f32 v163, v163, v60\n\t"
      "v_mul_f32 v164, v164, v60\n\t"
      "v_mul_f32 v165, v165, v60\n\t"
      "s_waitcnt lgkmcnt(0)\n\t"
      "s_barrier\n\t"
      "ds_read_b64_tr_b16 v[112:113], v15 offset:0\n\t"
      "ds_read_b64_tr_b16 v[114:115], v15 offset:2048\n\t"
      "ds_read_b64_tr_b16 v[116:117], v15 offset:4096\n\t"
      "ds_read_b64_tr_b16 v[118:119], v15 offset:6144\n\t"
      "v_mfma_f32_32x32x16_bf16 v[176:191], v[92:95], a[220:223], v[176:191]\n\t"
      "v_mul_f32 v166, v166, v60\n\t"
      "v_mul_f32 v167, v167, v60\n\t"
      "v_mul_f32 v168, v168, v60\n\t"
      "ds_read_b64_tr_b16 v[120:121], v15 offset:8192\n\t"
      "ds_read_b64_tr_b16 v[122:123], v15 offset:10240\n\t"
      "v_mfma_f32_32x32x16_bf16 v[176:191], v[96:99], a[224:227], v[176:191]\n\t"
      "v_mul_f32 v169, v169, v60\n\t"
      "v_mul_f32 v170, v170, v60\n\t"
      "ds_read_b64_tr_b16 v[124:125], v15 offset:12288\n\t"
      "ds_read_b64_tr_b16 v[126:127], v15 offset:14336\n\t"
      "v_mfma_f32_32x32x16_bf16 v[176:191], v[100:103], a[228:231], v[176:191]\n\t"
      "v_mul_f32 v171, v171, v60\n\t"
      "v_mul_f32 v172, v172, v60\n\t"
      "v_mul_f32 v173, v173, v60\n\t"
      "ds_read_b64_tr_b16 v[64:65], v15 offset:16384\n\t"
      "ds_read_b64_tr_b16 v[66:67], v15 offset:18432\n\t"
      "v_mfma_f32_32x32x16_bf16 v[176:191], v[104:107], a[232:235], v[176:191]\n\t"
      "v_mul_f32 v174, v174, v60\n\t"
      "v_mul_f32 v175, v175, v60\n\t"
      "ds_read_b64_tr_b16 v[68:69], v15 offset:20480\n\t"
      "ds_read_b64_tr_b16 v[70:71], v15 offset:22528\n\t"
      "v_mfma_f32_32x32x16_bf16 v[176:191], v[108:111], a[236:239], v[176:191]\n\t"
      "ds_read_b64_tr_b16 v[72:73], v15 offset:24576\n\t"
      "ds_read_b64_tr_b16 v[74:75], v15 offset:26624\n\t"
      "s_nop 0\n\t"
      "s_nop 0\n\t"
      "s_nop 0\n\t"
      "s_waitcnt vmcnt(7) lgkmcnt(12)\n\t"
      "v_mfma_f32_32x32x16_bf16 v[144:159], v[112:115], a[240:243], v[144:159]\n\t"
      "s_nop 4\n\t"
      "v_mul_f32 v176, v176, v60\n\t"
      "v_mul_f32 v177, v177, v60\n\t"
      "v_mul_f32 v178, v178, v60\n\t"
      "ds_read_b64_tr_b16 v[76:77], v15 offset:28672\n\t"
      "ds_read_b64_tr_b16 v[78:79], v15 offset:30720\n\t"
      "s_waitcnt vmcnt(6) lgkmcnt(12)\n\t"
      "v_mfma_f32_32x32x16_bf16 v[144:159], v[116:119], a[244:247], v[144:159]\n\t"
      "v_mul_f32 v179, v179, v60\n\t"
      "v_mul_f32 v180, v180, v60\n\t"
      "v_mul_f32 v181, v181, v60\n\t"
      "ds_read_b64_tr_b16 v[80:81], v16 offset:0\n\t"
      "ds_read_b64_tr_b16 v[82:83], v16 offset:2048\n\t"
      "s_waitcnt vmcnt(5) lgkmcnt(12)\n\t"
      "v_mfma_f32_32x32x16_bf16 v[144:159], v[120:123], a[248:251], v[144:159]\n\t"
      "v_mul_f32 v182, v182, v60\n\t"
      "v_mul_f32 v183, v183, v60\n\t"
      "v_mul_f32 v184, v184, v60\n\t"
      "ds_read_b64_tr_b16 v[84:85], v16 offset:4096\n\t"
      "ds_read_b64_tr_b16 v[86:87], v16 offset:6144\n\t"
      "s_waitcnt vmcnt(4) lgkmcnt(12)\n\t"
      "v_mfma_f32_32x32x16_bf16 v[144:159], v[124:127], a[252:255], v[144:159]\n\t"
      "v_mul_f32 v185, v185, v60\n\t"
      "v_mul_f32 v186, v186, v60\n\t"
      "v_mul_f32 v187, v187, v60\n\t"
      "ds_read_b64_tr_b16 v[88:89], v16 offset:8192\n\t"
      "ds_read_b64_tr_b16 v[90:91], v16 offset:10240\n\t"
      "s_waitcnt vmcnt(3) lgkmcnt(12)\n\t"
      "v_mfma_f32_32x32x16_bf16 v[144:159], v[64:67], a[0:3], v[144:159]\n\t"
      "v_mul_f32 v188, v188, v60\n\t"
      "v_mul_f32 v189, v189, v60\n\t"
      "ds_read_b64_tr_b16 v[92:93], v16 offset:12288\n\t"
      "ds_read_b64_tr_b16 v[94:95], v16 offset:14336\n\t"
      "s_waitcnt vmcnt(2) lgkmcnt(12)\n\t"
      "v_mfma_f32_32x32x16_bf16 v[144:159], v[68:71], a[4:7], v[144:159]\n\t"
      "v_mul_f32 v190, v190, v60\n\t"
      "v_mul_f32 v191, v191, v60\n\t"
      "ds_read_b64_tr_b16 v[96:97], v16 offset:16384\n\t"
      "ds_read_b64_tr_b16 v[98:99], v16 offset:18432\n\t"
      "s_waitcnt vmcnt(1) lgkmcnt(12)\n\t"
      "v_mfma_f32_32x32x16_bf16 v[144:159], v[72:75], a[8:11], v[144:159]\n\t"
      "ds_read_b64_tr_b16 v[100:101], v16 offset:20480\n\t"
      "ds_read_b64_tr_b16 v[102:103], v16 offset:22528\n\t"
      "s_waitcnt vmcnt(0) lgkmcnt(12)\n\t"
      "v_mfma_f32_32x32x16_bf16 v[144:159], v[76:79], a[12:15], v[144:159]\n\t"
      "ds_read_b64_tr_b16 v[104:105], v16 offset:24576\n\t"
      "ds_read_b64_tr_b16 v[106:107], v16 offset:26624\n\t"
      "s_nop 0\n\t"
      "s_nop 0\n\t"
      "s_nop 0\n\t"
      "s_waitcnt lgkmcnt(12)\n\t"
      "v_mfma_f32_32x32x16_bf16 v[160:175], v[80:83], a[240:243], v[160:175]\n\t"
      "s_nop 4\n\t"
      "v_cvt_pk_bf16_f32 v208, v144, v145\n\t"
      "v_cvt_pk_bf16_f32 v209, v146, v147\n\t"
      "v_cvt_pk_bf16_f32 v210, v148, v149\n\t"
      "ds_read_b64_tr_b16 v[108:109], v16 offset:28672\n\t"
      "ds_read_b64_tr_b16 v[110:111], v16 offset:30720\n\t"
      "s_waitcnt lgkmcnt(12)\n\t"
      "v_mfma_f32_32x32x16_bf16 v[160:175], v[84:87], a[244:247], v[160:175]\n\t"
      "v_cvt_pk_bf16_f32 v211, v150, v151\n\t"
      "v_cvt_pk_bf16_f32 v212, v152, v153\n\t"
      "v_cvt_pk_bf16_f32 v213, v154, v155\n\t"
      "ds_read_b64_tr_b16 v[112:113], v17 offset:0\n\t"
      "ds_read_b64_tr_b16 v[114:115], v17 offset:2048\n\t"
      "s_waitcnt lgkmcnt(12)\n\t"
      "v_mfma_f32_32x32x16_bf16 v[160:175], v[88:91], a[248:251], v[160:175]\n\t"
      "v_cvt_pk_bf16_f32 v214, v156, v157\n\t"
      "v_cvt_pk_bf16_f32 v215, v158, v159\n\t"
      "buffer_store_dwordx4 v[208:211], v1, s[24:27], s32 offen sc1\n\t"
      "buffer_store_dwordx4 v[212:215], v1, s[24:27], s32 offen offset:1024 sc1\n\t"
      "ds_read_b64_tr_b16 v[116:117], v17 offset:4096\n\t"
      "ds_read_b64_tr_b16 v[118:119], v17 offset:6144\n\t"
      "s_waitcnt lgkmcnt(12)\n\t"
      "v_mfma_f32_32x32x16_bf16 v[160:175], v[92:95], a[252:255], v[160:175]\n\t"
      "ds_read_b64_tr_b16 v[120:121], v17 offset:8192\n\t"
      "ds_read_b64_tr_b16 v[122:123], v17 offset:10240\n\t"
      "s_waitcnt lgkmcnt(12)\n\t"
      "v_mfma_f32_32x32x16_bf16 v[160:175], v[96:99], a[0:3], v[160:175]\n\t"
      "ds_read_b64_tr_b16 v[124:125], v17 offset:12288\n\t"
      "ds_read_b64_tr_b16 v[126:127], v17 offset:14336\n\t"
      "s_waitcnt lgkmcnt(12)\n\t"
      "v_mfma_f32_32x32x16_bf16 v[160:175], v[100:103], a[4:7], v[160:175]\n\t"
      "ds_read_b64_tr_b16 v[64:65], v17 offset:16384\n\t"
      "ds_read_b64_tr_b16 v[66:67], v17 offset:18432\n\t"
      "s_waitcnt lgkmcnt(12)\n\t"
      "v_mfma_f32_32x32x16_bf16 v[160:175], v[104:107], a[8:11], v[160:175]\n\t"
      "ds_read_b64_tr_b16 v[68:69], v17 offset:20480\n\t"
      "ds_read_b64_tr_b16 v[70:71], v17 offset:22528\n\t"
      "s_waitcnt lgkmcnt(12)\n\t"
      "v_mfma_f32_32x32x16_bf16 v[160:175], v[108:111], a[12:15], v[160:175]\n\t"
      "ds_read_b64_tr_b16 v[72:73], v17 offset:24576\n\t"
      "ds_read_b64_tr_b16 v[74:75], v17 offset:26624\n\t"
      "s_nop 0\n\t"
      "s_nop 0\n\t"
      "s_nop 0\n\t"
      "s_waitcnt lgkmcnt(12)\n\t"
      "v_mfma_f32_32x32x16_bf16 v[176:191], v[112:115], a[240:243], v[176:191]\n\t"
      "s_nop 4\n\t"
      "v_cvt_pk_bf16_f32 v216, v160, v161\n\t"
      "v_cvt_pk_bf16_f32 v217, v162, v163\n\t"
      "v_cvt_pk_bf16_f32 v218, v164, v165\n\t"
      "ds_read_b64_tr_b16 v[76:77], v17 offset:28672\n\t"
      "ds_read_b64_tr_b16 v[78:79], v17 offset:30720\n\t"
      "s_waitcnt lgkmcnt(12)\n\t"
      "v_mfma_f32_32x32x16_bf16 v[176:191], v[116:119], a[244:247], v[176:191]\n\t"
      "v_cvt_pk_bf16_f32 v219, v166, v167\n\t"
      "v_cvt_pk_bf16_f32 v220, v168, v169\n\t"
      "v_cvt_pk_bf16_f32 v221, v170, v171\n\t"
      "ds_read_b64_tr_b16 v[80:81], v22 offset:0\n\t"
      "ds_read_b64_tr_b16 v[82:83], v22 offset:2048\n\t"
      "s_waitcnt lgkmcnt(12)\n\t"
      "v_mfma_f32_32x32x16_bf16 v[176:191], v[120:123], a[248:251], v[176:191]\n\t"
      "v_cvt_pk_bf16_f32 v222, v172, v173\n\t"
      "v_cvt_pk_bf16_f32 v223, v174, v175\n\t"
      "buffer_store_dwordx4 v[216:219], v1, s[24:27], s33 offen sc1\n\t"
      "buffer_store_dwordx4 v[220:223], v1, s[24:27], s33 offen offset:1024 sc1\n\t"
      "ds_read_b64_tr_b16 v[84:85], v22 offset:4096\n\t"
      "ds_read_b64_tr_b16 v[86:87], v22 offset:6144\n\t"
      "s_waitcnt lgkmcnt(12)\n\t"
      "v_mfma_f32_32x32x16_bf16 v[176:191], v[124:127], a[252:255], v[176:191]\n\t"
      "ds_read_b64_tr_b16 v[88:89], v22 offset:8192\n\t"
      "ds_read_b64_tr_b16 v[90:91], v22 offset:10240\n\t"
      "s_waitcnt lgkmcnt(12)\n\t"
      "v_mfma_f32_32x32x16_bf16 v[176:191], v[64:67], a[0:3], v[176:191]\n\t"
      "ds_read_b64_tr_b16 v[92:93], v22 offset:12288\n\t"
      "ds_read_b64_tr_b16 v[94:95], v22 offset:14336\n\t"
      "s_waitcnt lgkmcnt(12)\n\t"
      "v_mfma_f32_32x32x16_bf16 v[176:191], v[68:71], a[4:7], v[176:191]\n\t"
      "ds_read_b64_tr_b16 v[96:97], v22 offset:16384\n\t"
      "ds_read_b64_tr_b16 v[98:99], v22 offset:18432\n\t"
      "s_waitcnt lgkmcnt(12)\n\t"
      "v_mfma_f32_32x32x16_bf16 v[176:191], v[72:75], a[8:11], v[176:191]\n\t"
      "ds_read_b64_tr_b16 v[100:101], v22 offset:20480\n\t"
      "ds_read_b64_tr_b16 v[102:103], v22 offset:22528\n\t"
      "s_waitcnt lgkmcnt(12)\n\t"
      "v_mfma_f32_32x32x16_bf16 v[176:191], v[76:79], a[12:15], v[176:191]\n\t"
      "ds_read_b64_tr_b16 v[104:105], v22 offset:24576\n\t"
      "ds_read_b64_tr_b16 v[106:107], v22 offset:26624\n\t"
      "s_waitcnt lgkmcnt(12)\n\t"
      "v_mfma_f32_32x32x16_bf16 v[128:143], v[80:83], a[160:163], v[128:143]\n\t"
      "s_nop 0\n\t"
      "s_nop 0\n\t"
      "s_nop 0\n\t"
      "ds_read_b64_tr_b16 v[108:109], v22 offset:28672\n\t"
      "ds_read_b64_tr_b16 v[110:111], v22 offset:30720\n\t"
      "s_waitcnt lgkmcnt(12)\n\t"
      "v_mfma_f32_32x32x16_bf16 v[128:143], v[84:87], a[164:167], v[128:143]\n\t"
      "s_nop 0\n\t"
      "v_cvt_pk_bf16_f32 v224, v176, v177\n\t"
      "v_cvt_pk_bf16_f32 v225, v178, v179\n\t"
      "v_cvt_pk_bf16_f32 v226, v180, v181\n\t"
      "ds_read_b64_tr_b16 v[112:113], v14 offset:32768\n\t"
      "ds_read_b64_tr_b16 v[114:115], v14 offset:34816\n\t"
      "s_waitcnt lgkmcnt(12)\n\t"
      "v_mfma_f32_32x32x16_bf16 v[128:143], v[88:91], a[168:171], v[128:143]\n\t"
      "v_cvt_pk_bf16_f32 v227, v182, v183\n\t"
      "v_cvt_pk_bf16_f32 v228, v184, v185\n\t"
      "v_cvt_pk_bf16_f32 v229, v186, v187\n\t"
      "ds_read_b64_tr_b16 v[116:117], v14 offset:36864\n\t"
      "ds_read_b64_tr_b16 v[118:119], v14 offset:38912\n\t"
      "s_waitcnt lgkmcnt(12)\n\t"
      "v_mfma_f32_32x32x16_bf16 v[128:143], v[92:95], a[172:175], v[128:143]\n\t"
      "v_cvt_pk_bf16_f32 v230, v188, v189\n\t"
      "v_cvt_pk_bf16_f32 v231, v190, v191\n\t"
      "buffer_store_dwordx4 v[224:227], v1, s[24:27], s34 offen sc1\n\t"
      "buffer_store_dwordx4 v[228:231], v1, s[24:27], s34 offen offset:1024 sc1\n\t"
      "ds_read_b64_tr_b16 v[120:121], v14 offset:40960\n\t"
      "ds_read_b64_tr_b16 v[122:123], v14 offset:43008\n\t"
      "s_waitcnt lgkmcnt(12)\n\t"
      "v_mfma_f32_32x32x16_bf16 v[128:143], v[96:99], a[176:179], v[128:143]\n\t"
      "ds_read_b64_tr_b16 v[124:125], v14 offset:45056\n\t"
      "ds_read_b64_tr_b16 v[126:127], v14 offset:47104\n\t"
      "s_waitcnt lgkmcnt(12)\n\t"
      "v_mfma_f32_32x32x16_bf16 v[128:143], v[100:103], a[180:183], v[128:143]\n\t"
      "ds_read_b64_tr_b16 v[64:65], v14 offset:49152\n\t"
      "ds_read_b64_tr_b16 v[66:67], v14 offset:51200\n\t"
      "s_waitcnt lgkmcnt(12)\n\t"
      "v_mfma_f32_32x32x16_bf16 v[128:143], v[104:107], a[184:187], v[128:143]\n\t"
      "ds_read_b64_tr_b16 v[68:69], v14 offset:53248\n\t"
      "ds_read_b64_tr_b16 v[70:71], v14 offset:55296\n\t"
      "s_waitcnt lgkmcnt(12)\n\t"
      "v_mfma_f32_32x32x16_bf16 v[128:143], v[108:111], a[188:191], v[128:143]\n\t"
      "ds_read_b64_tr_b16 v[72:73], v14 offset:57344\n\t"
      "ds_read_b64_tr_b16 v[74:75], v14 offset:59392\n\t"
      "s_waitcnt lgkmcnt(12)\n\t"
      "v_mfma_f32_32x32x16_bf16 v[128:143], v[112:115], a[192:195], v[128:143]\n\t"
      "ds_read_b64_tr_b16 v[76:77], v14 offset:61440\n\t"
      "ds_read_b64_tr_b16 v[78:79], v14 offset:63488\n\t"
      "s_waitcnt lgkmcnt(12)\n\t"
      "v_mfma_f32_32x32x16_bf16 v[128:143], v[116:119], a[196:199], v[128:143]\n\t"
      "ds_read_b64_tr_b16 v[80:81], v18 offset:0\n\t"
      "ds_read_b64_tr_b16 v[82:83], v18 offset:2048\n\t"
      "s_waitcnt lgkmcnt(12)\n\t"
      "v_mfma_f32_32x32x16_bf16 v[128:143], v[120:123], a[200:203], v[128:143]\n\t"
      "ds_read_b64_tr_b16 v[84:85], v18 offset:4096\n\t"
      "ds_read_b64_tr_b16 v[86:87], v18 offset:6144\n\t"
      "s_waitcnt lgkmcnt(12)\n\t"
      "v_mfma_f32_32x32x16_bf16 v[128:143], v[124:127], a[204:207], v[128:143]\n\t"
      "ds_read_b64_tr_b16 v[88:89], v18 offset:8192\n\t"
      "ds_read_b64_tr_b16 v[90:91], v18 offset:10240\n\t"
      "s_waitcnt lgkmcnt(12)\n\t"
      "v_mfma_f32_32x32x16_bf16 v[128:143], v[64:67], a[208:211], v[128:143]\n\t"
      "ds_read_b64_tr_b16 v[92:93], v18 offset:12288\n\t"
      "ds_read_b64_tr_b16 v[94:95], v18 offset:14336\n\t"
      "s_waitcnt lgkmcnt(12)\n\t"
      "v_mfma_f32_32x32x16_bf16 v[128:143], v[68:71], a[212:215], v[128:143]\n\t"
      "ds_read_b64_tr_b16 v[96:97], v14 offset:0\n\t"
      "ds_read_b64_tr_b16 v[98:99], v14 offset:2048\n\t"
      "s_waitcnt lgkmcnt(12)\n\t"
      "v_mfma_f32_32x32x16_bf16 v[128:143], v[72:75], a[216:219], v[128:143]\n\t"
      "s_waitcnt vmcnt(0)\n\t"
      "s_add_u32 s78, s47, 655360\n\t"
      "s_mov_b64 exec, 1\n\t"
      "buffer_store_dwordx2 v[62:63], off, s[24:27], s78 sc1\n\t"
      "s_mov_b64 exec, -1\n\t"
      "ds_read_b64_tr_b16 v[100:101], v14 offset:4096\n\t"
      "ds_read_b64_tr_b16 v[102:103], v14 offset:6144\n\t"
      "s_waitcnt lgkmcnt(12)\n\t"
      "v_mfma_f32_32x32x16_bf16 v[128:143], v[76:79], a[220:223], v[128:143]\n\t"
      "ds_read_b64_tr_b16 v[104:105], v14 offset:8192\n\t"
      "ds_read_b64_tr_b16 v[106:107], v14 offset:10240\n\t"
      "s_waitcnt lgkmcnt(12)\n\t"
      "v_mfma_f32_32x32x16_bf16 v[128:143], v[80:83], a[224:227], v[128:143]\n\t"
      "ds_read_b64_tr_b16 v[108:109], v14 offset:12288\n\t"
      "ds_read_b64_tr_b16 v[110:111], v14 offset:14336\n\t"
      "s_waitcnt lgkmcnt(12)\n\t"
      "v_mfma_f32_32x32x16_bf16 v[128:143], v[84:87], a[228:231], v[128:143]\n\t"
      "ds_read_b64_tr_b16 v[112:113], v14 offset:16384\n\t"
      "ds_read_b64_tr_b16 v[114:115], v14 offset:18432\n\t"
      "s_waitcnt lgkmcnt(12)\n\t"
      "v_mfma_f32_32x32x16_bf16 v[128:143], v[88:91], a[232:235], v[128:143]\n\t"
      "ds_read_b64_tr_b16 v[116:117], v14 offset:20480\n\t"
      "ds_read_b64_tr_b16 v[118:119], v14 offset:22528\n\t"
      "s_waitcnt lgkmcnt(12)\n\t"
      "v_mfma_f32_32x32x16_bf16 v[128:143], v[92:95], a[236:239], v[128:143]\n\t"
      "ds_read_b64_tr_b16 v[120:121], v14 offset:24576\n\t"
      "ds_read_b64_tr_b16 v[122:123], v14 offset:26624\n\t"
      "s_nop 9\n\t"
      "v_mul_f32 v128, v128, v60\n\t"
      "v_mul_f32 v129, v129, v60\n\t"
      "v_mul_f32 v130, v130, v60\n\t"
      "v_mul_f32 v131, v131, v60\n\t"
      "v_mul_f32 v132, v132, v60\n\t"
      "v_mul_f32 v133, v133, v60\n\t"
      "v_mul_f32 v134, v134, v60\n\t"
      "v_mul_f32 v135, v135, v60\n\t"
      "v_mul_f32 v136, v136, v60\n\t"
      "v_mul_f32 v137, v137, v60\n\t"
      "v_mul_f32 v138, v138, v60\n\t"
      "v_mul_f32 v139, v139, v60\n\t"
      "v_mul_f32 v140, v140, v60\n\t"
      "v_mul_f32 v141, v141, v60\n\t"
      "v_mul_f32 v142, v142, v60\n\t"
      "v_mul_f32 v143, v143, v60\n\t"
      "s_mov_b32 s78, 655360\n\t"
      "s_mov_b32 exec_lo, 7\n\t"
      "s_mov_b32 exec_hi, 0\n\t"
      "buffer_load_dwordx2 v[202:203], v34, s[24:27], s78 offen sc1\n\t"
      "s_mov_b64 exec, -1\n\t"
      "s_waitcnt lgkmcnt(12)\n\t"
      "v_mfma_f32_32x32x16_bf16 v[128:143], v[96:99], a[240:243], v[128:143]\n\t"
      "ds_read_b64_tr_b16 v[124:125], v14 offset:28672\n\t"
      "ds_read_b64_tr_b16 v[126:127], v14 offset:30720\n\t"
      "s_waitcnt lgkmcnt(12)\n\t"
      "v_mfma_f32_32x32x16_bf16 v[128:143], v[100:103], a[244:247], v[128:143]\n\t"
      "s_waitcnt lgkmcnt(10)\n\t"
      "v_mfma_f32_32x32x16_bf16 v[128:143], v[104:107], a[248:251], v[128:143]\n\t"
      "s_waitcnt lgkmcnt(8)\n\t"
      "v_mfma_f32_32x32x16_bf16 v[128:143], v[108:111], a[252:255], v[128:143]\n\t"
      "s_waitcnt lgkmcnt(6)\n\t"
      "v_mfma_f32_32x32x16_bf16 v[128:143], v[112:115], a[0:3], v[128:143]\n\t"
      "s_waitcnt lgkmcnt(4)\n\t"
      "v_mfma_f32_32x32x16_bf16 v[128:143], v[116:119], a[4:7], v[128:143]\n\t"
      "s_waitcnt lgkmcnt(2)\n\t"
      "v_mfma_f32_32x32x16_bf16 v[128:143], v[120:123], a[8:11], v[128:143]\n\t"
      "s_waitcnt lgkmcnt(0)\n\t"
      "v_mfma_f32_32x32x16_bf16 v[128:143], v[124:127], a[12:15], v[128:143]\n\t"
      "s_waitcnt vmcnt(0)\n\t"
      "v_cmp_eq_u64 vcc, s[48:49], v[202:203]\n\t"
      "s_nop 0\n\t"
      "s_and_b32 s78, vcc_lo, 7\n\t"
      "s_cmp_eq_u32 s78, 7\n\t"
      "s_cbranch_scc1 .Lskip_44_%=\n\t"
      "s_mov_b32 s79, 32768\n\t"
      ".Lspin_45_%=:\n\t"
      "s_sleep 1\n\t"
      "s_mov_b32 s78, 655360\n\t"
      "s_mov_b32 exec_lo, 7\n\t"
      "s_mov_b32 exec_hi, 0\n\t"
      "buffer_load_dwordx2 v[202:203], v34, s[24:27], s78 offen sc1\n\t"
      "s_mov_b64 exec, -1\n\t"
      "s_waitcnt vmcnt(0)\n\t"
      "v_cmp_eq_u64 vcc, s[48:49], v[202:203]\n\t"
      "s_nop 0\n\t"
      "s_and_b32 s78, vcc_lo, 7\n\t"
      "s_cmp_eq_u32 s78, 7\n\t"
      "s_cbranch_scc1 .Lskip_44_%=\n\t"
      "s_sub_u32 s79, s79, 1\n\t"
      "s_cmp_lg_u32 s79, 0\n\t"
      "s_cbranch_scc1 .Lspin_45_%=\n\t"
      ".Lskip_44_%=:\n\t"
      "s_add_u32 s63, s81, 0\n\t"
      "buffer_load_dwordx4 v[232:235], v1, s[24:27], s63 offen sc1\n\t"
      "buffer_load_dwordx4 v[236:239], v1, s[24:27], s63 offen offset:1024 sc1\n\t"
      "s_add_u32 s63, s81, 8192\n\t"
      "buffer_load_dwordx4 v[240:243], v1, s[24:27], s63 offen sc1\n\t"
      "buffer_load_dwordx4 v[244:247], v1, s[24:27], s63 offen offset:1024 sc1\n\t"
      "s_add_u32 s63, s81, 16384\n\t"
      "buffer_load_dwordx4 v[248:251], v1, s[24:27], s63 offen sc1\n\t"
      "buffer_load_dwordx4 v[252:255], v1, s[24:27], s63 offen offset:1024 sc1\n\t"
      "s_waitcnt vmcnt(5)\n\t"
      "v_mfma_f32_32x32x16_bf16 v[128:143], v[40:43], v[232:235], v[128:143]\n\t"
      "s_waitcnt vmcnt(4)\n\t"
      "v_mfma_f32_32x32x16_bf16 v[128:143], v[44:47], v[236:239], v[128:143]\n\t"
      "s_waitcnt vmcnt(3)\n\t"
      "v_mfma_f32_32x32x16_bf16 v[128:143], v[40:43], v[240:243], v[128:143]\n\t"
      "s_waitcnt vmcnt(2)\n\t"
      "v_mfma_f32_32x32x16_bf16 v[128:143], v[44:47], v[244:247], v[128:143]\n\t"
      "s_waitcnt vmcnt(1)\n\t"
      "v_mfma_f32_32x32x16_bf16 v[128:143], v[40:43], v[248:251], v[128:143]\n\t"
      "s_waitcnt vmcnt(0)\n\t"
      "v_mfma_f32_32x32x16_bf16 v[128:143], v[44:47], v[252:255], v[128:143]\n\t"
      "s_nop 11\n\t"
      "v_cvt_pk_bf16_f32 v48, v128, v129\n\t"
      "v_cvt_pk_bf16_f32 v49, v130, v131\n\t"
      "v_cvt_pk_bf16_f32 v50, v132, v133\n\t"
      "v_cvt_pk_bf16_f32 v51, v134, v135\n\t"
      "v_cvt_pk_bf16_f32 v52, v136, v137\n\t"
      "v_cvt_pk_bf16_f32 v53, v138, v139\n\t"
      "v_cvt_pk_bf16_f32 v54, v140, v141\n\t"
      "v_cvt_pk_bf16_f32 v55, v142, v143\n\t"
      "v_permlane32_swap_b32 v48, v50\n\t"
      "v_permlane32_swap_b32 v49, v51\n\t"
      "buffer_store_dwordx4 v[48:51], v35, s[28:31], s80 offen\n\t"
      "v_permlane32_swap_b32 v52, v54\n\t"
      "v_permlane32_swap_b32 v53, v55\n\t"
      "buffer_store_dwordx4 v[52:55], v35, s[28:31], s80 offen offset:32\n\t"
      "s_endpgm\n\t"
      ".Lk_end_%=:\n\t"
      :
      : "{s[0:1]}"(__builtin_amdgcn_kernarg_segment_ptr()), "{s2}"(blockIdx.x), "{v0}"(threadIdx.x), "s"((unsigned)(size_t)lds)
      : "v1", "v2", "v3", "v4", "v5", "v6", "v7", "v8", "v9", "v10", "v11", "v12", "v13", "v14", "v15", "v16", "v17", "v18", "v19", "v20", "v21", "v22", "v23", "v24", "v25", "v26", "v27", "v28", "v29", "v30", "v31", "v32", "v33", "v34", "v35", "v36", "v37", "v38", "v39", "v40", "v41", "v42", "v43", "v44", "v45", "v46", "v47", "v48", "v49", "v50", "v51", "v52", "v53", "v54", "v55", "v56", "v57", "v58", "v59", "v60", "v61", "v62", "v63", "v64", "v65", "v66", "v67", "v68", "v69", "v70", "v71", "v72", "v73", "v74", "v75", "v76", "v77", "v78", "v79", "v80", "v81", "v82", "v83", "v84", "v85", "v86", "v87", "v88", "v89", "v90", "v91", "v92", "v93", "v94", "v95", "v96", "v97", "v98", "v99", "v100", "v101", "v102", "v103", "v104", "v105", "v106", "v107", "v108", "v109", "v110", "v111", "v112", "v113", "v114", "v115", "v116", "v117", "v118", "v119", "v120", "v121", "v122", "v123", "v124", "v125", "v126", "v127", "v128", "v129", "v130", "v131", "v132", "v133", "v134", "v135", "v136", "v137", "v138", "v139", "v140", "v141", "v142", "v143", "v144", "v145", "v146", "v147", "v148", "v149", "v150", "v151", "v152", "v153", "v154", "v155", "v156", "v157", "v158", "v159", "v160", "v161", "v162", "v163", "v164", "v165", "v166", "v167", "v168", "v169", "v170", "v171", "v172", "v173", "v174", "v175", "v176", "v177", "v178", "v179", "v180", "v181", "v182", "v183", "v184", "v185", "v186", "v187", "v188", "v189", "v190", "v191", "v192", "v193", "v194", "v195", "v196", "v197", "v198", "v199", "v200", "v201", "v202", "v203", "v204", "v205", "v206", "v207", "v208", "v209", "v210", "v211", "v212", "v213", "v214", "v215", "v216", "v217", "v218", "v219", "v220", "v221", "v222", "v223", "v224", "v225", "v226", "v227", "v228", "v229", "v230", "v231", "v232", "v233", "v234", "v235", "v236", "v237", "v238", "v239", "v240", "v241", "v242", "v243", "v244", "v245", "v246", "v247", "v248", "v249", "v250", "v251", "v252", "v253", "v254", "v255", "a0", "a1", "a2", "a3", "a4", "a5", "a6", "a7", "a8", "a9", "a10", "a11", "a12", "a13", "a14", "a15", "a16", "a17", "a18", "a19", "a20", "a21", "a22", "a23", "a24", "a25", "a26", "a27", "a28", "a29", "a30", "a31", "a32", "a33", "a34", "a35", "a36", "a37", "a38", "a39", "a40", "a41", "a42", "a43", "a44", "a45", "a46", "a47", "a48", "a49", "a50", "a51", "a52", "a53", "a54", "a55", "a56", "a57", "a58", "a59", "a60", "a61", "a62", "a63", "a64", "a65", "a66", "a67", "a68", "a69", "a70", "a71", "a72", "a73", "a74", "a75", "a76", "a77", "a78", "a79", "a80", "a81", "a82", "a83", "a84", "a85", "a86", "a87", "a88", "a89", "a90", "a91", "a92", "a93", "a94", "a95", "a96", "a97", "a98", "a99", "a100", "a101", "a102", "a103", "a104", "a105", "a106", "a107", "a108", "a109", "a110", "a111", "a112", "a113", "a114", "a115", "a116", "a117", "a118", "a119", "a120", "a121", "a122", "a123", "a124", "a125", "a126", "a127", "a128", "a129", "a130", "a131", "a132", "a133", "a134", "a135", "a136", "a137", "a138", "a139", "a140", "a141", "a142", "a143", "a144", "a145", "a146", "a147", "a148", "a149", "a150", "a151", "a152", "a153", "a154", "a155", "a156", "a157", "a158", "a159", "a160", "a161", "a162", "a163", "a164", "a165", "a166", "a167", "a168", "a169", "a170", "a171", "a172", "a173", "a174", "a175", "a176", "a177", "a178", "a179", "a180", "a181", "a182", "a183", "a184", "a185", "a186", "a187", "a188", "a189", "a190", "a191", "a192", "a193", "a194", "a195", "a196", "a197", "a198", "a199", "a200", "a201", "a202", "a203", "a204", "a205", "a206", "a207", "a208", "a209", "a210", "a211", "a212", "a213", "a214", "a215", "a216", "a217", "a218", "a219", "a220", "a221", "a222", "a223", "a224", "a225", "a226", "a227", "a228", "a229", "a230", "a231", "a232", "a233", "a234", "a235", "a236", "a237", "a238", "a239", "a240", "a241", "a242", "a243", "a244", "a245", "a246", "a247", "a248", "a249", "a250", "a251", "a252", "a253", "a254", "a255", "s3", "s4", "s5", "s6", "s7", "s8", "s9", "s10", "s11", "s12", "s13", "s14", "s15", "s16", "s17", "s18", "s19", "s20", "s21", "s22", "s23", "s24", "s25", "s26", "s27", "s28", "s29", "s30", "s31", "s34", "s35", "s36", "s37", "s38", "s39", "s40", "s41", "s42", "s43", "s44", "s45", "s46", "s47", "s48", "s49", "s50", "s51", "s52", "s53", "s54", "s55", "s56", "s57", "s58", "s59", "s60", "s61", "s62", "s63", "s64", "s65", "s66", "s67", "s68", "s69", "s70", "s71", "s72", "s73", "s74", "s75", "s76", "s77", "s78", "s79", "s80", "s81", "s82", "s83", "s84", "s85", "s86", "s87", "s88", "s89", "s90", "s91", "s92", "s93", "s94", "s95", "s96", "s97", "s98", "s99", "vcc", "scc", "memory");
}

extern "C" void kernel_launch(void* const* d_in, const int* in_sizes, int n_in, void* d_out, int out_size, void* d_ws, size_t ws_size,
                              hipStream_t stream) {
  gated_mix_fused<<<256, 256, 0, stream>>>(d_in[0], d_in[1], d_in[5], d_in[6], d_in[3], (char*)d_ws + ((size_t)0 << 20), d_out, d_in[2], d_in[7]);
}
```
